```python
import torch

def _softmax(x, axis=-1):
    return torch.softmax(x, dim=axis)

def _gelu(x):
    return torch.nn.functional.gelu(x, approximate='tanh')

def _silu(x):
    return torch.nn.functional.silu(x)

def _sigmoid(x):
    return torch.sigmoid(x)

def _layernorm(x, axis=-1, eps=1e-05):
    m = x.mean(dim=axis, keepdim=True)
    v = x.var(dim=axis, keepdim=True, unbiased=False)
    return (x - m) / torch.sqrt(v + eps)

def _scatter_add(tgt, idx, src, axis=0):
    return tgt.clone().index_add_(axis, idx, src)

def _segment_sum(src, seg, n):
    out = torch.zeros((n,) + src.shape[1:], dtype=src.dtype)
    return out.index_add_(0, seg, src)

def _pad(x, pw):
    flat = tuple((v for pair in reversed(pw) for v in pair))
    return torch.nn.functional.pad(x, flat)
kernel_name = 'adaLN_cond__sliding_block_cached__dense_activation_squared_relu__prompt'

def setup_inputs():

    _w = lambda a: (lambda t: t.to(torch.bfloat16) if t.is_floating_point() else t)(torch.as_tensor(a))

    def _A_setup():
        B, Sq, D = (1, 2048, 640)
        Hq, Hkv, Sk, Dh = (32, 32, 2048, 128)
        assert Hq % Hkv == 0, (Hq, Hkv)
        return {'x': _w((lambda *s: torch.randn(*s, dtype=torch.bfloat16) * 0.05)(B, Sq, D)), 'k_cache': _w((lambda *s: torch.randn(*s, dtype=torch.bfloat16) * 3.2)(B, Hkv, Sk, Dh)), 'v_cache': _w((lambda *s: torch.randn(*s, dtype=torch.bfloat16) * 0.4)(B, Hkv, Sk, Dh)), 'wq': _w((lambda *s: torch.randn(*s, dtype=torch.bfloat16) * 3.2)(D, Hq * Dh) * D ** (-0.5)), 'wo': _w((lambda *s: torch.randn(*s, dtype=torch.bfloat16) * 0.4)(Hq * Dh, D) * (Hq * Dh) ** (-0.5))}
    _A_in = _A_setup()

    def _B_setup():
        B, S, D, F = (1, 2048, 640, 1728)
        return {'x': _w((lambda *s: torch.randn(*s, dtype=torch.bfloat16) * 0.05)(B, S, D)), 'w1': _w((lambda *s: torch.randn(*s, dtype=torch.bfloat16) * 0.05)(D, F)), 'b1': _w((lambda *s: torch.randn(*s, dtype=torch.bfloat16) * 0.8)(F)), 'w2': _w((lambda *s: torch.randn(*s, dtype=torch.bfloat16) * 0.05)(F, D)), 'b2': _w((lambda *s: torch.randn(*s, dtype=torch.bfloat16) * 6.4)(D))}
    _B_in = _B_setup()
    return {'A_x': _A_in['x'], 'A_k_cache': _A_in['k_cache'], 'A_v_cache': _A_in['v_cache'], 'A_wq': _A_in['wq'], 'A_wo': _A_in['wo'], 'B_x': _B_in['x'], 'B_w1': _B_in['w1'], 'B_b1': _B_in['b1'], 'B_w2': _B_in['w2'], 'B_b2': _B_in['b2'], 'c': _w((lambda *s: torch.randn(*s, dtype=torch.bfloat16) * 0.1)(1, 640) * 8.0), 'w_mod': _w((lambda *s: torch.randn(*s, dtype=torch.bfloat16) * 0.1)(640, 6 * 640) * 640 ** (-0.5) * 8.0)}

def reference(A_x, A_k_cache, A_v_cache, A_wq, A_wo, B_x, B_w1, B_b1, B_w2, B_b2, c, w_mod):

    def _A_ref(x, k_cache, v_cache, wq, wo):
        B, Sq, D = x.shape
        Hq, Dh = (32, 128)
        Hkv, Sk = (k_cache.shape[1], k_cache.shape[2])
        W = min(4096, max(256, Sk // 4))
        rep = Hq // Hkv
        q = (lambda x, *d: x.permute(*d))((x @ wq).reshape(B, Sq, Hq, Dh), 0, 2, 1, 3)
        k = (lambda x, n, axis: torch.repeat_interleave(x, n, dim=axis))(k_cache[:, :, Sk - W:, :], rep, axis=1)
        v = (lambda x, n, axis: torch.repeat_interleave(x, n, dim=axis))(v_cache[:, :, Sk - W:, :], rep, axis=1)
        scores = torch.einsum('bhqd,bhkd->bhqk', q, k) * Dh ** (-0.5)
        p = _softmax(scores, axis=-1)
        o = torch.einsum('bhqk,bhkd->bhqd', p, v)
        return (lambda x, *d: x.permute(*d))(o, 0, 2, 1, 3).reshape(B, Sq, Hq * Dh) @ wo

    def _B_ref(x, w1, b1, w2, b2):
        h = torch.einsum('bsd,df->bsf', x, w1) + b1
        zero = torch.zeros_like(h)
        r = torch.where(h > zero, h, zero)
        h = r * r
        out = torch.einsum('bsf,fd->bsd', h, w2) + b2
        return out
    x = A_x
    mod = (_silu(c) @ w_mod).reshape(c.shape[0], 6, 1, -1)
    sa, ca, ga, sb, cb, gb = (mod[:, 0].clone(), mod[:, 1].clone(), mod[:, 2].clone(), mod[:, 3].clone(), mod[:, 4].clone(), mod[:, 5].clone())
    h = _layernorm(x, axis=-1) * (1 + ca) + sa
    x = x + ga * _A_ref(h, A_k_cache, A_v_cache, A_wq, A_wo)
    h = _layernorm(x, axis=-1) * (1 + cb) + sb
    x = x + gb * _B_ref(h, B_w1, B_b1, B_w2, B_b2)
    return x
```

```cpp
#include <hip/hip_runtime.h>
#include <hip/hip_bf16.h>
#include <stdint.h>

typedef unsigned short u16;
__device__ __forceinline__ float bf2f(u16 x) { return __uint_as_float(((unsigned)x) << 16); }
__device__ __forceinline__ u16 f2bf(float f) { unsigned u = __float_as_uint(f); u += 0x7fffu + ((u >> 16) & 1u); return (u16)(u >> 16); }

constexpr int S = 2048, D = 640, H = 32, DH = 128, SK = 2048, W = 512, F = 1728;

__global__ void mod_kernel(const u16* c, const u16* wmod, float* mod) {
  int n = blockIdx.x * blockDim.x + threadIdx.x;
  if (n >= 6 * D) return;
  float acc = 0;
  for (int k = 0; k < D; ++k) { float cv = bf2f(c[k]); float s = cv / (1.f + __expf(-cv)); s = bf2f(f2bf(s)); acc += s * bf2f(wmod[k * 6 * D + n]); }
  mod[n] = acc;
}
__global__ void ln_kernel(const u16* x, const float* sh, const float* sc, u16* h) {
  int row = blockIdx.x; int t = threadIdx.x;
  float v[10]; float s = 0;
  for (int i = 0; i < 10; ++i) { v[i] = bf2f(x[row * D + t + 64 * i]); s += v[i]; }
  for (int o = 32; o; o >>= 1) s += __shfl_xor(s, o);
  float m = s / D; float q = 0;
  for (int i = 0; i < 10; ++i) { v[i] -= m; q += v[i] * v[i]; }
  for (int o = 32; o; o >>= 1) q += __shfl_xor(q, o);
  float r = rsqrtf(q / D + 1e-5f);
  for (int i = 0; i < 10; ++i) { int cidx = t + 64 * i; h[row * D + cidx] = f2bf(v[i] * r * (1.f + sc[cidx]) + sh[cidx]); }
}
__global__ void gemm_kernel(const u16* A, const u16* B, u16* C, int M, int N, int K, int mode, const u16* resid, const float* g, const u16* bias) {
  __shared__ float As[16][65], Bs[16][65];
  int bm = blockIdx.y * 64, bn = blockIdx.x * 64; int tx = threadIdx.x & 15, ty = threadIdx.x >> 4;
  float acc[4][4] = {};
  for (int k0 = 0; k0 < K; k0 += 16) {
    for (int i = threadIdx.x; i < 1024; i += 256) { int r = i >> 4, c = i & 15; As[c][r] = bf2f(A[(size_t)(bm + r) * K + k0 + c]); }
    for (int i = threadIdx.x; i < 1024; i += 256) { int r = i >> 6, c = i & 63; Bs[r][c] = bf2f(B[(size_t)(k0 + r) * N + bn + c]); }
    __syncthreads();
    for (int k = 0; k < 16; ++k) for (int i = 0; i < 4; ++i) for (int j = 0; j < 4; ++j) acc[i][j] += As[k][ty * 4 + i] * Bs[k][tx * 4 + j];
    __syncthreads();
  }
  for (int i = 0; i < 4; ++i) for (int j = 0; j < 4; ++j) {
    int r = bm + ty * 4 + i, c = bn + tx * 4 + j; float v = acc[i][j];
    if (mode == 1) v = bf2f(resid[(size_t)r * N + c]) + g[c] * v;
    else if (mode == 2) { v += bf2f(bias[c]); v = v > 0 ? v * v : 0.f; }
    else if (mode == 3) v = bf2f(resid[(size_t)r * N + c]) + g[c] * (v + bf2f(bias[c]));
    C[(size_t)r * N + c] = f2bf(v);
  }
}
__global__ void attn_kernel(const u16* q, const u16* kc, const u16* vc, u16* o) {
  int row = blockIdx.x, h = blockIdx.y, t = threadIdx.x;
  __shared__ float qs[DH], p[W], red[256];
  if (t < DH) qs[t] = bf2f(q[(size_t)row * H * DH + h * DH + t]);
  __syncthreads();
  float mx = -1e30f;
  for (int j = t; j < W; j += 256) { const u16* kr = kc + ((size_t)h * SK + SK - W + j) * DH; float a = 0; for (int d = 0; d < DH; ++d) a += qs[d] * bf2f(kr[d]); a *= 0.08838834764831845f; p[j] = a; mx = fmaxf(mx, a); }
  red[t] = mx; __syncthreads();
  for (int o2 = 128; o2; o2 >>= 1) { if (t < o2) red[t] = fmaxf(red[t], red[t + o2]); __syncthreads(); }
  mx = red[0]; __syncthreads();
  float s = 0; for (int j = t; j < W; j += 256) { float e = __expf(p[j] - mx); p[j] = e; s += e; }
  red[t] = s; __syncthreads();
  for (int o2 = 128; o2; o2 >>= 1) { if (t < o2) red[t] += red[t + o2]; __syncthreads(); }
  s = red[0];
  if (t < DH) { float a = 0; for (int j = 0; j < W; ++j) a += bf2f(f2bf(p[j])) * bf2f(vc[((size_t)h * SK + SK - W + j) * DH + t]); o[(size_t)row * H * DH + h * DH + t] = f2bf(a / s); }
}

extern "C" void kernel_launch(void* const* d_in, const int* in_sizes, int n_in, void* d_out, int out_size, void* d_ws, size_t ws_size, hipStream_t stream) {
  const u16* x = (const u16*)d_in[0]; const u16* kc = (const u16*)d_in[1]; const u16* vc = (const u16*)d_in[2];
  const u16* wq = (const u16*)d_in[3]; const u16* wo = (const u16*)d_in[4];
  const u16* w1 = (const u16*)d_in[6]; const u16* b1 = (const u16*)d_in[7]; const u16* w2 = (const u16*)d_in[8]; const u16* b2 = (const u16*)d_in[9];
  const u16* c = (const u16*)d_in[10]; const u16* wmod = (const u16*)d_in[11];
  char* ws = (char*)d_ws;
  float* mod = (float*)ws; ws += 65536;
  u16* h = (u16*)ws; ws += S * D * 2;
  u16* q = (u16*)ws; ws += (size_t)S * H * DH * 2;
  u16* o = (u16*)ws; ws += (size_t)S * H * DH * 2;
  u16* x1 = (u16*)ws; ws += S * D * 2;
  u16* h2 = (u16*)ws; ws += S * D * 2;
  u16* a1 = (u16*)ws; ws += (size_t)S * F * 2;
  mod_kernel<<<(6 * D + 63) / 64, 64, 0, stream>>>(c, wmod, mod);
  ln_kernel<<<S, 64, 0, stream>>>(x, mod, mod + D, h);
  gemm_kernel<<<dim3(H * DH / 64, S / 64), 256, 0, stream>>>(h, wq, q, S, H * DH, D, 0, nullptr, nullptr, nullptr);
  attn_kernel<<<dim3(S, H), 256, 0, stream>>>(q, kc, vc, o);
  gemm_kernel<<<dim3(D / 64, S / 64), 256, 0, stream>>>(o, wo, x1, S, D, H * DH, 1, x, mod + 2 * D, nullptr);
  ln_kernel<<<S, 64, 0, stream>>>(x1, mod + 3 * D, mod + 4 * D, h2);
  gemm_kernel<<<dim3(F / 64, S / 64), 256, 0, stream>>>(h2, w1, a1, S, F, D, 2, nullptr, nullptr, b1);
  gemm_kernel<<<dim3(D / 64, S / 64), 256, 0, stream>>>(a1, w2, (u16*)d_out, S, D, F, 3, x1, mod + 5 * D, b2);
}
```

```cpp
#include <hip/hip_runtime.h>
#include <hip/hip_bf16.h>
#include <stdint.h>

typedef unsigned short u16;
typedef unsigned int u32;
typedef short s16x4 __attribute__((ext_vector_type(4)));
typedef __bf16 bf16x8 __attribute__((ext_vector_type(8)));
typedef __bf16 bf16x2 __attribute__((ext_vector_type(2)));
typedef float f32x4 __attribute__((ext_vector_type(4)));
typedef float f32x2 __attribute__((ext_vector_type(2)));
typedef u32 u32x4 __attribute__((ext_vector_type(4)));
typedef u32 u32x2 __attribute__((ext_vector_type(2)));
#define DI __device__ __forceinline__

DI float bf2f(u16 x) { return __uint_as_float(((u32)x) << 16); }
DI float bflo(u32 w) { return __uint_as_float(w << 16); }
DI float bfhi(u32 w) { return __uint_as_float(w & 0xffff0000u); }
DI u32 cvtpk(float lo, float hi) { f32x2 v = {lo, hi}; bf16x2 b = __builtin_convertvector(v, bf16x2); return __builtin_bit_cast(u32, b); }

constexpr int S = 2048, D = 640, H = 32, DH = 128, SK = 2048, W = 512, F = 1728;
constexpr float QSCALE = 0.12751743082f;

constexpr size_t WS_MOD = 0;
constexpr size_t WS_SYNC = 16384;
constexpr size_t WS_G = 49152;
constexpr size_t WS_HF = 61440;
constexpr size_t WS_OF = WS_HF + (size_t)S * D * 2;
constexpr size_t WS_WOF = WS_OF + (size_t)S * 4096 * 2;
constexpr size_t WS_W1F = WS_WOF + (size_t)4096 * D * 2;
constexpr size_t WS_W2F = WS_W1F + (size_t)16 * 140 * 1024 + 4096;
constexpr size_t WS_X1 = WS_W2F + (size_t)8 * 56 * 5 * 1024;
constexpr size_t WS_H2F = WS_X1 + (size_t)S * D * 2;
constexpr size_t WS_A1F = WS_H2F + (size_t)S * D * 2;
constexpr size_t WS_P = WS_A1F + (size_t)128 * 56 * 1024;

typedef __amdgpu_buffer_rsrc_t rsrc_t;
DI rsrc_t mk_rsrc(const void* p, unsigned bytes) {
  const unsigned long long a = (unsigned long long)p;
  const unsigned lo = __builtin_amdgcn_readfirstlane((unsigned)a), hi = __builtin_amdgcn_readfirstlane((unsigned)(a >> 32));
  const void* q = (const void*)(((unsigned long long)hi << 32) | lo);
  return __builtin_amdgcn_make_buffer_rsrc((void*)q, (short)0, (int)__builtin_amdgcn_readfirstlane(bytes), 0x00020000);
}

template <int CTRL> DI float dpp_add(float x) {
  return x + __builtin_bit_cast(float, __builtin_amdgcn_update_dpp(0, __builtin_bit_cast(int, x), CTRL, 0xf, 0xf, true));
}
DI float sum16(float x) { x = dpp_add<0x121>(x); x = dpp_add<0x122>(x); x = dpp_add<0x124>(x); return dpp_add<0x128>(x); }
DI float swap16_add(float x) { auto r = __builtin_amdgcn_permlane16_swap(__float_as_uint(x), __float_as_uint(x), false, false); return __uint_as_float(r[0]) + __uint_as_float(r[1]); }
DI float swap32_add(float x) { auto r = __builtin_amdgcn_permlane32_swap(__float_as_uint(x), __float_as_uint(x), false, false); return __uint_as_float(r[0]) + __uint_as_float(r[1]); }
DI void ln_row(float (&v)[5][8], float& rstd) {
  float s = 0;
#pragma unroll
  for (int j = 0; j < 5; ++j)
#pragma unroll
    for (int e = 0; e < 8; ++e) s += v[j][e];
  s = sum16(s);
  const float m = s * (1.f / D); float q = 0;
#pragma unroll
  for (int j = 0; j < 5; ++j)
#pragma unroll
    for (int e = 0; e < 8; ++e) { v[j][e] -= m; q += v[j][e] * v[j][e]; }
  q = sum16(q);
  rstd = rsqrtf(q * (1.f / D) + 1e-5f);
}

typedef __attribute__((address_space(3))) s16x4* ldsp_t;
#define K1_ASM \
  "s_nop 4\n" \
  "v_mbcnt_lo_u32_b32 v0, -1, 0\n" \
  "v_mbcnt_hi_u32_b32 v0, -1, v0\n" \
  "v_lshlrev_b32 v232, 4, v0\n" \
  "s_mov_b32 s42, 0\n" \
  "buffer_load_dwordx4 a[128:131], v232, %[srdH], s42 offen offset:0 sc1\n" \
  "buffer_load_dwordx4 a[132:135], v232, %[srdH], s42 offen offset:1024 sc1\n" \
  "s_mov_b32 s42, 0\n" \
  "buffer_load_dwordx4 a[136:139], v232, %[srdH], s42 offen offset:2048 sc1\n" \
  "buffer_load_dwordx4 a[140:143], v232, %[srdH], s42 offen offset:3072 sc1\n" \
  "s_mov_b32 s42, 4096\n" \
  "buffer_load_dwordx4 a[144:147], v232, %[srdH], s42 offen offset:0 sc1\n" \
  "buffer_load_dwordx4 a[148:151], v232, %[srdH], s42 offen offset:1024 sc1\n" \
  "s_mov_b32 s42, 4096\n" \
  "buffer_load_dwordx4 a[152:155], v232, %[srdH], s42 offen offset:2048 sc1\n" \
  "buffer_load_dwordx4 a[156:159], v232, %[srdH], s42 offen offset:3072 sc1\n" \
  "s_lshl_b32 s60, %[w], 12\n" \
  "s_mul_i32 s61, %[w], 10240\n" \
  "s_lshl_b32 s40, %[w], 17\n" \
  "v_lshrrev_b32 v1, 4, v0\n" \
  "v_and_b32 v2, 15, v0\n" \
  "v_xor_b32 v3, v2, v1\n" \
  "v_lshlrev_b32 v4, 8, v1\n" \
  "v_lshl_add_u32 v233, v3, 4, v4\n" \
  "v_add_u32 v233, s60, v233\n" \
  "v_xor_b32 v234, 64, v233\n" \
  "v_xor_b32 v235, 128, v233\n" \
  "v_xor_b32 v236, 192, v233\n" \
  "v_lshrrev_b32 v5, 5, v0\n" \
  "v_and_b32 v6, 3, v0\n" \
  "v_bfe_u32 v7, v0, 2, 3\n" \
  "v_bfe_u32 v8, v0, 2, 2\n" \
  "v_bfe_u32 v9, v0, 4, 1\n" \
  "v_lshlrev_b32 v10, 6, v5\n" \
  "v_lshl_add_u32 v10, v6, 4, v10\n" \
  "v_lshl_add_u32 v237, v7, 8, v10\n" \
  "v_add_u32 v237, s60, v237\n" \
  "v_add_u32 v237, 1024, v237\n" \
  "v_add_u32 v238, -896, v237\n" \
  "v_lshl_add_u32 v239, v7, 13, v10\n" \
  "v_add_u32 v239, s40, v239\n" \
  "v_add_u32 v239, 1024, v239\n" \
  "v_add_u32 v240, -896, v239\n" \
  "v_and_b32 v11, 1, v6\n" \
  "v_lshlrev_b32 v11, 1, v11\n" \
  "v_lshrrev_b32 v13, 1, v6\n" \
  "v_or_b32 v11, v11, v13\n" \
  "v_lshlrev_b32 v12, 6, v8\n" \
  "v_lshl_add_u32 v12, v9, 5, v12\n" \
  "v_lshl_add_u32 v241, v5, 11, v12\n" \
  "v_lshl_add_u32 v241, v11, 3, v241\n" \
  "v_add_u32 v241, 65536, v241\n" \
  "v_add_u32 v242, 65536, v241\n" \
  "v_lshl_add_u32 v243, v5, 8, v12\n" \
  "v_lshl_add_u32 v243, v6, 3, v243\n" \
  "v_add_u32 v243, 65536, v243\n" \
  "v_add_u32 v244, 65536, v243\n" \
  "v_and_b32 v14, 31, v0\n" \
  "v_and_b32 v15, 15, v0\n" \
  "v_xor_b32 v15, v15, v5\n" \
  "v_lshlrev_b32 v14, 8, v14\n" \
  "v_lshl_add_u32 v245, v15, 4, v14\n" \
  "v_xor_b32 v246, 32, v245\n" \
  "v_xor_b32 v247, 64, v245\n" \
  "v_xor_b32 v248, 96, v245\n" \
  "v_xor_b32 v249, 128, v245\n" \
  "v_xor_b32 v250, 160, v245\n" \
  "v_xor_b32 v251, 192, v245\n" \
  "v_xor_b32 v252, 224, v245\n" \
  "v_add_u32 v253, s61, v232\n" \
  "ds_read_b64_tr_b16 a[64:65], v241 offset:0\n" \
  "ds_read_b64_tr_b16 a[66:67], v241 offset:256\n" \
  "ds_read_b64_tr_b16 a[68:69], v241 offset:512\n" \
  "ds_read_b64_tr_b16 a[70:71], v241 offset:768\n" \
  "ds_read_b64_tr_b16 a[72:73], v241 offset:1024\n" \
  "ds_read_b64_tr_b16 a[74:75], v241 offset:1280\n" \
  "ds_read_b64_tr_b16 a[76:77], v241 offset:1536\n" \
  "ds_read_b64_tr_b16 a[78:79], v241 offset:1792\n" \
  "ds_read_b64_tr_b16 a[80:81], v241 offset:4096\n" \
  "ds_read_b64_tr_b16 a[82:83], v241 offset:4352\n" \
  "ds_read_b64_tr_b16 a[84:85], v241 offset:4608\n" \
  "ds_read_b64_tr_b16 a[86:87], v241 offset:4864\n" \
  "ds_read_b64_tr_b16 a[88:89], v241 offset:5120\n" \
  "ds_read_b64_tr_b16 a[90:91], v241 offset:5376\n" \
  "ds_read_b64_tr_b16 a[92:93], v241 offset:5632\n" \
  "ds_read_b64_tr_b16 a[94:95], v241 offset:5888\n" \
  "s_waitcnt vmcnt(7) lgkmcnt(14)\n" \
  "v_mfma_f32_32x32x16_bf16 v[0:15], a[64:67], a[128:131], 0\n" \
  "ds_read_b64_tr_b16 a[96:97], v241 offset:8192\n" \
  "s_waitcnt vmcnt(6)\n" \
  "v_mfma_f32_32x32x16_bf16 v[16:31], a[64:67], a[132:135], 0\n" \
  "ds_read_b64_tr_b16 a[98:99], v241 offset:8448\n" \
  "s_mov_b32 s42, 8192\n" \
  "s_waitcnt lgkmcnt(14)\n" \
  "v_mfma_f32_32x32x16_bf16 v[32:47], a[68:71], a[128:131], 0\n" \
  "ds_read_b64_tr_b16 a[100:101], v241 offset:8704\n" \
  "buffer_load_dwordx4 a[160:163], v232, %[srdH], s42 offen offset:0 sc1\n" \
  "v_mfma_f32_32x32x16_bf16 v[48:63], a[68:71], a[132:135], 0\n" \
  "ds_read_b64_tr_b16 a[102:103], v241 offset:8960\n" \
  "buffer_load_dwordx4 a[164:167], v232, %[srdH], s42 offen offset:1024 sc1\n" \
  "s_waitcnt lgkmcnt(14)\n" \
  "v_mfma_f32_32x32x16_bf16 v[64:79], a[72:75], a[128:131], 0\n" \
  "ds_read_b64_tr_b16 a[104:105], v241 offset:9216\n" \
  "v_mfma_f32_32x32x16_bf16 v[80:95], a[72:75], a[132:135], 0\n" \
  "ds_read_b64_tr_b16 a[106:107], v241 offset:9472\n" \
  "s_mov_b32 s42, 8192\n" \
  "s_waitcnt lgkmcnt(5)\n" \
  "v_mfma_f32_32x32x16_bf16 v[96:111], a[76:79], a[128:131], 0\n" \
  "ds_read_b64_tr_b16 a[108:109], v241 offset:9728\n" \
  "buffer_load_dwordx4 a[168:171], v232, %[srdH], s42 offen offset:2048 sc1\n" \
  "v_mfma_f32_32x32x16_bf16 v[112:127], a[76:79], a[132:135], 0\n" \
  "ds_read_b64_tr_b16 a[110:111], v241 offset:9984\n" \
  "buffer_load_dwordx4 a[172:175], v232, %[srdH], s42 offen offset:3072 sc1\n" \
  "s_waitcnt vmcnt(9)\n" \
  "v_mfma_f32_32x32x16_bf16 v[0:15], a[80:83], a[136:139], v[0:15]\n" \
  "ds_read_b64_tr_b16 a[64:65], v241 offset:12288\n" \
  "s_waitcnt vmcnt(8)\n" \
  "v_mfma_f32_32x32x16_bf16 v[16:31], a[80:83], a[140:143], v[16:31]\n" \
  "ds_read_b64_tr_b16 a[66:67], v241 offset:12544\n" \
  "s_mov_b32 s42, 12288\n" \
  "v_mfma_f32_32x32x16_bf16 v[32:47], a[84:87], a[136:139], v[32:47]\n" \
  "ds_read_b64_tr_b16 a[68:69], v241 offset:12800\n" \
  "buffer_load_dwordx4 a[176:179], v232, %[srdH], s42 offen offset:0 sc1\n" \
  "v_mfma_f32_32x32x16_bf16 v[48:63], a[84:87], a[140:143], v[48:63]\n" \
  "ds_read_b64_tr_b16 a[70:71], v241 offset:13056\n" \
  "buffer_load_dwordx4 a[180:183], v232, %[srdH], s42 offen offset:1024 sc1\n" \
  "v_mfma_f32_32x32x16_bf16 v[64:79], a[88:91], a[136:139], v[64:79]\n" \
  "ds_read_b64_tr_b16 a[72:73], v241 offset:13312\n" \
  "v_mfma_f32_32x32x16_bf16 v[80:95], a[88:91], a[140:143], v[80:95]\n" \
  "ds_read_b64_tr_b16 a[74:75], v241 offset:13568\n" \
  "s_mov_b32 s42, 12288\n" \
  "v_mfma_f32_32x32x16_bf16 v[96:111], a[92:95], a[136:139], v[96:111]\n" \
  "ds_read_b64_tr_b16 a[76:77], v241 offset:13824\n" \
  "buffer_load_dwordx4 a[184:187], v232, %[srdH], s42 offen offset:2048 sc1\n" \
  "v_mfma_f32_32x32x16_bf16 v[112:127], a[92:95], a[140:143], v[112:127]\n" \
  "ds_read_b64_tr_b16 a[78:79], v241 offset:14080\n" \
  "buffer_load_dwordx4 a[188:191], v232, %[srdH], s42 offen offset:3072 sc1\n" \
  "s_waitcnt vmcnt(11) lgkmcnt(5)\n" \
  "v_mfma_f32_32x32x16_bf16 v[0:15], a[96:99], a[144:147], v[0:15]\n" \
  "ds_read_b64_tr_b16 a[80:81], v241 offset:16384\n" \
  "s_waitcnt vmcnt(10)\n" \
  "v_mfma_f32_32x32x16_bf16 v[16:31], a[96:99], a[148:151], v[16:31]\n" \
  "ds_read_b64_tr_b16 a[82:83], v241 offset:16640\n" \
  "s_mov_b32 s42, 16384\n" \
  "v_mfma_f32_32x32x16_bf16 v[32:47], a[100:103], a[144:147], v[32:47]\n" \
  "ds_read_b64_tr_b16 a[84:85], v241 offset:16896\n" \
  "buffer_load_dwordx4 a[192:195], v232, %[srdH], s42 offen offset:0 sc1\n" \
  "v_mfma_f32_32x32x16_bf16 v[48:63], a[100:103], a[148:151], v[48:63]\n" \
  "ds_read_b64_tr_b16 a[86:87], v241 offset:17152\n" \
  "buffer_load_dwordx4 a[196:199], v232, %[srdH], s42 offen offset:1024 sc1\n" \
  "v_mfma_f32_32x32x16_bf16 v[64:79], a[104:107], a[144:147], v[64:79]\n" \
  "ds_read_b64_tr_b16 a[88:89], v241 offset:17408\n" \
  "v_mfma_f32_32x32x16_bf16 v[80:95], a[104:107], a[148:151], v[80:95]\n" \
  "ds_read_b64_tr_b16 a[90:91], v241 offset:17664\n" \
  "s_mov_b32 s42, 16384\n" \
  "v_mfma_f32_32x32x16_bf16 v[96:111], a[108:111], a[144:147], v[96:111]\n" \
  "ds_read_b64_tr_b16 a[92:93], v241 offset:17920\n" \
  "buffer_load_dwordx4 a[200:203], v232, %[srdH], s42 offen offset:2048 sc1\n" \
  "v_mfma_f32_32x32x16_bf16 v[112:127], a[108:111], a[148:151], v[112:127]\n" \
  "ds_read_b64_tr_b16 a[94:95], v241 offset:18176\n" \
  "buffer_load_dwordx4 a[204:207], v232, %[srdH], s42 offen offset:3072 sc1\n" \
  "s_waitcnt vmcnt(13)\n" \
  "v_mfma_f32_32x32x16_bf16 v[0:15], a[64:67], a[152:155], v[0:15]\n" \
  "ds_read_b64_tr_b16 a[96:97], v241 offset:20480\n" \
  "s_waitcnt vmcnt(12)\n" \
  "v_mfma_f32_32x32x16_bf16 v[16:31], a[64:67], a[156:159], v[16:31]\n" \
  "ds_read_b64_tr_b16 a[98:99], v241 offset:20736\n" \
  "s_mov_b32 s42, 20480\n" \
  "s_waitcnt lgkmcnt(5)\n" \
  "v_mfma_f32_32x32x16_bf16 v[32:47], a[68:71], a[152:155], v[32:47]\n" \
  "ds_read_b64_tr_b16 a[100:101], v241 offset:20992\n" \
  "buffer_load_dwordx4 a[208:211], v232, %[srdH], s42 offen offset:0 sc1\n" \
  "v_mfma_f32_32x32x16_bf16 v[48:63], a[68:71], a[156:159], v[48:63]\n" \
  "ds_read_b64_tr_b16 a[102:103], v241 offset:21248\n" \
  "buffer_load_dwordx4 a[212:215], v232, %[srdH], s42 offen offset:1024 sc1\n" \
  "v_mfma_f32_32x32x16_bf16 v[64:79], a[72:75], a[152:155], v[64:79]\n" \
  "ds_read_b64_tr_b16 a[104:105], v241 offset:21504\n" \
  "v_mfma_f32_32x32x16_bf16 v[80:95], a[72:75], a[156:159], v[80:95]\n" \
  "ds_read_b64_tr_b16 a[106:107], v241 offset:21760\n" \
  "s_mov_b32 s42, 20480\n" \
  "v_mfma_f32_32x32x16_bf16 v[96:111], a[76:79], a[152:155], v[96:111]\n" \
  "ds_read_b64_tr_b16 a[108:109], v241 offset:22016\n" \
  "buffer_load_dwordx4 a[216:219], v232, %[srdH], s42 offen offset:2048 sc1\n" \
  "v_mfma_f32_32x32x16_bf16 v[112:127], a[76:79], a[156:159], v[112:127]\n" \
  "ds_read_b64_tr_b16 a[110:111], v241 offset:22272\n" \
  "buffer_load_dwordx4 a[220:223], v232, %[srdH], s42 offen offset:3072 sc1\n" \
  "s_waitcnt vmcnt(15)\n" \
  "v_mfma_f32_32x32x16_bf16 v[0:15], a[80:83], a[160:163], v[0:15]\n" \
  "ds_read_b64_tr_b16 a[64:65], v241 offset:24576\n" \
  "s_waitcnt vmcnt(14)\n" \
  "v_mfma_f32_32x32x16_bf16 v[16:31], a[80:83], a[164:167], v[16:31]\n" \
  "ds_read_b64_tr_b16 a[66:67], v241 offset:24832\n" \
  "s_mov_b32 s42, 24576\n" \
  "v_mfma_f32_32x32x16_bf16 v[32:47], a[84:87], a[160:163], v[32:47]\n" \
  "ds_read_b64_tr_b16 a[68:69], v241 offset:25088\n" \
  "buffer_load_dwordx4 a[224:227], v232, %[srdH], s42 offen offset:0 sc1\n" \
  "v_mfma_f32_32x32x16_bf16 v[48:63], a[84:87], a[164:167], v[48:63]\n" \
  "ds_read_b64_tr_b16 a[70:71], v241 offset:25344\n" \
  "buffer_load_dwordx4 a[228:231], v232, %[srdH], s42 offen offset:1024 sc1\n" \
  "s_waitcnt lgkmcnt(5)\n" \
  "v_mfma_f32_32x32x16_bf16 v[64:79], a[88:91], a[160:163], v[64:79]\n" \
  "ds_read_b64_tr_b16 a[72:73], v241 offset:25600\n" \
  "v_mfma_f32_32x32x16_bf16 v[80:95], a[88:91], a[164:167], v[80:95]\n" \
  "ds_read_b64_tr_b16 a[74:75], v241 offset:25856\n" \
  "s_mov_b32 s42, 24576\n" \
  "v_mfma_f32_32x32x16_bf16 v[96:111], a[92:95], a[160:163], v[96:111]\n" \
  "ds_read_b64_tr_b16 a[76:77], v241 offset:26112\n" \
  "buffer_load_dwordx4 a[232:235], v232, %[srdH], s42 offen offset:2048 sc1\n" \
  "v_mfma_f32_32x32x16_bf16 v[112:127], a[92:95], a[164:167], v[112:127]\n" \
  "ds_read_b64_tr_b16 a[78:79], v241 offset:26368\n" \
  "buffer_load_dwordx4 a[236:239], v232, %[srdH], s42 offen offset:3072 sc1\n" \
  "s_waitcnt vmcnt(17)\n" \
  "v_mfma_f32_32x32x16_bf16 v[0:15], a[96:99], a[168:171], v[0:15]\n" \
  "ds_read_b64_tr_b16 a[80:81], v241 offset:28672\n" \
  "s_waitcnt vmcnt(16)\n" \
  "v_mfma_f32_32x32x16_bf16 v[16:31], a[96:99], a[172:175], v[16:31]\n" \
  "ds_read_b64_tr_b16 a[82:83], v241 offset:28928\n" \
  "s_mov_b32 s42, 28672\n" \
  "v_mfma_f32_32x32x16_bf16 v[32:47], a[100:103], a[168:171], v[32:47]\n" \
  "ds_read_b64_tr_b16 a[84:85], v241 offset:29184\n" \
  "buffer_load_dwordx4 a[240:243], v232, %[srdH], s42 offen offset:0 sc1\n" \
  "v_mfma_f32_32x32x16_bf16 v[48:63], a[100:103], a[172:175], v[48:63]\n" \
  "ds_read_b64_tr_b16 a[86:87], v241 offset:29440\n" \
  "buffer_load_dwordx4 a[244:247], v232, %[srdH], s42 offen offset:1024 sc1\n" \
  "v_mfma_f32_32x32x16_bf16 v[64:79], a[104:107], a[168:171], v[64:79]\n" \
  "ds_read_b64_tr_b16 a[88:89], v241 offset:29696\n" \
  "v_mfma_f32_32x32x16_bf16 v[80:95], a[104:107], a[172:175], v[80:95]\n" \
  "ds_read_b64_tr_b16 a[90:91], v241 offset:29952\n" \
  "s_mov_b32 s42, 28672\n" \
  "s_waitcnt lgkmcnt(5)\n" \
  "v_mfma_f32_32x32x16_bf16 v[96:111], a[108:111], a[168:171], v[96:111]\n" \
  "ds_read_b64_tr_b16 a[92:93], v241 offset:30208\n" \
  "buffer_load_dwordx4 a[248:251], v232, %[srdH], s42 offen offset:2048 sc1\n" \
  "v_mfma_f32_32x32x16_bf16 v[112:127], a[108:111], a[172:175], v[112:127]\n" \
  "ds_read_b64_tr_b16 a[94:95], v241 offset:30464\n" \
  "buffer_load_dwordx4 a[252:255], v232, %[srdH], s42 offen offset:3072 sc1\n" \
  "s_barrier\n" \
  "s_cmp_lg_u32 %[w], 0\n" \
  "s_cbranch_scc1 .Lrelp_%=\n" \
  "v_mov_b32 v228, %[tag]\n" \
  "v_mov_b32 v229, 0\n" \
  "s_mov_b64 exec, 1\n" \
  "global_store_dword v229, v228, %[frelo] sc1\n" \
  "s_mov_b64 exec, -1\n" \
  ".Lrelp_%=:\n" \
  "s_waitcnt vmcnt(19)\n" \
  "v_mfma_f32_32x32x16_bf16 v[0:15], a[64:67], a[176:179], v[0:15]\n" \
  "ds_read_b64_tr_b16 a[96:97], v241 offset:32768\n" \
  "s_waitcnt vmcnt(18)\n" \
  "v_mfma_f32_32x32x16_bf16 v[16:31], a[64:67], a[180:183], v[16:31]\n" \
  "ds_read_b64_tr_b16 a[98:99], v241 offset:33024\n" \
  "s_mov_b32 s42, 32768\n" \
  "s_add_u32 m0, s60, 65536\n" \
  "v_mfma_f32_32x32x16_bf16 v[32:47], a[68:71], a[176:179], v[32:47]\n" \
  "ds_read_b64_tr_b16 a[100:101], v241 offset:33280\n" \
  "buffer_load_dwordx4 a[128:131], v232, %[srdH], s42 offen offset:0 sc1\n" \
  "s_mov_b32 s40, 3145728\n" \
  "v_mfma_f32_32x32x16_bf16 v[48:63], a[68:71], a[180:183], v[48:63]\n" \
  "ds_read_b64_tr_b16 a[102:103], v241 offset:33536\n" \
  "buffer_load_dwordx4 a[132:135], v232, %[srdH], s42 offen offset:1024 sc1\n" \
  "buffer_load_dwordx4 v239, %[srdW], s40 offen offset:0 lds\n" \
  "v_mfma_f32_32x32x16_bf16 v[64:79], a[72:75], a[176:179], v[64:79]\n" \
  "ds_read_b64_tr_b16 a[104:105], v241 offset:33792\n" \
  "s_mov_b32 s41, 3209216\n" \
  "v_mfma_f32_32x32x16_bf16 v[80:95], a[72:75], a[180:183], v[80:95]\n" \
  "ds_read_b64_tr_b16 a[106:107], v241 offset:34048\n" \
  "s_mov_b32 s42, 32768\n" \
  "buffer_load_dwordx4 v240, %[srdW], s40 offen offset:1024 lds\n" \
  "v_mfma_f32_32x32x16_bf16 v[96:111], a[76:79], a[176:179], v[96:111]\n" \
  "ds_read_b64_tr_b16 a[108:109], v241 offset:34304\n" \
  "buffer_load_dwordx4 a[136:139], v232, %[srdH], s42 offen offset:2048 sc1\n" \
  "buffer_load_dwordx4 v239, %[srdW], s41 offen offset:2048 lds\n" \
  "v_mfma_f32_32x32x16_bf16 v[112:127], a[76:79], a[180:183], v[112:127]\n" \
  "ds_read_b64_tr_b16 a[110:111], v241 offset:34560\n" \
  "buffer_load_dwordx4 a[140:143], v232, %[srdH], s42 offen offset:3072 sc1\n" \
  "buffer_load_dwordx4 v240, %[srdW], s41 offen offset:3072 lds\n" \
  "s_waitcnt vmcnt(25) lgkmcnt(5)\n" \
  "v_mfma_f32_32x32x16_bf16 v[0:15], a[80:83], a[184:187], v[0:15]\n" \
  "ds_read_b64_tr_b16 a[64:65], v241 offset:36864\n" \
  "s_waitcnt vmcnt(24)\n" \
  "v_mfma_f32_32x32x16_bf16 v[16:31], a[80:83], a[188:191], v[16:31]\n" \
  "ds_read_b64_tr_b16 a[66:67], v241 offset:37120\n" \
  "v_mfma_f32_32x32x16_bf16 v[32:47], a[84:87], a[184:187], v[32:47]\n" \
  "ds_read_b64_tr_b16 a[68:69], v241 offset:37376\n" \
  "s_mov_b32 s42, 36864\n" \
  "v_mfma_f32_32x32x16_bf16 v[48:63], a[84:87], a[188:191], v[48:63]\n" \
  "ds_read_b64_tr_b16 a[70:71], v241 offset:37632\n" \
  "v_mfma_f32_32x32x16_bf16 v[64:79], a[88:91], a[184:187], v[64:79]\n" \
  "ds_read_b64_tr_b16 a[72:73], v241 offset:37888\n" \
  "v_mfma_f32_32x32x16_bf16 v[80:95], a[88:91], a[188:191], v[80:95]\n" \
  "ds_read_b64_tr_b16 a[74:75], v241 offset:38144\n" \
  "buffer_load_dwordx4 a[144:147], v232, %[srdH], s42 offen offset:0 sc1\n" \
  "v_mfma_f32_32x32x16_bf16 v[96:111], a[92:95], a[184:187], v[96:111]\n" \
  "ds_read_b64_tr_b16 a[76:77], v241 offset:38400\n" \
  "v_mfma_f32_32x32x16_bf16 v[112:127], a[92:95], a[188:191], v[112:127]\n" \
  "ds_read_b64_tr_b16 a[78:79], v241 offset:38656\n" \
  "buffer_load_dwordx4 a[148:151], v232, %[srdH], s42 offen offset:1024 sc1\n" \
  "s_waitcnt vmcnt(25)\n" \
  "v_mfma_f32_32x32x16_bf16 v[0:15], a[96:99], a[192:195], v[0:15]\n" \
  "ds_read_b64_tr_b16 a[80:81], v241 offset:40960\n" \
  "s_waitcnt vmcnt(24)\n" \
  "v_mfma_f32_32x32x16_bf16 v[16:31], a[96:99], a[196:199], v[16:31]\n" \
  "ds_read_b64_tr_b16 a[82:83], v241 offset:41216\n" \
  "s_waitcnt lgkmcnt(5)\n" \
  "v_mfma_f32_32x32x16_bf16 v[32:47], a[100:103], a[192:195], v[32:47]\n" \
  "ds_read_b64_tr_b16 a[84:85], v241 offset:41472\n" \
  "s_mov_b32 s42, 36864\n" \
  "v_mfma_f32_32x32x16_bf16 v[48:63], a[100:103], a[196:199], v[48:63]\n" \
  "ds_read_b64_tr_b16 a[86:87], v241 offset:41728\n" \
  "v_mfma_f32_32x32x16_bf16 v[64:79], a[104:107], a[192:195], v[64:79]\n" \
  "ds_read_b64_tr_b16 a[88:89], v241 offset:41984\n" \
  "v_mfma_f32_32x32x16_bf16 v[80:95], a[104:107], a[196:199], v[80:95]\n" \
  "ds_read_b64_tr_b16 a[90:91], v241 offset:42240\n" \
  "buffer_load_dwordx4 a[152:155], v232, %[srdH], s42 offen offset:2048 sc1\n" \
  "v_mfma_f32_32x32x16_bf16 v[96:111], a[108:111], a[192:195], v[96:111]\n" \
  "ds_read_b64_tr_b16 a[92:93], v241 offset:42496\n" \
  "v_mfma_f32_32x32x16_bf16 v[112:127], a[108:111], a[196:199], v[112:127]\n" \
  "ds_read_b64_tr_b16 a[94:95], v241 offset:42752\n" \
  "buffer_load_dwordx4 a[156:159], v232, %[srdH], s42 offen offset:3072 sc1\n" \
  "s_waitcnt vmcnt(25)\n" \
  "v_mfma_f32_32x32x16_bf16 v[0:15], a[64:67], a[200:203], v[0:15]\n" \
  "ds_read_b64_tr_b16 a[96:97], v241 offset:45056\n" \
  "s_waitcnt vmcnt(24)\n" \
  "v_mfma_f32_32x32x16_bf16 v[16:31], a[64:67], a[204:207], v[16:31]\n" \
  "ds_read_b64_tr_b16 a[98:99], v241 offset:45312\n" \
  "v_mfma_f32_32x32x16_bf16 v[32:47], a[68:71], a[200:203], v[32:47]\n" \
  "ds_read_b64_tr_b16 a[100:101], v241 offset:45568\n" \
  "s_mov_b32 s42, 40960\n" \
  "v_mfma_f32_32x32x16_bf16 v[48:63], a[68:71], a[204:207], v[48:63]\n" \
  "ds_read_b64_tr_b16 a[102:103], v241 offset:45824\n" \
  "s_waitcnt lgkmcnt(5)\n" \
  "v_mfma_f32_32x32x16_bf16 v[64:79], a[72:75], a[200:203], v[64:79]\n" \
  "ds_read_b64_tr_b16 a[104:105], v241 offset:46080\n" \
  "v_mfma_f32_32x32x16_bf16 v[80:95], a[72:75], a[204:207], v[80:95]\n" \
  "ds_read_b64_tr_b16 a[106:107], v241 offset:46336\n" \
  "buffer_load_dwordx4 a[160:163], v232, %[srdH], s42 offen offset:0 sc1\n" \
  "v_mfma_f32_32x32x16_bf16 v[96:111], a[76:79], a[200:203], v[96:111]\n" \
  "ds_read_b64_tr_b16 a[108:109], v241 offset:46592\n" \
  "v_mfma_f32_32x32x16_bf16 v[112:127], a[76:79], a[204:207], v[112:127]\n" \
  "ds_read_b64_tr_b16 a[110:111], v241 offset:46848\n" \
  "buffer_load_dwordx4 a[164:167], v232, %[srdH], s42 offen offset:1024 sc1\n" \
  "s_barrier\n" \
  "s_waitcnt vmcnt(25)\n" \
  "v_mfma_f32_32x32x16_bf16 v[0:15], a[80:83], a[208:211], v[0:15]\n" \
  "ds_read_b64_tr_b16 a[64:65], v241 offset:49152\n" \
  "s_waitcnt vmcnt(24)\n" \
  "v_mfma_f32_32x32x16_bf16 v[16:31], a[80:83], a[212:215], v[16:31]\n" \
  "ds_read_b64_tr_b16 a[66:67], v241 offset:49408\n" \
  "s_add_u32 m0, s60, 81920\n" \
  "v_mfma_f32_32x32x16_bf16 v[32:47], a[84:87], a[208:211], v[32:47]\n" \
  "ds_read_b64_tr_b16 a[68:69], v241 offset:49664\n" \
  "s_mov_b32 s42, 40960\n" \
  "s_mov_b32 s40, 3670016\n" \
  "v_mfma_f32_32x32x16_bf16 v[48:63], a[84:87], a[212:215], v[48:63]\n" \
  "ds_read_b64_tr_b16 a[70:71], v241 offset:49920\n" \
  "buffer_load_dwordx4 v239, %[srdW], s40 offen offset:0 lds\n" \
  "v_mfma_f32_32x32x16_bf16 v[64:79], a[88:91], a[208:211], v[64:79]\n" \
  "ds_read_b64_tr_b16 a[72:73], v241 offset:50176\n" \
  "s_mov_b32 s41, 3733504\n" \
  "v_mfma_f32_32x32x16_bf16 v[80:95], a[88:91], a[212:215], v[80:95]\n" \
  "ds_read_b64_tr_b16 a[74:75], v241 offset:50432\n" \
  "buffer_load_dwordx4 a[168:171], v232, %[srdH], s42 offen offset:2048 sc1\n" \
  "buffer_load_dwordx4 v240, %[srdW], s40 offen offset:1024 lds\n" \
  "s_waitcnt lgkmcnt(5)\n" \
  "v_mfma_f32_32x32x16_bf16 v[96:111], a[92:95], a[208:211], v[96:111]\n" \
  "ds_read_b64_tr_b16 a[76:77], v241 offset:50688\n" \
  "buffer_load_dwordx4 v239, %[srdW], s41 offen offset:2048 lds\n" \
  "v_mfma_f32_32x32x16_bf16 v[112:127], a[92:95], a[212:215], v[112:127]\n" \
  "ds_read_b64_tr_b16 a[78:79], v241 offset:50944\n" \
  "buffer_load_dwordx4 a[172:175], v232, %[srdH], s42 offen offset:3072 sc1\n" \
  "buffer_load_dwordx4 v240, %[srdW], s41 offen offset:3072 lds\n" \
  "s_waitcnt vmcnt(29)\n" \
  "v_mfma_f32_32x32x16_bf16 v[0:15], a[96:99], a[216:219], v[0:15]\n" \
  "ds_read_b64_tr_b16 a[80:81], v241 offset:53248\n" \
  "s_waitcnt vmcnt(28)\n" \
  "v_mfma_f32_32x32x16_bf16 v[16:31], a[96:99], a[220:223], v[16:31]\n" \
  "ds_read_b64_tr_b16 a[82:83], v241 offset:53504\n" \
  "v_mfma_f32_32x32x16_bf16 v[32:47], a[100:103], a[216:219], v[32:47]\n" \
  "ds_read_b64_tr_b16 a[84:85], v241 offset:53760\n" \
  "s_mov_b32 s42, 45056\n" \
  "v_mfma_f32_32x32x16_bf16 v[48:63], a[100:103], a[220:223], v[48:63]\n" \
  "ds_read_b64_tr_b16 a[86:87], v241 offset:54016\n" \
  "v_mfma_f32_32x32x16_bf16 v[64:79], a[104:107], a[216:219], v[64:79]\n" \
  "ds_read_b64_tr_b16 a[88:89], v241 offset:54272\n" \
  "v_mfma_f32_32x32x16_bf16 v[80:95], a[104:107], a[220:223], v[80:95]\n" \
  "ds_read_b64_tr_b16 a[90:91], v241 offset:54528\n" \
  "buffer_load_dwordx4 a[176:179], v232, %[srdH], s42 offen offset:0 sc1\n" \
  "v_mfma_f32_32x32x16_bf16 v[96:111], a[108:111], a[216:219], v[96:111]\n" \
  "ds_read_b64_tr_b16 a[92:93], v241 offset:54784\n" \
  "v_mfma_f32_32x32x16_bf16 v[112:127], a[108:111], a[220:223], v[112:127]\n" \
  "ds_read_b64_tr_b16 a[94:95], v241 offset:55040\n" \
  "buffer_load_dwordx4 a[180:183], v232, %[srdH], s42 offen offset:1024 sc1\n" \
  "s_waitcnt vmcnt(29) lgkmcnt(5)\n" \
  "v_mfma_f32_32x32x16_bf16 v[0:15], a[64:67], a[224:227], v[0:15]\n" \
  "ds_read_b64_tr_b16 a[96:97], v241 offset:57344\n" \
  "s_waitcnt vmcnt(28)\n" \
  "v_mfma_f32_32x32x16_bf16 v[16:31], a[64:67], a[228:231], v[16:31]\n" \
  "ds_read_b64_tr_b16 a[98:99], v241 offset:57600\n" \
  "v_mfma_f32_32x32x16_bf16 v[32:47], a[68:71], a[224:227], v[32:47]\n" \
  "ds_read_b64_tr_b16 a[100:101], v241 offset:57856\n" \
  "s_mov_b32 s42, 45056\n" \
  "v_mfma_f32_32x32x16_bf16 v[48:63], a[68:71], a[228:231], v[48:63]\n" \
  "ds_read_b64_tr_b16 a[102:103], v241 offset:58112\n" \
  "v_mfma_f32_32x32x16_bf16 v[64:79], a[72:75], a[224:227], v[64:79]\n" \
  "ds_read_b64_tr_b16 a[104:105], v241 offset:58368\n" \
  "v_mfma_f32_32x32x16_bf16 v[80:95], a[72:75], a[228:231], v[80:95]\n" \
  "ds_read_b64_tr_b16 a[106:107], v241 offset:58624\n" \
  "buffer_load_dwordx4 a[184:187], v232, %[srdH], s42 offen offset:2048 sc1\n" \
  "v_mfma_f32_32x32x16_bf16 v[96:111], a[76:79], a[224:227], v[96:111]\n" \
  "ds_read_b64_tr_b16 a[108:109], v241 offset:58880\n" \
  "v_mfma_f32_32x32x16_bf16 v[112:127], a[76:79], a[228:231], v[112:127]\n" \
  "ds_read_b64_tr_b16 a[110:111], v241 offset:59136\n" \
  "buffer_load_dwordx4 a[188:191], v232, %[srdH], s42 offen offset:3072 sc1\n" \
  "s_waitcnt vmcnt(29)\n" \
  "v_mfma_f32_32x32x16_bf16 v[0:15], a[80:83], a[232:235], v[0:15]\n" \
  "ds_read_b64_tr_b16 a[64:65], v241 offset:61440\n" \
  "s_waitcnt vmcnt(28)\n" \
  "v_mfma_f32_32x32x16_bf16 v[16:31], a[80:83], a[236:239], v[16:31]\n" \
  "ds_read_b64_tr_b16 a[66:67], v241 offset:61696\n" \
  "s_waitcnt lgkmcnt(5)\n" \
  "v_mfma_f32_32x32x16_bf16 v[32:47], a[84:87], a[232:235], v[32:47]\n" \
  "ds_read_b64_tr_b16 a[68:69], v241 offset:61952\n" \
  "s_mov_b32 s42, 49152\n" \
  "v_mfma_f32_32x32x16_bf16 v[48:63], a[84:87], a[236:239], v[48:63]\n" \
  "ds_read_b64_tr_b16 a[70:71], v241 offset:62208\n" \
  "v_mfma_f32_32x32x16_bf16 v[64:79], a[88:91], a[232:235], v[64:79]\n" \
  "ds_read_b64_tr_b16 a[72:73], v241 offset:62464\n" \
  "v_mfma_f32_32x32x16_bf16 v[80:95], a[88:91], a[236:239], v[80:95]\n" \
  "ds_read_b64_tr_b16 a[74:75], v241 offset:62720\n" \
  "buffer_load_dwordx4 a[192:195], v232, %[srdH], s42 offen offset:0 sc1\n" \
  "v_mfma_f32_32x32x16_bf16 v[96:111], a[92:95], a[232:235], v[96:111]\n" \
  "ds_read_b64_tr_b16 a[76:77], v241 offset:62976\n" \
  "v_mfma_f32_32x32x16_bf16 v[112:127], a[92:95], a[236:239], v[112:127]\n" \
  "ds_read_b64_tr_b16 a[78:79], v241 offset:63232\n" \
  "buffer_load_dwordx4 a[196:199], v232, %[srdH], s42 offen offset:1024 sc1\n" \
  "s_barrier\n" \
  "s_waitcnt vmcnt(29)\n" \
  "v_mfma_f32_32x32x16_bf16 v[0:15], a[96:99], a[240:243], v[0:15]\n" \
  "ds_read_b64_tr_b16 a[80:81], v242 offset:0\n" \
  "s_waitcnt vmcnt(28)\n" \
  "v_mfma_f32_32x32x16_bf16 v[16:31], a[96:99], a[244:247], v[16:31]\n" \
  "ds_read_b64_tr_b16 a[82:83], v242 offset:256\n" \
  "s_add_u32 m0, s60, 98304\n" \
  "v_mfma_f32_32x32x16_bf16 v[32:47], a[100:103], a[240:243], v[32:47]\n" \
  "ds_read_b64_tr_b16 a[84:85], v242 offset:512\n" \
  "s_mov_b32 s42, 49152\n" \
  "s_mov_b32 s40, 4194304\n" \
  "v_mfma_f32_32x32x16_bf16 v[48:63], a[100:103], a[244:247], v[48:63]\n" \
  "ds_read_b64_tr_b16 a[86:87], v242 offset:768\n" \
  "buffer_load_dwordx4 v239, %[srdW], s40 offen offset:0 lds\n" \
  "s_waitcnt lgkmcnt(5)\n" \
  "v_mfma_f32_32x32x16_bf16 v[64:79], a[104:107], a[240:243], v[64:79]\n" \
  "ds_read_b64_tr_b16 a[88:89], v242 offset:1024\n" \
  "s_mov_b32 s41, 4257792\n" \
  "v_mfma_f32_32x32x16_bf16 v[80:95], a[104:107], a[244:247], v[80:95]\n" \
  "ds_read_b64_tr_b16 a[90:91], v242 offset:1280\n" \
  "buffer_load_dwordx4 a[200:203], v232, %[srdH], s42 offen offset:2048 sc1\n" \
  "buffer_load_dwordx4 v240, %[srdW], s40 offen offset:1024 lds\n" \
  "v_mfma_f32_32x32x16_bf16 v[96:111], a[108:111], a[240:243], v[96:111]\n" \
  "ds_read_b64_tr_b16 a[92:93], v242 offset:1536\n" \
  "buffer_load_dwordx4 v239, %[srdW], s41 offen offset:2048 lds\n" \
  "v_mfma_f32_32x32x16_bf16 v[112:127], a[108:111], a[244:247], v[112:127]\n" \
  "ds_read_b64_tr_b16 a[94:95], v242 offset:1792\n" \
  "buffer_load_dwordx4 a[204:207], v232, %[srdH], s42 offen offset:3072 sc1\n" \
  "buffer_load_dwordx4 v240, %[srdW], s41 offen offset:3072 lds\n" \
  "s_waitcnt vmcnt(33)\n" \
  "v_mfma_f32_32x32x16_bf16 v[0:15], a[64:67], a[248:251], v[0:15]\n" \
  "ds_read_b64_tr_b16 a[96:97], v242 offset:4096\n" \
  "s_waitcnt vmcnt(32)\n" \
  "v_mfma_f32_32x32x16_bf16 v[16:31], a[64:67], a[252:255], v[16:31]\n" \
  "ds_read_b64_tr_b16 a[98:99], v242 offset:4352\n" \
  "v_mfma_f32_32x32x16_bf16 v[32:47], a[68:71], a[248:251], v[32:47]\n" \
  "ds_read_b64_tr_b16 a[100:101], v242 offset:4608\n" \
  "s_mov_b32 s42, 53248\n" \
  "v_mfma_f32_32x32x16_bf16 v[48:63], a[68:71], a[252:255], v[48:63]\n" \
  "ds_read_b64_tr_b16 a[102:103], v242 offset:4864\n" \
  "v_mfma_f32_32x32x16_bf16 v[64:79], a[72:75], a[248:251], v[64:79]\n" \
  "ds_read_b64_tr_b16 a[104:105], v242 offset:5120\n" \
  "v_mfma_f32_32x32x16_bf16 v[80:95], a[72:75], a[252:255], v[80:95]\n" \
  "ds_read_b64_tr_b16 a[106:107], v242 offset:5376\n" \
  "buffer_load_dwordx4 a[208:211], v232, %[srdH], s42 offen offset:0 sc1\n" \
  "s_waitcnt lgkmcnt(5)\n" \
  "v_mfma_f32_32x32x16_bf16 v[96:111], a[76:79], a[248:251], v[96:111]\n" \
  "ds_read_b64_tr_b16 a[108:109], v242 offset:5632\n" \
  "v_mfma_f32_32x32x16_bf16 v[112:127], a[76:79], a[252:255], v[112:127]\n" \
  "ds_read_b64_tr_b16 a[110:111], v242 offset:5888\n" \
  "buffer_load_dwordx4 a[212:215], v232, %[srdH], s42 offen offset:1024 sc1\n" \
  "s_waitcnt vmcnt(33)\n" \
  "v_mfma_f32_32x32x16_bf16 v[0:15], a[80:83], a[128:131], v[0:15]\n" \
  "ds_read_b64_tr_b16 a[64:65], v242 offset:8192\n" \
  "s_waitcnt vmcnt(32)\n" \
  "v_mfma_f32_32x32x16_bf16 v[16:31], a[80:83], a[132:135], v[16:31]\n" \
  "ds_read_b64_tr_b16 a[66:67], v242 offset:8448\n" \
  "v_mfma_f32_32x32x16_bf16 v[32:47], a[84:87], a[128:131], v[32:47]\n" \
  "ds_read_b64_tr_b16 a[68:69], v242 offset:8704\n" \
  "s_mov_b32 s42, 53248\n" \
  "v_mfma_f32_32x32x16_bf16 v[48:63], a[84:87], a[132:135], v[48:63]\n" \
  "ds_read_b64_tr_b16 a[70:71], v242 offset:8960\n" \
  "v_mfma_f32_32x32x16_bf16 v[64:79], a[88:91], a[128:131], v[64:79]\n" \
  "ds_read_b64_tr_b16 a[72:73], v242 offset:9216\n" \
  "v_mfma_f32_32x32x16_bf16 v[80:95], a[88:91], a[132:135], v[80:95]\n" \
  "ds_read_b64_tr_b16 a[74:75], v242 offset:9472\n" \
  "buffer_load_dwordx4 a[216:219], v232, %[srdH], s42 offen offset:2048 sc1\n" \
  "v_mfma_f32_32x32x16_bf16 v[96:111], a[92:95], a[128:131], v[96:111]\n" \
  "ds_read_b64_tr_b16 a[76:77], v242 offset:9728\n" \
  "v_mfma_f32_32x32x16_bf16 v[112:127], a[92:95], a[132:135], v[112:127]\n" \
  "ds_read_b64_tr_b16 a[78:79], v242 offset:9984\n" \
  "buffer_load_dwordx4 a[220:223], v232, %[srdH], s42 offen offset:3072 sc1\n" \
  "s_waitcnt vmcnt(31) lgkmcnt(5)\n" \
  "v_mfma_f32_32x32x16_bf16 v[0:15], a[96:99], a[136:139], v[0:15]\n" \
  "ds_read_b64_tr_b16 a[80:81], v242 offset:12288\n" \
  "s_waitcnt vmcnt(29)\n" \
  "v_mfma_f32_32x32x16_bf16 v[16:31], a[96:99], a[140:143], v[16:31]\n" \
  "ds_read_b64_tr_b16 a[82:83], v242 offset:12544\n" \
  "v_mfma_f32_32x32x16_bf16 v[32:47], a[100:103], a[136:139], v[32:47]\n" \
  "ds_read_b64_tr_b16 a[84:85], v242 offset:12800\n" \
  "s_mov_b32 s42, 57344\n" \
  "v_mfma_f32_32x32x16_bf16 v[48:63], a[100:103], a[140:143], v[48:63]\n" \
  "ds_read_b64_tr_b16 a[86:87], v242 offset:13056\n" \
  "v_mfma_f32_32x32x16_bf16 v[64:79], a[104:107], a[136:139], v[64:79]\n" \
  "ds_read_b64_tr_b16 a[88:89], v242 offset:13312\n" \
  "v_mfma_f32_32x32x16_bf16 v[80:95], a[104:107], a[140:143], v[80:95]\n" \
  "ds_read_b64_tr_b16 a[90:91], v242 offset:13568\n" \
  "buffer_load_dwordx4 a[224:227], v232, %[srdH], s42 offen offset:0 sc1\n" \
  "v_mfma_f32_32x32x16_bf16 v[96:111], a[108:111], a[136:139], v[96:111]\n" \
  "ds_read_b64_tr_b16 a[92:93], v242 offset:13824\n" \
  "v_mfma_f32_32x32x16_bf16 v[112:127], a[108:111], a[140:143], v[112:127]\n" \
  "ds_read_b64_tr_b16 a[94:95], v242 offset:14080\n" \
  "buffer_load_dwordx4 a[228:231], v232, %[srdH], s42 offen offset:1024 sc1\n" \
  "s_barrier\n" \
  "s_waitcnt vmcnt(29)\n" \
  "v_mfma_f32_32x32x16_bf16 v[0:15], a[64:67], a[144:147], v[0:15]\n" \
  "ds_read_b64_tr_b16 a[96:97], v242 offset:16384\n" \
  "s_waitcnt vmcnt(28)\n" \
  "v_mfma_f32_32x32x16_bf16 v[16:31], a[64:67], a[148:151], v[16:31]\n" \
  "ds_read_b64_tr_b16 a[98:99], v242 offset:16640\n" \
  "s_add_u32 m0, s60, 114688\n" \
  "s_waitcnt lgkmcnt(5)\n" \
  "v_mfma_f32_32x32x16_bf16 v[32:47], a[68:71], a[144:147], v[32:47]\n" \
  "ds_read_b64_tr_b16 a[100:101], v242 offset:16896\n" \
  "s_mov_b32 s42, 57344\n" \
  "s_mov_b32 s40, 4718592\n" \
  "v_mfma_f32_32x32x16_bf16 v[48:63], a[68:71], a[148:151], v[48:63]\n" \
  "ds_read_b64_tr_b16 a[102:103], v242 offset:17152\n" \
  "buffer_load_dwordx4 v239, %[srdW], s40 offen offset:0 lds\n" \
  "v_mfma_f32_32x32x16_bf16 v[64:79], a[72:75], a[144:147], v[64:79]\n" \
  "ds_read_b64_tr_b16 a[104:105], v242 offset:17408\n" \
  "s_mov_b32 s41, 4782080\n" \
  "v_mfma_f32_32x32x16_bf16 v[80:95], a[72:75], a[148:151], v[80:95]\n" \
  "ds_read_b64_tr_b16 a[106:107], v242 offset:17664\n" \
  "buffer_load_dwordx4 a[232:235], v232, %[srdH], s42 offen offset:2048 sc1\n" \
  "buffer_load_dwordx4 v240, %[srdW], s40 offen offset:1024 lds\n" \
  "v_mfma_f32_32x32x16_bf16 v[96:111], a[76:79], a[144:147], v[96:111]\n" \
  "ds_read_b64_tr_b16 a[108:109], v242 offset:17920\n" \
  "buffer_load_dwordx4 v239, %[srdW], s41 offen offset:2048 lds\n" \
  "v_mfma_f32_32x32x16_bf16 v[112:127], a[76:79], a[148:151], v[112:127]\n" \
  "ds_read_b64_tr_b16 a[110:111], v242 offset:18176\n" \
  "buffer_load_dwordx4 a[236:239], v232, %[srdH], s42 offen offset:3072 sc1\n" \
  "buffer_load_dwordx4 v240, %[srdW], s41 offen offset:3072 lds\n" \
  "s_waitcnt vmcnt(33)\n" \
  "v_mfma_f32_32x32x16_bf16 v[0:15], a[80:83], a[152:155], v[0:15]\n" \
  "ds_read_b64_tr_b16 a[64:65], v242 offset:20480\n" \
  "s_waitcnt vmcnt(32)\n" \
  "v_mfma_f32_32x32x16_bf16 v[16:31], a[80:83], a[156:159], v[16:31]\n" \
  "ds_read_b64_tr_b16 a[66:67], v242 offset:20736\n" \
  "v_mfma_f32_32x32x16_bf16 v[32:47], a[84:87], a[152:155], v[32:47]\n" \
  "ds_read_b64_tr_b16 a[68:69], v242 offset:20992\n" \
  "s_mov_b32 s42, 61440\n" \
  "v_mfma_f32_32x32x16_bf16 v[48:63], a[84:87], a[156:159], v[48:63]\n" \
  "ds_read_b64_tr_b16 a[70:71], v242 offset:21248\n" \
  "s_waitcnt lgkmcnt(5)\n" \
  "v_mfma_f32_32x32x16_bf16 v[64:79], a[88:91], a[152:155], v[64:79]\n" \
  "ds_read_b64_tr_b16 a[72:73], v242 offset:21504\n" \
  "v_mfma_f32_32x32x16_bf16 v[80:95], a[88:91], a[156:159], v[80:95]\n" \
  "ds_read_b64_tr_b16 a[74:75], v242 offset:21760\n" \
  "buffer_load_dwordx4 a[240:243], v232, %[srdH], s42 offen offset:0 sc1\n" \
  "v_mfma_f32_32x32x16_bf16 v[96:111], a[92:95], a[152:155], v[96:111]\n" \
  "ds_read_b64_tr_b16 a[76:77], v242 offset:22016\n" \
  "v_mfma_f32_32x32x16_bf16 v[112:127], a[92:95], a[156:159], v[112:127]\n" \
  "ds_read_b64_tr_b16 a[78:79], v242 offset:22272\n" \
  "buffer_load_dwordx4 a[244:247], v232, %[srdH], s42 offen offset:1024 sc1\n" \
  "s_waitcnt vmcnt(33)\n" \
  "v_mfma_f32_32x32x16_bf16 v[0:15], a[96:99], a[160:163], v[0:15]\n" \
  "ds_read_b64_tr_b16 a[80:81], v242 offset:24576\n" \
  "s_waitcnt vmcnt(32)\n" \
  "v_mfma_f32_32x32x16_bf16 v[16:31], a[96:99], a[164:167], v[16:31]\n" \
  "ds_read_b64_tr_b16 a[82:83], v242 offset:24832\n" \
  "v_mfma_f32_32x32x16_bf16 v[32:47], a[100:103], a[160:163], v[32:47]\n" \
  "ds_read_b64_tr_b16 a[84:85], v242 offset:25088\n" \
  "s_mov_b32 s42, 61440\n" \
  "v_mfma_f32_32x32x16_bf16 v[48:63], a[100:103], a[164:167], v[48:63]\n" \
  "ds_read_b64_tr_b16 a[86:87], v242 offset:25344\n" \
  "v_mfma_f32_32x32x16_bf16 v[64:79], a[104:107], a[160:163], v[64:79]\n" \
  "ds_read_b64_tr_b16 a[88:89], v242 offset:25600\n" \
  "v_mfma_f32_32x32x16_bf16 v[80:95], a[104:107], a[164:167], v[80:95]\n" \
  "ds_read_b64_tr_b16 a[90:91], v242 offset:25856\n" \
  "buffer_load_dwordx4 a[248:251], v232, %[srdH], s42 offen offset:2048 sc1\n" \
  "s_waitcnt lgkmcnt(5)\n" \
  "v_mfma_f32_32x32x16_bf16 v[96:111], a[108:111], a[160:163], v[96:111]\n" \
  "ds_read_b64_tr_b16 a[92:93], v242 offset:26112\n" \
  "v_mfma_f32_32x32x16_bf16 v[112:127], a[108:111], a[164:167], v[112:127]\n" \
  "ds_read_b64_tr_b16 a[94:95], v242 offset:26368\n" \
  "buffer_load_dwordx4 a[252:255], v232, %[srdH], s42 offen offset:3072 sc1\n" \
  "s_waitcnt vmcnt(32)\n" \
  "v_mfma_f32_32x32x16_bf16 v[0:15], a[64:67], a[168:171], v[0:15]\n" \
  "ds_read_b64_tr_b16 a[96:97], v242 offset:28672\n" \
  "s_waitcnt vmcnt(29)\n" \
  "v_mfma_f32_32x32x16_bf16 v[16:31], a[64:67], a[172:175], v[16:31]\n" \
  "ds_read_b64_tr_b16 a[98:99], v242 offset:28928\n" \
  "v_mfma_f32_32x32x16_bf16 v[32:47], a[68:71], a[168:171], v[32:47]\n" \
  "ds_read_b64_tr_b16 a[100:101], v242 offset:29184\n" \
  "s_mov_b32 s42, 65536\n" \
  "v_mfma_f32_32x32x16_bf16 v[48:63], a[68:71], a[172:175], v[48:63]\n" \
  "ds_read_b64_tr_b16 a[102:103], v242 offset:29440\n" \
  "v_mfma_f32_32x32x16_bf16 v[64:79], a[72:75], a[168:171], v[64:79]\n" \
  "ds_read_b64_tr_b16 a[104:105], v242 offset:29696\n" \
  "v_mfma_f32_32x32x16_bf16 v[80:95], a[72:75], a[172:175], v[80:95]\n" \
  "ds_read_b64_tr_b16 a[106:107], v242 offset:29952\n" \
  "buffer_load_dwordx4 a[128:131], v232, %[srdH], s42 offen offset:0 sc1\n" \
  "v_mfma_f32_32x32x16_bf16 v[96:111], a[76:79], a[168:171], v[96:111]\n" \
  "ds_read_b64_tr_b16 a[108:109], v242 offset:30208\n" \
  "v_mfma_f32_32x32x16_bf16 v[112:127], a[76:79], a[172:175], v[112:127]\n" \
  "ds_read_b64_tr_b16 a[110:111], v242 offset:30464\n" \
  "buffer_load_dwordx4 a[132:135], v232, %[srdH], s42 offen offset:1024 sc1\n" \
  "s_barrier\n" \
  "s_waitcnt vmcnt(29) lgkmcnt(5)\n" \
  "v_mfma_f32_32x32x16_bf16 v[0:15], a[80:83], a[176:179], v[0:15]\n" \
  "ds_read_b64_tr_b16 a[64:65], v241 offset:0\n" \
  "s_waitcnt vmcnt(28)\n" \
  "v_mfma_f32_32x32x16_bf16 v[16:31], a[80:83], a[180:183], v[16:31]\n" \
  "ds_read_b64_tr_b16 a[66:67], v241 offset:256\n" \
  "v_mfma_f32_32x32x16_bf16 v[32:47], a[84:87], a[176:179], v[32:47]\n" \
  "ds_read_b64_tr_b16 a[68:69], v241 offset:512\n" \
  "s_mov_b32 s42, 65536\n" \
  "v_mfma_f32_32x32x16_bf16 v[48:63], a[84:87], a[180:183], v[48:63]\n" \
  "ds_read_b64_tr_b16 a[70:71], v241 offset:768\n" \
  "v_mfma_f32_32x32x16_bf16 v[64:79], a[88:91], a[176:179], v[64:79]\n" \
  "ds_read_b64_tr_b16 a[72:73], v241 offset:1024\n" \
  "v_mfma_f32_32x32x16_bf16 v[80:95], a[88:91], a[180:183], v[80:95]\n" \
  "ds_read_b64_tr_b16 a[74:75], v241 offset:1280\n" \
  "buffer_load_dwordx4 a[136:139], v232, %[srdH], s42 offen offset:2048 sc1\n" \
  "v_mfma_f32_32x32x16_bf16 v[96:111], a[92:95], a[176:179], v[96:111]\n" \
  "ds_read_b64_tr_b16 a[76:77], v241 offset:1536\n" \
  "v_mfma_f32_32x32x16_bf16 v[112:127], a[92:95], a[180:183], v[112:127]\n" \
  "ds_read_b64_tr_b16 a[78:79], v241 offset:1792\n" \
  "buffer_load_dwordx4 a[140:143], v232, %[srdH], s42 offen offset:3072 sc1\n" \
  "s_waitcnt vmcnt(29)\n" \
  "v_mfma_f32_32x32x16_bf16 v[0:15], a[96:99], a[184:187], v[0:15]\n" \
  "ds_read_b64_tr_b16 a[80:81], v241 offset:4096\n" \
  "s_waitcnt vmcnt(28)\n" \
  "v_mfma_f32_32x32x16_bf16 v[16:31], a[96:99], a[188:191], v[16:31]\n" \
  "ds_read_b64_tr_b16 a[82:83], v241 offset:4352\n" \
  "s_waitcnt lgkmcnt(5)\n" \
  "v_mfma_f32_32x32x16_bf16 v[32:47], a[100:103], a[184:187], v[32:47]\n" \
  "ds_read_b64_tr_b16 a[84:85], v241 offset:4608\n" \
  "s_mov_b32 s42, 69632\n" \
  "v_mfma_f32_32x32x16_bf16 v[48:63], a[100:103], a[188:191], v[48:63]\n" \
  "ds_read_b64_tr_b16 a[86:87], v241 offset:4864\n" \
  "v_mfma_f32_32x32x16_bf16 v[64:79], a[104:107], a[184:187], v[64:79]\n" \
  "ds_read_b64_tr_b16 a[88:89], v241 offset:5120\n" \
  "v_mfma_f32_32x32x16_bf16 v[80:95], a[104:107], a[188:191], v[80:95]\n" \
  "ds_read_b64_tr_b16 a[90:91], v241 offset:5376\n" \
  "buffer_load_dwordx4 a[144:147], v232, %[srdH], s42 offen offset:0 sc1\n" \
  "v_mfma_f32_32x32x16_bf16 v[96:111], a[108:111], a[184:187], v[96:111]\n" \
  "ds_read_b64_tr_b16 a[92:93], v241 offset:5632\n" \
  "v_mfma_f32_32x32x16_bf16 v[112:127], a[108:111], a[188:191], v[112:127]\n" \
  "ds_read_b64_tr_b16 a[94:95], v241 offset:5888\n" \
  "buffer_load_dwordx4 a[148:151], v232, %[srdH], s42 offen offset:1024 sc1\n" \
  "s_waitcnt vmcnt(29)\n" \
  "v_mfma_f32_32x32x16_bf16 v[0:15], a[64:67], a[192:195], v[0:15]\n" \
  "ds_read_b64_tr_b16 a[96:97], v241 offset:8192\n" \
  "s_waitcnt vmcnt(28)\n" \
  "v_mfma_f32_32x32x16_bf16 v[16:31], a[64:67], a[196:199], v[16:31]\n" \
  "ds_read_b64_tr_b16 a[98:99], v241 offset:8448\n" \
  "v_mfma_f32_32x32x16_bf16 v[32:47], a[68:71], a[192:195], v[32:47]\n" \
  "ds_read_b64_tr_b16 a[100:101], v241 offset:8704\n" \
  "s_mov_b32 s42, 69632\n" \
  "v_mfma_f32_32x32x16_bf16 v[48:63], a[68:71], a[196:199], v[48:63]\n" \
  "ds_read_b64_tr_b16 a[102:103], v241 offset:8960\n" \
  "s_waitcnt lgkmcnt(5)\n" \
  "v_mfma_f32_32x32x16_bf16 v[64:79], a[72:75], a[192:195], v[64:79]\n" \
  "ds_read_b64_tr_b16 a[104:105], v241 offset:9216\n" \
  "v_mfma_f32_32x32x16_bf16 v[80:95], a[72:75], a[196:199], v[80:95]\n" \
  "ds_read_b64_tr_b16 a[106:107], v241 offset:9472\n" \
  "buffer_load_dwordx4 a[152:155], v232, %[srdH], s42 offen offset:2048 sc1\n" \
  "v_mfma_f32_32x32x16_bf16 v[96:111], a[76:79], a[192:195], v[96:111]\n" \
  "ds_read_b64_tr_b16 a[108:109], v241 offset:9728\n" \
  "v_mfma_f32_32x32x16_bf16 v[112:127], a[76:79], a[196:199], v[112:127]\n" \
  "ds_read_b64_tr_b16 a[110:111], v241 offset:9984\n" \
  "buffer_load_dwordx4 a[156:159], v232, %[srdH], s42 offen offset:3072 sc1\n" \
  "s_waitcnt vmcnt(28)\n" \
  "v_mfma_f32_32x32x16_bf16 v[0:15], a[80:83], a[200:203], v[0:15]\n" \
  "ds_read_b64_tr_b16 a[64:65], v241 offset:12288\n" \
  "s_waitcnt vmcnt(25)\n" \
  "v_mfma_f32_32x32x16_bf16 v[16:31], a[80:83], a[204:207], v[16:31]\n" \
  "ds_read_b64_tr_b16 a[66:67], v241 offset:12544\n" \
  "v_mfma_f32_32x32x16_bf16 v[32:47], a[84:87], a[200:203], v[32:47]\n" \
  "ds_read_b64_tr_b16 a[68:69], v241 offset:12800\n" \
  "s_mov_b32 s42, 73728\n" \
  "v_mfma_f32_32x32x16_bf16 v[48:63], a[84:87], a[204:207], v[48:63]\n" \
  "ds_read_b64_tr_b16 a[70:71], v241 offset:13056\n" \
  "v_mfma_f32_32x32x16_bf16 v[64:79], a[88:91], a[200:203], v[64:79]\n" \
  "ds_read_b64_tr_b16 a[72:73], v241 offset:13312\n" \
  "v_mfma_f32_32x32x16_bf16 v[80:95], a[88:91], a[204:207], v[80:95]\n" \
  "ds_read_b64_tr_b16 a[74:75], v241 offset:13568\n" \
  "buffer_load_dwordx4 a[160:163], v232, %[srdH], s42 offen offset:0 sc1\n" \
  "s_waitcnt lgkmcnt(5)\n" \
  "v_mfma_f32_32x32x16_bf16 v[96:111], a[92:95], a[200:203], v[96:111]\n" \
  "ds_read_b64_tr_b16 a[76:77], v241 offset:13824\n" \
  "v_mfma_f32_32x32x16_bf16 v[112:127], a[92:95], a[204:207], v[112:127]\n" \
  "ds_read_b64_tr_b16 a[78:79], v241 offset:14080\n" \
  "buffer_load_dwordx4 a[164:167], v232, %[srdH], s42 offen offset:1024 sc1\n" \
  "s_barrier\n" \
  "s_waitcnt vmcnt(25)\n" \
  "v_mfma_f32_32x32x16_bf16 v[0:15], a[96:99], a[208:211], v[0:15]\n" \
  "ds_read_b64_tr_b16 a[80:81], v241 offset:16384\n" \
  "s_waitcnt vmcnt(24)\n" \
  "v_mfma_f32_32x32x16_bf16 v[16:31], a[96:99], a[212:215], v[16:31]\n" \
  "ds_read_b64_tr_b16 a[82:83], v241 offset:16640\n" \
  "v_mfma_f32_32x32x16_bf16 v[32:47], a[100:103], a[208:211], v[32:47]\n" \
  "ds_read_b64_tr_b16 a[84:85], v241 offset:16896\n" \
  "s_mov_b32 s42, 73728\n" \
  "v_mfma_f32_32x32x16_bf16 v[48:63], a[100:103], a[212:215], v[48:63]\n" \
  "ds_read_b64_tr_b16 a[86:87], v241 offset:17152\n" \
  "v_mfma_f32_32x32x16_bf16 v[64:79], a[104:107], a[208:211], v[64:79]\n" \
  "ds_read_b64_tr_b16 a[88:89], v241 offset:17408\n" \
  "v_mfma_f32_32x32x16_bf16 v[80:95], a[104:107], a[212:215], v[80:95]\n" \
  "ds_read_b64_tr_b16 a[90:91], v241 offset:17664\n" \
  "buffer_load_dwordx4 a[168:171], v232, %[srdH], s42 offen offset:2048 sc1\n" \
  "v_mfma_f32_32x32x16_bf16 v[96:111], a[108:111], a[208:211], v[96:111]\n" \
  "ds_read_b64_tr_b16 a[92:93], v241 offset:17920\n" \
  "v_mfma_f32_32x32x16_bf16 v[112:127], a[108:111], a[212:215], v[112:127]\n" \
  "ds_read_b64_tr_b16 a[94:95], v241 offset:18176\n" \
  "buffer_load_dwordx4 a[172:175], v232, %[srdH], s42 offen offset:3072 sc1\n" \
  "s_waitcnt vmcnt(25) lgkmcnt(5)\n" \
  "v_mfma_f32_32x32x16_bf16 v[0:15], a[64:67], a[216:219], v[0:15]\n" \
  "ds_read_b64_tr_b16 a[96:97], v241 offset:20480\n" \
  "s_waitcnt vmcnt(24)\n" \
  "v_mfma_f32_32x32x16_bf16 v[16:31], a[64:67], a[220:223], v[16:31]\n" \
  "ds_read_b64_tr_b16 a[98:99], v241 offset:20736\n" \
  "v_mfma_f32_32x32x16_bf16 v[32:47], a[68:71], a[216:219], v[32:47]\n" \
  "ds_read_b64_tr_b16 a[100:101], v241 offset:20992\n" \
  "s_mov_b32 s42, 77824\n" \
  "v_mfma_f32_32x32x16_bf16 v[48:63], a[68:71], a[220:223], v[48:63]\n" \
  "ds_read_b64_tr_b16 a[102:103], v241 offset:21248\n" \
  "v_mfma_f32_32x32x16_bf16 v[64:79], a[72:75], a[216:219], v[64:79]\n" \
  "ds_read_b64_tr_b16 a[104:105], v241 offset:21504\n" \
  "v_mfma_f32_32x32x16_bf16 v[80:95], a[72:75], a[220:223], v[80:95]\n" \
  "ds_read_b64_tr_b16 a[106:107], v241 offset:21760\n" \
  "buffer_load_dwordx4 a[176:179], v232, %[srdH], s42 offen offset:0 sc1\n" \
  "v_mfma_f32_32x32x16_bf16 v[96:111], a[76:79], a[216:219], v[96:111]\n" \
  "ds_read_b64_tr_b16 a[108:109], v241 offset:22016\n" \
  "v_mfma_f32_32x32x16_bf16 v[112:127], a[76:79], a[220:223], v[112:127]\n" \
  "ds_read_b64_tr_b16 a[110:111], v241 offset:22272\n" \
  "buffer_load_dwordx4 a[180:183], v232, %[srdH], s42 offen offset:1024 sc1\n" \
  "s_waitcnt vmcnt(25)\n" \
  "v_mfma_f32_32x32x16_bf16 v[0:15], a[80:83], a[224:227], v[0:15]\n" \
  "ds_read_b64_tr_b16 a[64:65], v241 offset:24576\n" \
  "s_waitcnt vmcnt(24)\n" \
  "v_mfma_f32_32x32x16_bf16 v[16:31], a[80:83], a[228:231], v[16:31]\n" \
  "ds_read_b64_tr_b16 a[66:67], v241 offset:24832\n" \
  "s_waitcnt lgkmcnt(5)\n" \
  "v_mfma_f32_32x32x16_bf16 v[32:47], a[84:87], a[224:227], v[32:47]\n" \
  "ds_read_b64_tr_b16 a[68:69], v241 offset:25088\n" \
  "s_mov_b32 s42, 77824\n" \
  "v_mfma_f32_32x32x16_bf16 v[48:63], a[84:87], a[228:231], v[48:63]\n" \
  "ds_read_b64_tr_b16 a[70:71], v241 offset:25344\n" \
  "v_mfma_f32_32x32x16_bf16 v[64:79], a[88:91], a[224:227], v[64:79]\n" \
  "ds_read_b64_tr_b16 a[72:73], v241 offset:25600\n" \
  "v_mfma_f32_32x32x16_bf16 v[80:95], a[88:91], a[228:231], v[80:95]\n" \
  "ds_read_b64_tr_b16 a[74:75], v241 offset:25856\n" \
  "buffer_load_dwordx4 a[184:187], v232, %[srdH], s42 offen offset:2048 sc1\n" \
  "v_mfma_f32_32x32x16_bf16 v[96:111], a[92:95], a[224:227], v[96:111]\n" \
  "ds_read_b64_tr_b16 a[76:77], v241 offset:26112\n" \
  "v_mfma_f32_32x32x16_bf16 v[112:127], a[92:95], a[228:231], v[112:127]\n" \
  "ds_read_b64_tr_b16 a[78:79], v241 offset:26368\n" \
  "buffer_load_dwordx4 a[188:191], v232, %[srdH], s42 offen offset:3072 sc1\n" \
  "s_waitcnt vmcnt(24)\n" \
  "v_mfma_f32_32x32x16_bf16 v[0:15], a[96:99], a[232:235], v[0:15]\n" \
  "ds_read_b64_tr_b16 a[80:81], v241 offset:28672\n" \
  "s_waitcnt vmcnt(21)\n" \
  "v_mfma_f32_32x32x16_bf16 v[16:31], a[96:99], a[236:239], v[16:31]\n" \
  "ds_read_b64_tr_b16 a[82:83], v241 offset:28928\n" \
  "v_mfma_f32_32x32x16_bf16 v[32:47], a[100:103], a[232:235], v[32:47]\n" \
  "ds_read_b64_tr_b16 a[84:85], v241 offset:29184\n" \
  "v_mfma_f32_32x32x16_bf16 v[48:63], a[100:103], a[236:239], v[48:63]\n" \
  "ds_read_b64_tr_b16 a[86:87], v241 offset:29440\n" \
  "s_waitcnt lgkmcnt(5)\n" \
  "v_mfma_f32_32x32x16_bf16 v[64:79], a[104:107], a[232:235], v[64:79]\n" \
  "ds_read_b64_tr_b16 a[88:89], v241 offset:29696\n" \
  "v_mfma_f32_32x32x16_bf16 v[80:95], a[104:107], a[236:239], v[80:95]\n" \
  "ds_read_b64_tr_b16 a[90:91], v241 offset:29952\n" \
  "v_mfma_f32_32x32x16_bf16 v[96:111], a[108:111], a[232:235], v[96:111]\n" \
  "ds_read_b64_tr_b16 a[92:93], v241 offset:30208\n" \
  "v_mfma_f32_32x32x16_bf16 v[112:127], a[108:111], a[236:239], v[112:127]\n" \
  "ds_read_b64_tr_b16 a[94:95], v241 offset:30464\n" \
  "s_barrier\n" \
  "s_waitcnt vmcnt(19)\n" \
  "v_mfma_f32_32x32x16_bf16 v[0:15], a[64:67], a[240:243], v[0:15]\n" \
  "ds_read_b64_tr_b16 a[96:97], v241 offset:32768\n" \
  "s_waitcnt vmcnt(18)\n" \
  "v_mfma_f32_32x32x16_bf16 v[16:31], a[64:67], a[244:247], v[16:31]\n" \
  "ds_read_b64_tr_b16 a[98:99], v241 offset:33024\n" \
  "s_add_u32 m0, s60, 131072\n" \
  "v_mfma_f32_32x32x16_bf16 v[32:47], a[68:71], a[240:243], v[32:47]\n" \
  "ds_read_b64_tr_b16 a[100:101], v241 offset:33280\n" \
  "s_mov_b32 s40, 0\n" \
  "v_mfma_f32_32x32x16_bf16 v[48:63], a[68:71], a[244:247], v[48:63]\n" \
  "ds_read_b64_tr_b16 a[102:103], v241 offset:33536\n" \
  "buffer_load_dwordx4 v237, %[srdV], s40 offen offset:0 lds\n" \
  "v_mfma_f32_32x32x16_bf16 v[64:79], a[72:75], a[240:243], v[64:79]\n" \
  "ds_read_b64_tr_b16 a[104:105], v241 offset:33792\n" \
  "v_mfma_f32_32x32x16_bf16 v[80:95], a[72:75], a[244:247], v[80:95]\n" \
  "ds_read_b64_tr_b16 a[106:107], v241 offset:34048\n" \
  "buffer_load_dwordx4 v238, %[srdV], s40 offen offset:1024 lds\n" \
  "s_waitcnt lgkmcnt(5)\n" \
  "v_mfma_f32_32x32x16_bf16 v[96:111], a[76:79], a[240:243], v[96:111]\n" \
  "ds_read_b64_tr_b16 a[108:109], v241 offset:34304\n" \
  "buffer_load_dwordx4 v237, %[srdV], s40 offen offset:2048 lds\n" \
  "v_mfma_f32_32x32x16_bf16 v[112:127], a[76:79], a[244:247], v[112:127]\n" \
  "ds_read_b64_tr_b16 a[110:111], v241 offset:34560\n" \
  "buffer_load_dwordx4 v238, %[srdV], s40 offen offset:3072 lds\n" \
  "s_waitcnt vmcnt(21)\n" \
  "v_mfma_f32_32x32x16_bf16 v[0:15], a[80:83], a[248:251], v[0:15]\n" \
  "ds_read_b64_tr_b16 a[64:65], v241 offset:36864\n" \
  "s_waitcnt vmcnt(20)\n" \
  "v_mfma_f32_32x32x16_bf16 v[16:31], a[80:83], a[252:255], v[16:31]\n" \
  "ds_read_b64_tr_b16 a[66:67], v241 offset:37120\n" \
  "v_mfma_f32_32x32x16_bf16 v[32:47], a[84:87], a[248:251], v[32:47]\n" \
  "ds_read_b64_tr_b16 a[68:69], v241 offset:37376\n" \
  "v_mfma_f32_32x32x16_bf16 v[48:63], a[84:87], a[252:255], v[48:63]\n" \
  "ds_read_b64_tr_b16 a[70:71], v241 offset:37632\n" \
  "v_mfma_f32_32x32x16_bf16 v[64:79], a[88:91], a[248:251], v[64:79]\n" \
  "ds_read_b64_tr_b16 a[72:73], v241 offset:37888\n" \
  "v_mfma_f32_32x32x16_bf16 v[80:95], a[88:91], a[252:255], v[80:95]\n" \
  "ds_read_b64_tr_b16 a[74:75], v241 offset:38144\n" \
  "v_mfma_f32_32x32x16_bf16 v[96:111], a[92:95], a[248:251], v[96:111]\n" \
  "ds_read_b64_tr_b16 a[76:77], v241 offset:38400\n" \
  "v_mfma_f32_32x32x16_bf16 v[112:127], a[92:95], a[252:255], v[112:127]\n" \
  "ds_read_b64_tr_b16 a[78:79], v241 offset:38656\n" \
  "s_waitcnt vmcnt(19) lgkmcnt(5)\n" \
  "v_mfma_f32_32x32x16_bf16 v[0:15], a[96:99], a[128:131], v[0:15]\n" \
  "ds_read_b64_tr_b16 a[80:81], v241 offset:40960\n" \
  "s_waitcnt vmcnt(18)\n" \
  "v_mfma_f32_32x32x16_bf16 v[16:31], a[96:99], a[132:135], v[16:31]\n" \
  "ds_read_b64_tr_b16 a[82:83], v241 offset:41216\n" \
  "v_mfma_f32_32x32x16_bf16 v[32:47], a[100:103], a[128:131], v[32:47]\n" \
  "ds_read_b64_tr_b16 a[84:85], v241 offset:41472\n" \
  "v_mfma_f32_32x32x16_bf16 v[48:63], a[100:103], a[132:135], v[48:63]\n" \
  "ds_read_b64_tr_b16 a[86:87], v241 offset:41728\n" \
  "v_mfma_f32_32x32x16_bf16 v[64:79], a[104:107], a[128:131], v[64:79]\n" \
  "ds_read_b64_tr_b16 a[88:89], v241 offset:41984\n" \
  "v_mfma_f32_32x32x16_bf16 v[80:95], a[104:107], a[132:135], v[80:95]\n" \
  "ds_read_b64_tr_b16 a[90:91], v241 offset:42240\n" \
  "v_mfma_f32_32x32x16_bf16 v[96:111], a[108:111], a[128:131], v[96:111]\n" \
  "ds_read_b64_tr_b16 a[92:93], v241 offset:42496\n" \
  "v_mfma_f32_32x32x16_bf16 v[112:127], a[108:111], a[132:135], v[112:127]\n" \
  "ds_read_b64_tr_b16 a[94:95], v241 offset:42752\n" \
  "s_waitcnt vmcnt(17)\n" \
  "v_mfma_f32_32x32x16_bf16 v[0:15], a[64:67], a[136:139], v[0:15]\n" \
  "ds_read_b64_tr_b16 a[96:97], v241 offset:45056\n" \
  "s_waitcnt vmcnt(16)\n" \
  "v_mfma_f32_32x32x16_bf16 v[16:31], a[64:67], a[140:143], v[16:31]\n" \
  "ds_read_b64_tr_b16 a[98:99], v241 offset:45312\n" \
  "s_waitcnt lgkmcnt(5)\n" \
  "v_mfma_f32_32x32x16_bf16 v[32:47], a[68:71], a[136:139], v[32:47]\n" \
  "ds_read_b64_tr_b16 a[100:101], v241 offset:45568\n" \
  "v_mfma_f32_32x32x16_bf16 v[48:63], a[68:71], a[140:143], v[48:63]\n" \
  "ds_read_b64_tr_b16 a[102:103], v241 offset:45824\n" \
  "v_mfma_f32_32x32x16_bf16 v[64:79], a[72:75], a[136:139], v[64:79]\n" \
  "ds_read_b64_tr_b16 a[104:105], v241 offset:46080\n" \
  "v_mfma_f32_32x32x16_bf16 v[80:95], a[72:75], a[140:143], v[80:95]\n" \
  "ds_read_b64_tr_b16 a[106:107], v241 offset:46336\n" \
  "v_mfma_f32_32x32x16_bf16 v[96:111], a[76:79], a[136:139], v[96:111]\n" \
  "ds_read_b64_tr_b16 a[108:109], v241 offset:46592\n" \
  "v_mfma_f32_32x32x16_bf16 v[112:127], a[76:79], a[140:143], v[112:127]\n" \
  "ds_read_b64_tr_b16 a[110:111], v241 offset:46848\n" \
  "s_barrier\n" \
  "s_waitcnt vmcnt(15)\n" \
  "v_mfma_f32_32x32x16_bf16 v[0:15], a[80:83], a[144:147], v[0:15]\n" \
  "ds_read_b64_tr_b16 a[64:65], v241 offset:49152\n" \
  "s_add_u32 m0, s60, 32768\n" \
  "s_waitcnt vmcnt(14)\n" \
  "v_mfma_f32_32x32x16_bf16 v[16:31], a[80:83], a[148:151], v[16:31]\n" \
  "ds_read_b64_tr_b16 a[66:67], v241 offset:49408\n" \
  "s_mov_b32 s40, 32768\n" \
  "buffer_load_dwordx4 v233, %[srdK], s40 offen offset:0 lds\n" \
  "v_mfma_f32_32x32x16_bf16 v[32:47], a[84:87], a[144:147], v[32:47]\n" \
  "ds_read_b64_tr_b16 a[68:69], v241 offset:49664\n" \
  "buffer_load_dwordx4 v234, %[srdK], s40 offen offset:1024 lds\n" \
  "v_mfma_f32_32x32x16_bf16 v[48:63], a[84:87], a[148:151], v[48:63]\n" \
  "ds_read_b64_tr_b16 a[70:71], v241 offset:49920\n" \
  "buffer_load_dwordx4 v235, %[srdK], s40 offen offset:2048 lds\n" \
  "buffer_load_dwordx4 v236, %[srdK], s40 offen offset:3072 lds\n" \
  "s_waitcnt lgkmcnt(5)\n" \
  "v_mfma_f32_32x32x16_bf16 v[64:79], a[88:91], a[144:147], v[64:79]\n" \
  "ds_read_b64_tr_b16 a[72:73], v241 offset:50176\n" \
  "s_add_u32 m0, s60, 147456\n" \
  "v_mfma_f32_32x32x16_bf16 v[80:95], a[88:91], a[148:151], v[80:95]\n" \
  "ds_read_b64_tr_b16 a[74:75], v241 offset:50432\n" \
  "s_mov_b32 s40, 16384\n" \
  "buffer_load_dwordx4 v237, %[srdV], s40 offen offset:0 lds\n" \
  "v_mfma_f32_32x32x16_bf16 v[96:111], a[92:95], a[144:147], v[96:111]\n" \
  "ds_read_b64_tr_b16 a[76:77], v241 offset:50688\n" \
  "buffer_load_dwordx4 v238, %[srdV], s40 offen offset:1024 lds\n" \
  "v_mfma_f32_32x32x16_bf16 v[112:127], a[92:95], a[148:151], v[112:127]\n" \
  "ds_read_b64_tr_b16 a[78:79], v241 offset:50944\n" \
  "buffer_load_dwordx4 v237, %[srdV], s40 offen offset:2048 lds\n" \
  "buffer_load_dwordx4 v238, %[srdV], s40 offen offset:3072 lds\n" \
  "s_waitcnt vmcnt(21)\n" \
  "v_mfma_f32_32x32x16_bf16 v[0:15], a[96:99], a[152:155], v[0:15]\n" \
  "ds_read_b64_tr_b16 a[80:81], v241 offset:53248\n" \
  "s_waitcnt vmcnt(20)\n" \
  "v_mfma_f32_32x32x16_bf16 v[16:31], a[96:99], a[156:159], v[16:31]\n" \
  "ds_read_b64_tr_b16 a[82:83], v241 offset:53504\n" \
  "v_mfma_f32_32x32x16_bf16 v[32:47], a[100:103], a[152:155], v[32:47]\n" \
  "ds_read_b64_tr_b16 a[84:85], v241 offset:53760\n" \
  "v_mfma_f32_32x32x16_bf16 v[48:63], a[100:103], a[156:159], v[48:63]\n" \
  "ds_read_b64_tr_b16 a[86:87], v241 offset:54016\n" \
  "v_mfma_f32_32x32x16_bf16 v[64:79], a[104:107], a[152:155], v[64:79]\n" \
  "ds_read_b64_tr_b16 a[88:89], v241 offset:54272\n" \
  "v_mfma_f32_32x32x16_bf16 v[80:95], a[104:107], a[156:159], v[80:95]\n" \
  "ds_read_b64_tr_b16 a[90:91], v241 offset:54528\n" \
  "s_waitcnt lgkmcnt(5)\n" \
  "v_mfma_f32_32x32x16_bf16 v[96:111], a[108:111], a[152:155], v[96:111]\n" \
  "ds_read_b64_tr_b16 a[92:93], v241 offset:54784\n" \
  "v_mfma_f32_32x32x16_bf16 v[112:127], a[108:111], a[156:159], v[112:127]\n" \
  "ds_read_b64_tr_b16 a[94:95], v241 offset:55040\n" \
  "s_waitcnt vmcnt(19)\n" \
  "v_mfma_f32_32x32x16_bf16 v[0:15], a[64:67], a[160:163], v[0:15]\n" \
  "ds_read_b64_tr_b16 a[96:97], v241 offset:57344\n" \
  "s_waitcnt vmcnt(18)\n" \
  "v_mfma_f32_32x32x16_bf16 v[16:31], a[64:67], a[164:167], v[16:31]\n" \
  "ds_read_b64_tr_b16 a[98:99], v241 offset:57600\n" \
  "v_mfma_f32_32x32x16_bf16 v[32:47], a[68:71], a[160:163], v[32:47]\n" \
  "ds_read_b64_tr_b16 a[100:101], v241 offset:57856\n" \
  "v_mfma_f32_32x32x16_bf16 v[48:63], a[68:71], a[164:167], v[48:63]\n" \
  "ds_read_b64_tr_b16 a[102:103], v241 offset:58112\n" \
  "v_mfma_f32_32x32x16_bf16 v[64:79], a[72:75], a[160:163], v[64:79]\n" \
  "ds_read_b64_tr_b16 a[104:105], v241 offset:58368\n" \
  "v_mfma_f32_32x32x16_bf16 v[80:95], a[72:75], a[164:167], v[80:95]\n" \
  "ds_read_b64_tr_b16 a[106:107], v241 offset:58624\n" \
  "v_mfma_f32_32x32x16_bf16 v[96:111], a[76:79], a[160:163], v[96:111]\n" \
  "ds_read_b64_tr_b16 a[108:109], v241 offset:58880\n" \
  "v_mfma_f32_32x32x16_bf16 v[112:127], a[76:79], a[164:167], v[112:127]\n" \
  "ds_read_b64_tr_b16 a[110:111], v241 offset:59136\n" \
  "s_waitcnt vmcnt(17) lgkmcnt(5)\n" \
  "v_mfma_f32_32x32x16_bf16 v[0:15], a[80:83], a[168:171], v[0:15]\n" \
  "ds_read_b64_tr_b16 a[64:65], v241 offset:61440\n" \
  "s_waitcnt vmcnt(16)\n" \
  "v_mfma_f32_32x32x16_bf16 v[16:31], a[80:83], a[172:175], v[16:31]\n" \
  "ds_read_b64_tr_b16 a[66:67], v241 offset:61696\n" \
  "v_mfma_f32_32x32x16_bf16 v[32:47], a[84:87], a[168:171], v[32:47]\n" \
  "ds_read_b64_tr_b16 a[68:69], v241 offset:61952\n" \
  "v_mfma_f32_32x32x16_bf16 v[48:63], a[84:87], a[172:175], v[48:63]\n" \
  "ds_read_b64_tr_b16 a[70:71], v241 offset:62208\n" \
  "v_mfma_f32_32x32x16_bf16 v[64:79], a[88:91], a[168:171], v[64:79]\n" \
  "ds_read_b64_tr_b16 a[72:73], v241 offset:62464\n" \
  "v_mfma_f32_32x32x16_bf16 v[80:95], a[88:91], a[172:175], v[80:95]\n" \
  "ds_read_b64_tr_b16 a[74:75], v241 offset:62720\n" \
  "v_mfma_f32_32x32x16_bf16 v[96:111], a[92:95], a[168:171], v[96:111]\n" \
  "ds_read_b64_tr_b16 a[76:77], v241 offset:62976\n" \
  "v_mfma_f32_32x32x16_bf16 v[112:127], a[92:95], a[172:175], v[112:127]\n" \
  "ds_read_b64_tr_b16 a[78:79], v241 offset:63232\n" \
  "s_waitcnt vmcnt(15)\n" \
  "v_mfma_f32_32x32x16_bf16 v[0:15], a[96:99], a[176:179], v[0:15]\n" \
  "ds_read_b128 a[192:195], v245 offset:0\n" \
  "s_waitcnt vmcnt(14)\n" \
  "v_mfma_f32_32x32x16_bf16 v[16:31], a[96:99], a[180:183], v[16:31]\n" \
  "ds_read_b128 a[196:199], v246 offset:0\n" \
  "s_waitcnt lgkmcnt(5)\n" \
  "v_mfma_f32_32x32x16_bf16 v[32:47], a[100:103], a[176:179], v[32:47]\n" \
  "ds_read_b128 a[200:203], v247 offset:0\n" \
  "v_mfma_f32_32x32x16_bf16 v[48:63], a[100:103], a[180:183], v[48:63]\n" \
  "ds_read_b128 a[204:207], v248 offset:0\n" \
  "v_mfma_f32_32x32x16_bf16 v[64:79], a[104:107], a[176:179], v[64:79]\n" \
  "ds_read_b128 a[208:211], v249 offset:0\n" \
  "v_mfma_f32_32x32x16_bf16 v[80:95], a[104:107], a[180:183], v[80:95]\n" \
  "ds_read_b128 a[212:215], v250 offset:0\n" \
  "v_mfma_f32_32x32x16_bf16 v[96:111], a[108:111], a[176:179], v[96:111]\n" \
  "ds_read_b128 a[216:219], v251 offset:0\n" \
  "v_mfma_f32_32x32x16_bf16 v[112:127], a[108:111], a[180:183], v[112:127]\n" \
  "ds_read_b128 a[220:223], v252 offset:0\n" \
  "s_waitcnt vmcnt(13)\n" \
  "v_mfma_f32_32x32x16_bf16 v[0:15], a[64:67], a[184:187], v[0:15]\n" \
  "v_mfma_f32_32x32x16_bf16 v[32:47], a[68:71], a[184:187], v[32:47]\n" \
  "s_waitcnt lgkmcnt(3)\n" \
  "v_mfma_f32_32x32x16_bf16 v[64:79], a[72:75], a[184:187], v[64:79]\n" \
  "v_mfma_f32_32x32x16_bf16 v[96:111], a[76:79], a[184:187], v[96:111]\n" \
  "s_waitcnt vmcnt(12)\n" \
  "v_mfma_f32_32x32x16_bf16 v[16:31], a[64:67], a[188:191], v[16:31]\n" \
  "v_mfma_f32_32x32x16_bf16 v[48:63], a[68:71], a[188:191], v[48:63]\n" \
  "s_nop 4\n" \
  "v_cvt_pk_bf16_f32 v160, v0, v1\n" \
  "v_cvt_pk_bf16_f32 v161, v2, v3\n" \
  "v_cvt_pk_bf16_f32 v162, v4, v5\n" \
  "v_cvt_pk_bf16_f32 v163, v6, v7\n" \
  "v_accvgpr_write_b32 a0, v160\n" \
  "v_accvgpr_write_b32 a1, v161\n" \
  "v_accvgpr_write_b32 a2, v162\n" \
  "v_accvgpr_write_b32 a3, v163\n" \
  "v_cvt_pk_bf16_f32 v164, v8, v9\n" \
  "v_cvt_pk_bf16_f32 v165, v10, v11\n" \
  "v_cvt_pk_bf16_f32 v166, v12, v13\n" \
  "v_cvt_pk_bf16_f32 v167, v14, v15\n" \
  "v_accvgpr_write_b32 a4, v164\n" \
  "v_accvgpr_write_b32 a5, v165\n" \
  "v_accvgpr_write_b32 a6, v166\n" \
  "v_accvgpr_write_b32 a7, v167\n" \
  "v_cvt_pk_bf16_f32 v168, v32, v33\n" \
  "v_cvt_pk_bf16_f32 v169, v34, v35\n" \
  "v_cvt_pk_bf16_f32 v170, v36, v37\n" \
  "v_cvt_pk_bf16_f32 v171, v38, v39\n" \
  "v_accvgpr_write_b32 a8, v168\n" \
  "v_mfma_f32_32x32x16_bf16 v[80:95], a[72:75], a[188:191], v[80:95]\n" \
  "v_accvgpr_write_b32 a9, v169\n" \
  "v_accvgpr_write_b32 a10, v170\n" \
  "v_accvgpr_write_b32 a11, v171\n" \
  "v_cvt_pk_bf16_f32 v172, v40, v41\n" \
  "v_cvt_pk_bf16_f32 v173, v42, v43\n" \
  "v_cvt_pk_bf16_f32 v174, v44, v45\n" \
  "v_cvt_pk_bf16_f32 v175, v46, v47\n" \
  "v_accvgpr_write_b32 a12, v172\n" \
  "v_accvgpr_write_b32 a13, v173\n" \
  "v_accvgpr_write_b32 a14, v174\n" \
  "v_accvgpr_write_b32 a15, v175\n" \
  "v_cvt_pk_bf16_f32 v176, v64, v65\n" \
  "v_cvt_pk_bf16_f32 v177, v66, v67\n" \
  "v_cvt_pk_bf16_f32 v178, v68, v69\n" \
  "v_cvt_pk_bf16_f32 v179, v70, v71\n" \
  "v_accvgpr_write_b32 a16, v176\n" \
  "v_accvgpr_write_b32 a17, v177\n" \
  "v_accvgpr_write_b32 a18, v178\n" \
  "v_accvgpr_write_b32 a19, v179\n" \
  "v_cvt_pk_bf16_f32 v180, v72, v73\n" \
  "v_cvt_pk_bf16_f32 v181, v74, v75\n" \
  "v_mfma_f32_32x32x16_bf16 v[112:127], a[76:79], a[188:191], v[112:127]\n" \
  "v_cvt_pk_bf16_f32 v182, v76, v77\n" \
  "v_cvt_pk_bf16_f32 v183, v78, v79\n" \
  "v_accvgpr_write_b32 a20, v180\n" \
  "v_accvgpr_write_b32 a21, v181\n" \
  "v_accvgpr_write_b32 a22, v182\n" \
  "v_accvgpr_write_b32 a23, v183\n" \
  "v_cvt_pk_bf16_f32 v184, v96, v97\n" \
  "v_cvt_pk_bf16_f32 v185, v98, v99\n" \
  "v_cvt_pk_bf16_f32 v186, v100, v101\n" \
  "v_cvt_pk_bf16_f32 v187, v102, v103\n" \
  "v_accvgpr_write_b32 a24, v184\n" \
  "v_accvgpr_write_b32 a25, v185\n" \
  "v_accvgpr_write_b32 a26, v186\n" \
  "v_accvgpr_write_b32 a27, v187\n" \
  "v_cvt_pk_bf16_f32 v188, v104, v105\n" \
  "v_cvt_pk_bf16_f32 v189, v106, v107\n" \
  "v_cvt_pk_bf16_f32 v190, v108, v109\n" \
  "v_cvt_pk_bf16_f32 v191, v110, v111\n" \
  "v_accvgpr_write_b32 a28, v188\n" \
  "v_accvgpr_write_b32 a29, v189\n" \
  "v_accvgpr_write_b32 a30, v190\n" \
  "v_accvgpr_write_b32 a31, v191\n" \
  "v_mfma_f32_32x32x16_bf16 v[128:143], a[192:195], a[0:3], 0\n" \
  "v_cvt_pk_bf16_f32 v160, v16, v17\n" \
  "v_cvt_pk_bf16_f32 v161, v18, v19\n" \
  "v_cvt_pk_bf16_f32 v162, v20, v21\n" \
  "v_cvt_pk_bf16_f32 v163, v22, v23\n" \
  "v_accvgpr_write_b32 a32, v160\n" \
  "v_accvgpr_write_b32 a33, v161\n" \
  "v_accvgpr_write_b32 a34, v162\n" \
  "v_accvgpr_write_b32 a35, v163\n" \
  "v_mfma_f32_32x32x16_bf16 v[128:143], a[196:199], a[4:7], v[128:143]\n" \
  "v_cvt_pk_bf16_f32 v164, v24, v25\n" \
  "v_cvt_pk_bf16_f32 v165, v26, v27\n" \
  "v_cvt_pk_bf16_f32 v166, v28, v29\n" \
  "v_cvt_pk_bf16_f32 v167, v30, v31\n" \
  "v_accvgpr_write_b32 a36, v164\n" \
  "v_accvgpr_write_b32 a37, v165\n" \
  "v_accvgpr_write_b32 a38, v166\n" \
  "v_accvgpr_write_b32 a39, v167\n" \
  "v_mfma_f32_32x32x16_bf16 v[128:143], a[200:203], a[8:11], v[128:143]\n" \
  "v_cvt_pk_bf16_f32 v168, v48, v49\n" \
  "v_cvt_pk_bf16_f32 v169, v50, v51\n" \
  "v_cvt_pk_bf16_f32 v170, v52, v53\n" \
  "v_cvt_pk_bf16_f32 v171, v54, v55\n" \
  "v_accvgpr_write_b32 a40, v168\n" \
  "v_accvgpr_write_b32 a41, v169\n" \
  "v_accvgpr_write_b32 a42, v170\n" \
  "v_accvgpr_write_b32 a43, v171\n" \
  "v_mfma_f32_32x32x16_bf16 v[128:143], a[204:207], a[12:15], v[128:143]\n" \
  "v_cvt_pk_bf16_f32 v172, v56, v57\n" \
  "v_cvt_pk_bf16_f32 v173, v58, v59\n" \
  "v_cvt_pk_bf16_f32 v174, v60, v61\n" \
  "v_cvt_pk_bf16_f32 v175, v62, v63\n" \
  "v_accvgpr_write_b32 a44, v172\n" \
  "v_accvgpr_write_b32 a45, v173\n" \
  "v_accvgpr_write_b32 a46, v174\n" \
  "v_accvgpr_write_b32 a47, v175\n" \
  "v_mfma_f32_32x32x16_bf16 v[128:143], a[208:211], a[16:19], v[128:143]\n" \
  "v_cvt_pk_bf16_f32 v176, v80, v81\n" \
  "v_cvt_pk_bf16_f32 v177, v82, v83\n" \
  "v_cvt_pk_bf16_f32 v178, v84, v85\n" \
  "v_cvt_pk_bf16_f32 v179, v86, v87\n" \
  "v_accvgpr_write_b32 a48, v176\n" \
  "v_accvgpr_write_b32 a49, v177\n" \
  "v_accvgpr_write_b32 a50, v178\n" \
  "v_accvgpr_write_b32 a51, v179\n" \
  "s_waitcnt lgkmcnt(0)\n" \
  "v_mfma_f32_32x32x16_bf16 v[128:143], a[212:215], a[20:23], v[128:143]\n" \
  "v_cvt_pk_bf16_f32 v180, v88, v89\n" \
  "v_cvt_pk_bf16_f32 v181, v90, v91\n" \
  "v_cvt_pk_bf16_f32 v182, v92, v93\n" \
  "v_cvt_pk_bf16_f32 v183, v94, v95\n" \
  "v_accvgpr_write_b32 a52, v180\n" \
  "v_accvgpr_write_b32 a53, v181\n" \
  "v_accvgpr_write_b32 a54, v182\n" \
  "v_accvgpr_write_b32 a55, v183\n" \
  "v_mfma_f32_32x32x16_bf16 v[128:143], a[216:219], a[24:27], v[128:143]\n" \
  "v_cvt_pk_bf16_f32 v184, v112, v113\n" \
  "v_cvt_pk_bf16_f32 v185, v114, v115\n" \
  "v_cvt_pk_bf16_f32 v186, v116, v117\n" \
  "v_cvt_pk_bf16_f32 v187, v118, v119\n" \
  "v_accvgpr_write_b32 a56, v184\n" \
  "v_accvgpr_write_b32 a57, v185\n" \
  "v_accvgpr_write_b32 a58, v186\n" \
  "v_accvgpr_write_b32 a59, v187\n" \
  "v_mfma_f32_32x32x16_bf16 v[128:143], a[220:223], a[28:31], v[128:143]\n" \
  "v_cvt_pk_bf16_f32 v188, v120, v121\n" \
  "v_cvt_pk_bf16_f32 v189, v122, v123\n" \
  "v_cvt_pk_bf16_f32 v190, v124, v125\n" \
  "v_cvt_pk_bf16_f32 v191, v126, v127\n" \
  "v_accvgpr_write_b32 a60, v188\n" \
  "v_accvgpr_write_b32 a61, v189\n" \
  "v_accvgpr_write_b32 a62, v190\n" \
  "v_accvgpr_write_b32 a63, v191\n" \
  "s_waitcnt vmcnt(8)\n" \
  "s_barrier\n" \
  "v_mfma_f32_32x32x16_bf16 v[144:159], a[192:195], a[32:35], 0\n" \
  "ds_read_b128 a[224:227], v245 offset:8192\n" \
  "ds_read_b128 a[228:231], v246 offset:8192\n" \
  "ds_read_b128 a[232:235], v247 offset:8192\n" \
  "v_mfma_f32_32x32x16_bf16 v[144:159], a[196:199], a[36:39], v[144:159]\n" \
  "ds_read_b128 a[236:239], v248 offset:8192\n" \
  "ds_read_b128 a[240:243], v249 offset:8192\n" \
  "ds_read_b128 a[244:247], v250 offset:8192\n" \
  "v_mfma_f32_32x32x16_bf16 v[144:159], a[200:203], a[40:43], v[144:159]\n" \
  "ds_read_b128 a[248:251], v251 offset:8192\n" \
  "ds_read_b128 a[252:255], v252 offset:8192\n" \
  "ds_read_b64_tr_b16 a[64:65], v244 offset:0\n" \
  "v_max3_f32 v228, v128, v129, v130\n" \
  "v_max3_f32 v229, v131, v132, v133\n" \
  "v_max3_f32 v228, v228, v134, v135\n" \
  "v_max3_f32 v229, v229, v136, v137\n" \
  "v_mfma_f32_32x32x16_bf16 v[144:159], a[204:207], a[44:47], v[144:159]\n" \
  "ds_read_b64_tr_b16 a[66:67], v244 offset:2048\n" \
  "ds_read_b64_tr_b16 a[68:69], v244 offset:512\n" \
  "ds_read_b64_tr_b16 a[70:71], v244 offset:2560\n" \
  "v_max3_f32 v228, v228, v138, v139\n" \
  "v_max3_f32 v229, v229, v140, v141\n" \
  "v_max3_f32 v228, v228, v142, v143\n" \
  "v_max_f32 v228, v228, v229\n" \
  "v_mov_b32 v229, v228\n" \
  "v_mfma_f32_32x32x16_bf16 v[144:159], a[208:211], a[48:51], v[144:159]\n" \
  "ds_read_b64_tr_b16 a[72:73], v244 offset:1024\n" \
  "ds_read_b64_tr_b16 a[74:75], v244 offset:3072\n" \
  "ds_read_b64_tr_b16 a[76:77], v244 offset:1536\n" \
  "v_permlane32_swap_b32 v228, v229\n" \
  "v_max_f32 v228, v228, v229\n" \
  "v_sub_f32 v192, 0xc2700000, v228\n" \
  "v_mov_b32 v193, v192\n" \
  "v_mfma_f32_32x32x16_bf16 v[144:159], a[212:215], a[52:55], v[144:159]\n" \
  "ds_read_b64_tr_b16 a[78:79], v244 offset:3584\n" \
  "ds_read_b64_tr_b16 a[80:81], v244 offset:4096\n" \
  "ds_read_b64_tr_b16 a[82:83], v244 offset:6144\n" \
  "v_mov_b32 v194, v192\n" \
  "v_mov_b32 v195, v192\n" \
  "v_mov_b32 v196, v192\n" \
  "v_mov_b32 v197, v192\n" \
  "v_mov_b32 v198, v192\n" \
  "v_mfma_f32_32x32x16_bf16 v[144:159], a[216:219], a[56:59], v[144:159]\n" \
  "ds_read_b64_tr_b16 a[84:85], v244 offset:4608\n" \
  "ds_read_b64_tr_b16 a[86:87], v244 offset:6656\n" \
  "ds_read_b64_tr_b16 a[88:89], v244 offset:5120\n" \
  "v_mov_b32 v199, v192\n" \
  "v_mov_b32 v200, v192\n" \
  "v_mov_b32 v201, v192\n" \
  "v_mov_b32 v202, v192\n" \
  "v_mfma_f32_32x32x16_bf16 v[144:159], a[220:223], a[60:63], v[144:159]\n" \
  "ds_read_b64_tr_b16 a[90:91], v244 offset:7168\n" \
  "ds_read_b64_tr_b16 a[92:93], v244 offset:5632\n" \
  "ds_read_b64_tr_b16 a[94:95], v244 offset:7680\n" \
  "v_mov_b32 v203, v192\n" \
  "v_mov_b32 v204, v192\n" \
  "v_mov_b32 v205, v192\n" \
  "v_mov_b32 v206, v192\n" \
  "v_mov_b32 v207, v192\n" \
  "s_waitcnt lgkmcnt(15)\n" \
  "v_mfma_f32_32x32x16_bf16 v[160:175], a[224:227], a[0:3], 0\n" \
  "v_add_f32 v128, v128, v192\n" \
  "v_add_f32 v129, v129, v192\n" \
  "v_add_f32 v130, v130, v192\n" \
  "v_mfma_f32_32x32x16_bf16 v[160:175], a[228:231], a[4:7], v[160:175]\n" \
  "ds_read_b64_tr_b16 a[96:97], v244 offset:8192\n" \
  "v_add_f32 v131, v131, v192\n" \
  "v_exp_f32 v128, v128\n" \
  "v_exp_f32 v129, v129\n" \
  "v_add_f32 v132, v132, v192\n" \
  "v_mfma_f32_32x32x16_bf16 v[160:175], a[232:235], a[8:11], v[160:175]\n" \
  "ds_read_b64_tr_b16 a[98:99], v244 offset:10240\n" \
  "v_add_f32 v133, v133, v192\n" \
  "v_max3_f32 v230, v144, v145, v146\n" \
  "v_exp_f32 v130, v130\n" \
  "v_max3_f32 v231, v147, v148, v149\n" \
  "v_mov_b32 v224, v128\n" \
  "v_max3_f32 v230, v230, v150, v151\n" \
  "v_mfma_f32_32x32x16_bf16 v[160:175], a[236:239], a[12:15], v[160:175]\n" \
  "ds_read_b64_tr_b16 a[100:101], v244 offset:8704\n" \
  "v_exp_f32 v131, v131\n" \
  "v_max3_f32 v231, v231, v152, v153\n" \
  "v_mov_b32 v225, v129\n" \
  "v_max3_f32 v230, v230, v154, v155\n" \
  "v_cvt_pk_bf16_f32 v128, v128, v129\n" \
  "v_max3_f32 v231, v231, v156, v157\n" \
  "v_add_f32 v134, v134, v192\n" \
  "v_max3_f32 v230, v230, v158, v159\n" \
  "v_mfma_f32_32x32x16_bf16 v[160:175], a[240:243], a[16:19], v[160:175]\n" \
  "ds_read_b64_tr_b16 a[102:103], v244 offset:10752\n" \
  "v_add_f32 v135, v135, v192\n" \
  "v_max_f32 v230, v230, v231\n" \
  "v_exp_f32 v132, v132\n" \
  "v_mov_b32 v231, v230\n" \
  "v_add_f32 v224, v224, v130\n" \
  "s_nop 0\n" \
  "v_permlane32_swap_b32 v230, v231\n" \
  "v_max_f32 v230, v230, v231\n" \
  "v_mfma_f32_32x32x16_bf16 v[160:175], a[244:247], a[20:23], v[160:175]\n" \
  "ds_read_b64_tr_b16 a[104:105], v244 offset:9216\n" \
  "v_exp_f32 v133, v133\n" \
  "v_sub_f32 v208, 0xc2700000, v230\n" \
  "v_add_f32 v225, v225, v131\n" \
  "v_mov_b32 v209, v208\n" \
  "v_cvt_pk_bf16_f32 v129, v130, v131\n" \
  "v_mov_b32 v210, v208\n" \
  "v_add_f32 v136, v136, v192\n" \
  "v_mov_b32 v211, v208\n" \
  "v_mfma_f32_32x32x16_bf16 v[160:175], a[248:251], a[24:27], v[160:175]\n" \
  "ds_read_b64_tr_b16 a[106:107], v244 offset:11264\n" \
  "v_add_f32 v137, v137, v192\n" \
  "v_mov_b32 v212, v208\n" \
  "v_exp_f32 v134, v134\n" \
  "v_mov_b32 v213, v208\n" \
  "v_add_f32 v224, v224, v132\n" \
  "v_mov_b32 v214, v208\n" \
  "v_mfma_f32_32x32x16_bf16 v[160:175], a[252:255], a[28:31], v[160:175]\n" \
  "v_exp_f32 v135, v135\n" \
  "v_mov_b32 v215, v208\n" \
  "v_add_f32 v225, v225, v133\n" \
  "v_mov_b32 v216, v208\n" \
  "v_cvt_pk_bf16_f32 v130, v132, v133\n" \
  "v_mov_b32 v217, v208\n" \
  "v_add_f32 v138, v138, v192\n" \
  "v_mov_b32 v218, v208\n" \
  "v_mfma_f32_32x32x16_bf16 v[176:191], a[224:227], a[32:35], 0\n" \
  "ds_read_b64_tr_b16 a[108:109], v244 offset:9728\n" \
  "v_add_f32 v139, v139, v192\n" \
  "v_mov_b32 v219, v208\n" \
  "v_exp_f32 v136, v136\n" \
  "v_mov_b32 v220, v208\n" \
  "v_add_f32 v224, v224, v134\n" \
  "v_mov_b32 v221, v208\n" \
  "v_mov_b32 v222, v208\n" \
  "v_mfma_f32_32x32x16_bf16 v[176:191], a[228:231], a[36:39], v[176:191]\n" \
  "s_add_u32 m0, s60, 49152\n" \
  "ds_read_b64_tr_b16 a[110:111], v244 offset:11776\n" \
  "v_exp_f32 v137, v137\n" \
  "v_mov_b32 v223, v208\n" \
  "v_add_f32 v225, v225, v135\n" \
  "v_add_f32 v144, v144, v208\n" \
  "v_cvt_pk_bf16_f32 v131, v134, v135\n" \
  "v_add_f32 v145, v145, v208\n" \
  "v_add_f32 v140, v140, v192\n" \
  "v_add_f32 v146, v146, v208\n" \
  "v_mfma_f32_32x32x16_bf16 v[176:191], a[232:235], a[40:43], v[176:191]\n" \
  "s_mov_b32 s40, 49152\n" \
  "ds_read_b64_tr_b16 a[112:113], v244 offset:12288\n" \
  "v_add_f32 v141, v141, v192\n" \
  "v_add_f32 v147, v147, v208\n" \
  "v_exp_f32 v138, v138\n" \
  "v_exp_f32 v144, v144\n" \
  "v_add_f32 v224, v224, v136\n" \
  "v_exp_f32 v145, v145\n" \
  "v_mfma_f32_32x32x16_bf16 v[176:191], a[236:239], a[44:47], v[176:191]\n" \
  "buffer_load_dwordx4 v233, %[srdK], s40 offen offset:0 lds\n" \
  "ds_read_b64_tr_b16 a[114:115], v244 offset:14336\n" \
  "v_exp_f32 v139, v139\n" \
  "v_add_f32 v148, v148, v208\n" \
  "v_add_f32 v225, v225, v137\n" \
  "v_add_f32 v149, v149, v208\n" \
  "v_cvt_pk_bf16_f32 v136, v136, v137\n" \
  "v_exp_f32 v146, v146\n" \
  "v_add_f32 v142, v142, v192\n" \
  "v_mov_b32 v226, v144\n" \
  "v_mfma_f32_32x32x16_bf16 v[176:191], a[240:243], a[48:51], v[176:191]\n" \
  "ds_read_b64_tr_b16 a[116:117], v244 offset:12800\n" \
  "v_add_f32 v143, v143, v192\n" \
  "v_exp_f32 v147, v147\n" \
  "v_exp_f32 v140, v140\n" \
  "v_mov_b32 v227, v145\n" \
  "v_add_f32 v224, v224, v138\n" \
  "v_cvt_pk_bf16_f32 v144, v144, v145\n" \
  "v_add_f32 v150, v150, v208\n" \
  "v_mfma_f32_32x32x16_bf16 v[176:191], a[244:247], a[52:55], v[176:191]\n" \
  "buffer_load_dwordx4 v234, %[srdK], s40 offen offset:1024 lds\n" \
  "ds_read_b64_tr_b16 a[118:119], v244 offset:14848\n" \
  "v_exp_f32 v141, v141\n" \
  "v_add_f32 v151, v151, v208\n" \
  "v_add_f32 v225, v225, v139\n" \
  "v_exp_f32 v148, v148\n" \
  "v_cvt_pk_bf16_f32 v137, v138, v139\n" \
  "v_add_f32 v226, v226, v146\n" \
  "v_exp_f32 v142, v142\n" \
  "v_exp_f32 v149, v149\n" \
  "v_mfma_f32_32x32x16_bf16 v[176:191], a[248:251], a[56:59], v[176:191]\n" \
  "buffer_load_dwordx4 v235, %[srdK], s40 offen offset:2048 lds\n" \
  "v_add_f32 v224, v224, v140\n" \
  "v_add_f32 v227, v227, v147\n" \
  "v_exp_f32 v143, v143\n" \
  "v_cvt_pk_bf16_f32 v145, v146, v147\n" \
  "v_add_f32 v225, v225, v141\n" \
  "v_add_f32 v152, v152, v208\n" \
  "v_add_f32 v153, v153, v208\n" \
  "v_mfma_f32_32x32x16_bf16 v[176:191], a[252:255], a[60:63], v[176:191]\n" \
  "buffer_load_dwordx4 v236, %[srdK], s40 offen offset:3072 lds\n" \
  "ds_read_b64_tr_b16 a[120:121], v244 offset:13312\n" \
  "v_cvt_pk_bf16_f32 v138, v140, v141\n" \
  "v_exp_f32 v150, v150\n" \
  "v_add_f32 v224, v224, v142\n" \
  "v_add_f32 v226, v226, v148\n" \
  "v_add_f32 v225, v225, v143\n" \
  "v_exp_f32 v151, v151\n" \
  "v_cvt_pk_bf16_f32 v139, v142, v143\n" \
  "s_waitcnt lgkmcnt(4)\n" \
  "v_mfma_f32_32x32x16_bf16 v[0:15], a[64:67], v[128:131], 0\n" \
  "ds_read_b64_tr_b16 a[122:123], v244 offset:15360\n" \
  "v_add_f32 v227, v227, v149\n" \
  "v_add_f32 v160, v160, v192\n" \
  "v_cvt_pk_bf16_f32 v146, v148, v149\n" \
  "v_add_f32 v161, v161, v192\n" \
  "v_add_f32 v154, v154, v208\n" \
  "v_add_f32 v162, v162, v192\n" \
  "v_add_f32 v155, v155, v208\n" \
  "v_mfma_f32_32x32x16_bf16 v[32:47], a[68:71], v[128:131], 0\n" \
  "s_add_u32 m0, s60, 65536\n" \
  "ds_read_b64_tr_b16 a[124:125], v244 offset:13824\n" \
  "v_exp_f32 v152, v152\n" \
  "v_add_f32 v163, v163, v192\n" \
  "v_add_f32 v226, v226, v150\n" \
  "v_exp_f32 v160, v160\n" \
  "v_exp_f32 v153, v153\n" \
  "v_exp_f32 v161, v161\n" \
  "v_add_f32 v227, v227, v151\n" \
  "v_add_f32 v164, v164, v192\n" \
  "v_mfma_f32_32x32x16_bf16 v[64:79], a[72:75], v[128:131], 0\n" \
  "s_mov_b32 s40, 32768\n" \
  "ds_read_b64_tr_b16 a[126:127], v244 offset:15872\n" \
  "v_cvt_pk_bf16_f32 v147, v150, v151\n" \
  "v_add_f32 v165, v165, v192\n" \
  "v_add_f32 v156, v156, v208\n" \
  "v_exp_f32 v162, v162\n" \
  "v_add_f32 v157, v157, v208\n" \
  "v_add_f32 v224, v224, v160\n" \
  "v_exp_f32 v154, v154\n" \
  "v_mfma_f32_32x32x16_bf16 v[96:111], a[76:79], v[128:131], 0\n" \
  "buffer_load_dwordx4 v237, %[srdV], s40 offen offset:0 lds\n" \
  "ds_read_b128 a[192:195], v245 offset:16384\n" \
  "v_add_f32 v226, v226, v152\n" \
  "v_exp_f32 v163, v163\n" \
  "v_exp_f32 v155, v155\n" \
  "v_add_f32 v225, v225, v161\n" \
  "v_add_f32 v227, v227, v153\n" \
  "v_cvt_pk_bf16_f32 v160, v160, v161\n" \
  "v_add_f32 v166, v166, v192\n" \
  "v_mfma_f32_32x32x16_bf16 v[0:15], a[80:83], v[136:139], v[0:15]\n" \
  "ds_read_b128 a[196:199], v246 offset:16384\n" \
  "v_cvt_pk_bf16_f32 v152, v152, v153\n" \
  "v_add_f32 v167, v167, v192\n" \
  "v_add_f32 v158, v158, v208\n" \
  "v_exp_f32 v164, v164\n" \
  "v_add_f32 v159, v159, v208\n" \
  "v_add_f32 v224, v224, v162\n" \
  "v_exp_f32 v156, v156\n" \
  "v_mfma_f32_32x32x16_bf16 v[32:47], a[84:87], v[136:139], v[32:47]\n" \
  "buffer_load_dwordx4 v238, %[srdV], s40 offen offset:1024 lds\n" \
  "v_add_f32 v226, v226, v154\n" \
  "v_exp_f32 v165, v165\n" \
  "v_exp_f32 v157, v157\n" \
  "v_add_f32 v225, v225, v163\n" \
  "v_add_f32 v227, v227, v155\n" \
  "v_cvt_pk_bf16_f32 v161, v162, v163\n" \
  "v_cvt_pk_bf16_f32 v153, v154, v155\n" \
  "v_add_f32 v168, v168, v192\n" \
  "v_mfma_f32_32x32x16_bf16 v[64:79], a[88:91], v[136:139], v[64:79]\n" \
  "buffer_load_dwordx4 v237, %[srdV], s40 offen offset:2048 lds\n" \
  "ds_read_b128 a[200:203], v247 offset:16384\n" \
  "v_exp_f32 v158, v158\n" \
  "v_add_f32 v169, v169, v192\n" \
  "v_add_f32 v226, v226, v156\n" \
  "v_exp_f32 v166, v166\n" \
  "v_exp_f32 v159, v159\n" \
  "v_add_f32 v224, v224, v164\n" \
  "v_add_f32 v227, v227, v157\n" \
  "v_mfma_f32_32x32x16_bf16 v[96:111], a[92:95], v[136:139], v[96:111]\n" \
  "buffer_load_dwordx4 v238, %[srdV], s40 offen offset:3072 lds\n" \
  "ds_read_b128 a[204:207], v248 offset:16384\n" \
  "v_cvt_pk_bf16_f32 v154, v156, v157\n" \
  "v_exp_f32 v167, v167\n" \
  "v_add_f32 v226, v226, v158\n" \
  "v_add_f32 v225, v225, v165\n" \
  "v_add_f32 v227, v227, v159\n" \
  "v_cvt_pk_bf16_f32 v162, v164, v165\n" \
  "v_cvt_pk_bf16_f32 v155, v158, v159\n" \
  "v_add_f32 v170, v170, v192\n" \
  "v_mfma_f32_32x32x16_bf16 v[16:31], a[64:67], v[144:147], 0\n" \
  "ds_read_b128 a[208:211], v249 offset:16384\n" \
  "v_add_f32 v171, v171, v192\n" \
  "v_exp_f32 v168, v168\n" \
  "v_add_f32 v224, v224, v166\n" \
  "v_mfma_f32_32x32x16_bf16 v[48:63], a[68:71], v[144:147], 0\n" \
  "ds_read_b128 a[212:215], v250 offset:16384\n" \
  "v_exp_f32 v169, v169\n" \
  "v_add_f32 v225, v225, v167\n" \
  "v_cvt_pk_bf16_f32 v163, v166, v167\n" \
  "v_add_f32 v172, v172, v192\n" \
  "v_mfma_f32_32x32x16_bf16 v[80:95], a[72:75], v[144:147], 0\n" \
  "ds_read_b128 a[216:219], v251 offset:16384\n" \
  "v_add_f32 v173, v173, v192\n" \
  "v_exp_f32 v170, v170\n" \
  "v_add_f32 v224, v224, v168\n" \
  "v_mfma_f32_32x32x16_bf16 v[112:127], a[76:79], v[144:147], 0\n" \
  "ds_read_b128 a[220:223], v252 offset:16384\n" \
  "v_exp_f32 v171, v171\n" \
  "v_add_f32 v225, v225, v169\n" \
  "v_cvt_pk_bf16_f32 v168, v168, v169\n" \
  "v_add_f32 v174, v174, v192\n" \
  "v_mfma_f32_32x32x16_bf16 v[16:31], a[80:83], v[152:155], v[16:31]\n" \
  "v_add_f32 v175, v175, v192\n" \
  "v_exp_f32 v172, v172\n" \
  "v_add_f32 v224, v224, v170\n" \
  "v_mfma_f32_32x32x16_bf16 v[48:63], a[84:87], v[152:155], v[48:63]\n" \
  "v_exp_f32 v173, v173\n" \
  "v_add_f32 v225, v225, v171\n" \
  "v_cvt_pk_bf16_f32 v169, v170, v171\n" \
  "v_exp_f32 v174, v174\n" \
  "v_mfma_f32_32x32x16_bf16 v[80:95], a[88:91], v[152:155], v[80:95]\n" \
  "v_add_f32 v224, v224, v172\n" \
  "v_exp_f32 v175, v175\n" \
  "v_add_f32 v225, v225, v173\n" \
  "v_mfma_f32_32x32x16_bf16 v[112:127], a[92:95], v[152:155], v[112:127]\n" \
  "v_cvt_pk_bf16_f32 v170, v172, v173\n" \
  "v_add_f32 v224, v224, v174\n" \
  "v_add_f32 v225, v225, v175\n" \
  "v_cvt_pk_bf16_f32 v171, v174, v175\n" \
  "global_load_dwordx4 a[188:191], v232, %[frel] sc1\n" \
  "s_waitcnt lgkmcnt(0)\n" \
  "s_waitcnt vmcnt(9)\n" \
  "s_barrier\n" \
  "v_mfma_f32_32x32x16_bf16 v[128:143], a[192:195], a[0:3], v[192:207]\n" \
  "v_add_f32 v176, v176, v208\n" \
  "v_add_f32 v177, v177, v208\n" \
  "v_add_f32 v178, v178, v208\n" \
  "v_mfma_f32_32x32x16_bf16 v[128:143], a[196:199], a[4:7], v[128:143]\n" \
  "s_add_u32 m0, s60, 0\n" \
  "ds_read_b64_tr_b16 a[64:65], v244 offset:16384\n" \
  "v_add_f32 v179, v179, v208\n" \
  "v_exp_f32 v176, v176\n" \
  "v_exp_f32 v177, v177\n" \
  "v_add_f32 v180, v180, v208\n" \
  "v_mfma_f32_32x32x16_bf16 v[128:143], a[200:203], a[8:11], v[128:143]\n" \
  "ds_read_b64_tr_b16 a[66:67], v244 offset:18432\n" \
  "v_add_f32 v181, v181, v208\n" \
  "v_exp_f32 v178, v178\n" \
  "v_add_f32 v226, v226, v176\n" \
  "v_mfma_f32_32x32x16_bf16 v[128:143], a[204:207], a[12:15], v[128:143]\n" \
  "s_mov_b32 s40, 65536\n" \
  "ds_read_b64_tr_b16 a[68:69], v244 offset:16896\n" \
  "v_exp_f32 v179, v179\n" \
  "v_add_f32 v227, v227, v177\n" \
  "v_cvt_pk_bf16_f32 v176, v176, v177\n" \
  "v_add_f32 v182, v182, v208\n" \
  "v_mfma_f32_32x32x16_bf16 v[128:143], a[208:211], a[16:19], v[128:143]\n" \
  "ds_read_b64_tr_b16 a[70:71], v244 offset:18944\n" \
  "v_add_f32 v183, v183, v208\n" \
  "v_exp_f32 v180, v180\n" \
  "v_add_f32 v226, v226, v178\n" \
  "v_mfma_f32_32x32x16_bf16 v[128:143], a[212:215], a[20:23], v[128:143]\n" \
  "buffer_load_dwordx4 v233, %[srdK], s40 offen offset:0 lds\n" \
  "ds_read_b64_tr_b16 a[72:73], v244 offset:17408\n" \
  "v_exp_f32 v181, v181\n" \
  "v_add_f32 v227, v227, v179\n" \
  "v_cvt_pk_bf16_f32 v177, v178, v179\n" \
  "v_add_f32 v184, v184, v208\n" \
  "v_mfma_f32_32x32x16_bf16 v[128:143], a[216:219], a[24:27], v[128:143]\n" \
  "ds_read_b64_tr_b16 a[74:75], v244 offset:19456\n" \
  "v_add_f32 v185, v185, v208\n" \
  "v_exp_f32 v182, v182\n" \
  "v_add_f32 v226, v226, v180\n" \
  "v_mfma_f32_32x32x16_bf16 v[128:143], a[220:223], a[28:31], v[128:143]\n" \
  "buffer_load_dwordx4 v234, %[srdK], s40 offen offset:1024 lds\n" \
  "v_exp_f32 v183, v183\n" \
  "v_add_f32 v227, v227, v181\n" \
  "v_cvt_pk_bf16_f32 v178, v180, v181\n" \
  "v_add_f32 v186, v186, v208\n" \
  "v_mfma_f32_32x32x16_bf16 v[144:159], a[192:195], a[32:35], v[208:223]\n" \
  "ds_read_b64_tr_b16 a[76:77], v244 offset:17920\n" \
  "v_add_f32 v187, v187, v208\n" \
  "v_exp_f32 v184, v184\n" \
  "v_add_f32 v226, v226, v182\n" \
  "v_mfma_f32_32x32x16_bf16 v[144:159], a[196:199], a[36:39], v[144:159]\n" \
  "buffer_load_dwordx4 v235, %[srdK], s40 offen offset:2048 lds\n" \
  "ds_read_b64_tr_b16 a[78:79], v244 offset:19968\n" \
  "v_exp_f32 v185, v185\n" \
  "v_add_f32 v227, v227, v183\n" \
  "v_cvt_pk_bf16_f32 v179, v182, v183\n" \
  "v_add_f32 v188, v188, v208\n" \
  "v_mfma_f32_32x32x16_bf16 v[144:159], a[200:203], a[40:43], v[144:159]\n" \
  "ds_read_b64_tr_b16 a[80:81], v244 offset:20480\n" \
  "v_add_f32 v189, v189, v208\n" \
  "v_exp_f32 v186, v186\n" \
  "v_add_f32 v226, v226, v184\n" \
  "v_mfma_f32_32x32x16_bf16 v[144:159], a[204:207], a[44:47], v[144:159]\n" \
  "buffer_load_dwordx4 v236, %[srdK], s40 offen offset:3072 lds\n" \
  "ds_read_b64_tr_b16 a[82:83], v244 offset:22528\n" \
  "v_exp_f32 v187, v187\n" \
  "v_add_f32 v227, v227, v185\n" \
  "v_cvt_pk_bf16_f32 v184, v184, v185\n" \
  "v_add_f32 v190, v190, v208\n" \
  "v_mfma_f32_32x32x16_bf16 v[144:159], a[208:211], a[48:51], v[144:159]\n" \
  "ds_read_b64_tr_b16 a[84:85], v244 offset:20992\n" \
  "v_add_f32 v191, v191, v208\n" \
  "v_exp_f32 v188, v188\n" \
  "v_add_f32 v226, v226, v186\n" \
  "v_mfma_f32_32x32x16_bf16 v[144:159], a[212:215], a[52:55], v[144:159]\n" \
  "s_add_u32 m0, s60, 81920\n" \
  "ds_read_b64_tr_b16 a[86:87], v244 offset:23040\n" \
  "v_exp_f32 v189, v189\n" \
  "v_add_f32 v227, v227, v187\n" \
  "v_cvt_pk_bf16_f32 v185, v186, v187\n" \
  "v_exp_f32 v190, v190\n" \
  "v_mfma_f32_32x32x16_bf16 v[144:159], a[216:219], a[56:59], v[144:159]\n" \
  "v_add_f32 v226, v226, v188\n" \
  "v_exp_f32 v191, v191\n" \
  "v_add_f32 v227, v227, v189\n" \
  "v_mfma_f32_32x32x16_bf16 v[144:159], a[220:223], a[60:63], v[144:159]\n" \
  "s_mov_b32 s40, 49152\n" \
  "ds_read_b64_tr_b16 a[88:89], v244 offset:21504\n" \
  "v_cvt_pk_bf16_f32 v186, v188, v189\n" \
  "v_add_f32 v226, v226, v190\n" \
  "v_add_f32 v227, v227, v191\n" \
  "v_cvt_pk_bf16_f32 v187, v190, v191\n" \
  "v_mfma_f32_32x32x16_bf16 v[0:15], a[96:99], v[160:163], v[0:15]\n" \
  "ds_read_b64_tr_b16 a[90:91], v244 offset:23552\n" \
  "v_exp_f32 v128, v128\n" \
  "v_exp_f32 v129, v129\n" \
  "v_mfma_f32_32x32x16_bf16 v[32:47], a[100:103], v[160:163], v[32:47]\n" \
  "buffer_load_dwordx4 v237, %[srdV], s40 offen offset:0 lds\n" \
  "ds_read_b64_tr_b16 a[92:93], v244 offset:22016\n" \
  "v_exp_f32 v130, v130\n" \
  "v_add_f32 v224, v224, v128\n" \
  "v_exp_f32 v131, v131\n" \
  "v_mfma_f32_32x32x16_bf16 v[64:79], a[104:107], v[160:163], v[64:79]\n" \
  "ds_read_b64_tr_b16 a[94:95], v244 offset:24064\n" \
  "v_add_f32 v225, v225, v129\n" \
  "v_cvt_pk_bf16_f32 v128, v128, v129\n" \
  "v_mfma_f32_32x32x16_bf16 v[96:111], a[108:111], v[160:163], v[96:111]\n" \
  "buffer_load_dwordx4 v238, %[srdV], s40 offen offset:1024 lds\n" \
  "ds_read_b128 a[224:227], v245 offset:24576\n" \
  "v_exp_f32 v132, v132\n" \
  "v_add_f32 v224, v224, v130\n" \
  "v_exp_f32 v133, v133\n" \
  "v_mfma_f32_32x32x16_bf16 v[0:15], a[112:115], v[168:171], v[0:15]\n" \
  "ds_read_b128 a[228:231], v246 offset:24576\n" \
  "v_add_f32 v225, v225, v131\n" \
  "v_cvt_pk_bf16_f32 v129, v130, v131\n" \
  "v_mfma_f32_32x32x16_bf16 v[32:47], a[116:119], v[168:171], v[32:47]\n" \
  "buffer_load_dwordx4 v237, %[srdV], s40 offen offset:2048 lds\n" \
  "v_exp_f32 v134, v134\n" \
  "v_add_f32 v224, v224, v132\n" \
  "v_exp_f32 v135, v135\n" \
  "v_mfma_f32_32x32x16_bf16 v[64:79], a[120:123], v[168:171], v[64:79]\n" \
  "ds_read_b128 a[232:235], v247 offset:24576\n" \
  "v_add_f32 v225, v225, v133\n" \
  "v_cvt_pk_bf16_f32 v130, v132, v133\n" \
  "v_mfma_f32_32x32x16_bf16 v[96:111], a[124:127], v[168:171], v[96:111]\n" \
  "buffer_load_dwordx4 v238, %[srdV], s40 offen offset:3072 lds\n" \
  "ds_read_b128 a[236:239], v248 offset:24576\n" \
  "v_exp_f32 v136, v136\n" \
  "v_add_f32 v224, v224, v134\n" \
  "v_exp_f32 v137, v137\n" \
  "v_mfma_f32_32x32x16_bf16 v[16:31], a[96:99], v[176:179], v[16:31]\n" \
  "ds_read_b128 a[240:243], v249 offset:24576\n" \
  "v_add_f32 v225, v225, v135\n" \
  "v_cvt_pk_bf16_f32 v131, v134, v135\n" \
  "v_mfma_f32_32x32x16_bf16 v[48:63], a[100:103], v[176:179], v[48:63]\n" \
  "ds_read_b128 a[244:247], v250 offset:24576\n" \
  "v_exp_f32 v138, v138\n" \
  "v_add_f32 v224, v224, v136\n" \
  "v_exp_f32 v139, v139\n" \
  "v_mfma_f32_32x32x16_bf16 v[80:95], a[104:107], v[176:179], v[80:95]\n" \
  "ds_read_b128 a[248:251], v251 offset:24576\n" \
  "v_add_f32 v225, v225, v137\n" \
  "v_cvt_pk_bf16_f32 v136, v136, v137\n" \
  "v_mfma_f32_32x32x16_bf16 v[112:127], a[108:111], v[176:179], v[112:127]\n" \
  "ds_read_b128 a[252:255], v252 offset:24576\n" \
  "v_exp_f32 v140, v140\n" \
  "v_add_f32 v224, v224, v138\n" \
  "v_exp_f32 v141, v141\n" \
  "v_mfma_f32_32x32x16_bf16 v[16:31], a[112:115], v[184:187], v[16:31]\n" \
  "v_add_f32 v225, v225, v139\n" \
  "v_cvt_pk_bf16_f32 v137, v138, v139\n" \
  "v_mfma_f32_32x32x16_bf16 v[48:63], a[116:119], v[184:187], v[48:63]\n" \
  "v_exp_f32 v142, v142\n" \
  "v_add_f32 v224, v224, v140\n" \
  "v_exp_f32 v143, v143\n" \
  "v_mfma_f32_32x32x16_bf16 v[80:95], a[120:123], v[184:187], v[80:95]\n" \
  "v_add_f32 v225, v225, v141\n" \
  "v_cvt_pk_bf16_f32 v138, v140, v141\n" \
  "v_mfma_f32_32x32x16_bf16 v[112:127], a[124:127], v[184:187], v[112:127]\n" \
  "v_add_f32 v224, v224, v142\n" \
  "v_add_f32 v225, v225, v143\n" \
  "v_cvt_pk_bf16_f32 v139, v142, v143\n" \
  "s_waitcnt lgkmcnt(1)\n" \
  "v_mfma_f32_32x32x16_bf16 v[160:175], a[224:227], a[0:3], v[192:207]\n" \
  "v_exp_f32 v144, v144\n" \
  "v_exp_f32 v145, v145\n" \
  "v_mfma_f32_32x32x16_bf16 v[160:175], a[228:231], a[4:7], v[160:175]\n" \
  "ds_read_b64_tr_b16 a[96:97], v244 offset:24576\n" \
  "v_exp_f32 v146, v146\n" \
  "v_add_f32 v226, v226, v144\n" \
  "v_exp_f32 v147, v147\n" \
  "v_mfma_f32_32x32x16_bf16 v[160:175], a[232:235], a[8:11], v[160:175]\n" \
  "ds_read_b64_tr_b16 a[98:99], v244 offset:26624\n" \
  "v_add_f32 v227, v227, v145\n" \
  "v_cvt_pk_bf16_f32 v144, v144, v145\n" \
  "v_mfma_f32_32x32x16_bf16 v[160:175], a[236:239], a[12:15], v[160:175]\n" \
  "ds_read_b64_tr_b16 a[100:101], v244 offset:25088\n" \
  "v_exp_f32 v148, v148\n" \
  "v_add_f32 v226, v226, v146\n" \
  "v_exp_f32 v149, v149\n" \
  "v_mfma_f32_32x32x16_bf16 v[160:175], a[240:243], a[16:19], v[160:175]\n" \
  "ds_read_b64_tr_b16 a[102:103], v244 offset:27136\n" \
  "v_add_f32 v227, v227, v147\n" \
  "v_cvt_pk_bf16_f32 v145, v146, v147\n" \
  "v_mfma_f32_32x32x16_bf16 v[160:175], a[244:247], a[20:23], v[160:175]\n" \
  "ds_read_b64_tr_b16 a[104:105], v244 offset:25600\n" \
  "v_exp_f32 v150, v150\n" \
  "v_add_f32 v226, v226, v148\n" \
  "v_exp_f32 v151, v151\n" \
  "v_mfma_f32_32x32x16_bf16 v[160:175], a[248:251], a[24:27], v[160:175]\n" \
  "ds_read_b64_tr_b16 a[106:107], v244 offset:27648\n" \
  "v_add_f32 v227, v227, v149\n" \
  "v_cvt_pk_bf16_f32 v146, v148, v149\n" \
  "s_waitcnt lgkmcnt(5)\n" \
  "v_mfma_f32_32x32x16_bf16 v[160:175], a[252:255], a[28:31], v[160:175]\n" \
  "v_exp_f32 v152, v152\n" \
  "v_add_f32 v226, v226, v150\n" \
  "v_exp_f32 v153, v153\n" \
  "v_mfma_f32_32x32x16_bf16 v[176:191], a[224:227], a[32:35], v[208:223]\n" \
  "ds_read_b64_tr_b16 a[108:109], v244 offset:26112\n" \
  "v_add_f32 v227, v227, v151\n" \
  "v_cvt_pk_bf16_f32 v147, v150, v151\n" \
  "v_mfma_f32_32x32x16_bf16 v[176:191], a[228:231], a[36:39], v[176:191]\n" \
  "ds_read_b64_tr_b16 a[110:111], v244 offset:28160\n" \
  "v_exp_f32 v154, v154\n" \
  "v_add_f32 v226, v226, v152\n" \
  "v_exp_f32 v155, v155\n" \
  "v_mfma_f32_32x32x16_bf16 v[176:191], a[232:235], a[40:43], v[176:191]\n" \
  "ds_read_b64_tr_b16 a[112:113], v244 offset:28672\n" \
  "v_add_f32 v227, v227, v153\n" \
  "v_cvt_pk_bf16_f32 v152, v152, v153\n" \
  "v_mfma_f32_32x32x16_bf16 v[176:191], a[236:239], a[44:47], v[176:191]\n" \
  "ds_read_b64_tr_b16 a[114:115], v244 offset:30720\n" \
  "v_exp_f32 v156, v156\n" \
  "v_add_f32 v226, v226, v154\n" \
  "v_exp_f32 v157, v157\n" \
  "v_mfma_f32_32x32x16_bf16 v[176:191], a[240:243], a[48:51], v[176:191]\n" \
  "ds_read_b64_tr_b16 a[116:117], v244 offset:29184\n" \
  "v_add_f32 v227, v227, v155\n" \
  "v_cvt_pk_bf16_f32 v153, v154, v155\n" \
  "v_mfma_f32_32x32x16_bf16 v[176:191], a[244:247], a[52:55], v[176:191]\n" \
  "ds_read_b64_tr_b16 a[118:119], v244 offset:31232\n" \
  "v_exp_f32 v158, v158\n" \
  "v_add_f32 v226, v226, v156\n" \
  "v_exp_f32 v159, v159\n" \
  "v_mfma_f32_32x32x16_bf16 v[176:191], a[248:251], a[56:59], v[176:191]\n" \
  "v_add_f32 v227, v227, v157\n" \
  "v_cvt_pk_bf16_f32 v154, v156, v157\n" \
  "v_mfma_f32_32x32x16_bf16 v[176:191], a[252:255], a[60:63], v[176:191]\n" \
  "ds_read_b64_tr_b16 a[120:121], v244 offset:29696\n" \
  "v_add_f32 v226, v226, v158\n" \
  "v_add_f32 v227, v227, v159\n" \
  "v_cvt_pk_bf16_f32 v155, v158, v159\n" \
  "v_mfma_f32_32x32x16_bf16 v[0:15], a[64:67], v[128:131], v[0:15]\n" \
  "ds_read_b64_tr_b16 a[122:123], v244 offset:31744\n" \
  "v_exp_f32 v160, v160\n" \
  "v_exp_f32 v161, v161\n" \
  "v_mfma_f32_32x32x16_bf16 v[32:47], a[68:71], v[128:131], v[32:47]\n" \
  "ds_read_b64_tr_b16 a[124:125], v244 offset:30208\n" \
  "v_exp_f32 v162, v162\n" \
  "v_add_f32 v224, v224, v160\n" \
  "v_exp_f32 v163, v163\n" \
  "v_mfma_f32_32x32x16_bf16 v[64:79], a[72:75], v[128:131], v[64:79]\n" \
  "ds_read_b64_tr_b16 a[126:127], v244 offset:32256\n" \
  "v_add_f32 v225, v225, v161\n" \
  "v_cvt_pk_bf16_f32 v160, v160, v161\n" \
  "v_mfma_f32_32x32x16_bf16 v[96:111], a[76:79], v[128:131], v[96:111]\n" \
  "ds_read_b128 a[192:195], v245 offset:32768\n" \
  "v_exp_f32 v164, v164\n" \
  "v_add_f32 v224, v224, v162\n" \
  "v_exp_f32 v165, v165\n" \
  "v_mfma_f32_32x32x16_bf16 v[0:15], a[80:83], v[136:139], v[0:15]\n" \
  "ds_read_b128 a[196:199], v246 offset:32768\n" \
  "v_add_f32 v225, v225, v163\n" \
  "v_cvt_pk_bf16_f32 v161, v162, v163\n" \
  "v_mfma_f32_32x32x16_bf16 v[32:47], a[84:87], v[136:139], v[32:47]\n" \
  "v_exp_f32 v166, v166\n" \
  "v_add_f32 v224, v224, v164\n" \
  "v_exp_f32 v167, v167\n" \
  "v_mfma_f32_32x32x16_bf16 v[64:79], a[88:91], v[136:139], v[64:79]\n" \
  "ds_read_b128 a[200:203], v247 offset:32768\n" \
  "v_add_f32 v225, v225, v165\n" \
  "v_cvt_pk_bf16_f32 v162, v164, v165\n" \
  "v_mfma_f32_32x32x16_bf16 v[96:111], a[92:95], v[136:139], v[96:111]\n" \
  "ds_read_b128 a[204:207], v248 offset:32768\n" \
  "v_exp_f32 v168, v168\n" \
  "v_add_f32 v224, v224, v166\n" \
  "v_exp_f32 v169, v169\n" \
  "v_mfma_f32_32x32x16_bf16 v[16:31], a[64:67], v[144:147], v[16:31]\n" \
  "ds_read_b128 a[208:211], v249 offset:32768\n" \
  "v_add_f32 v225, v225, v167\n" \
  "v_cvt_pk_bf16_f32 v163, v166, v167\n" \
  "v_mfma_f32_32x32x16_bf16 v[48:63], a[68:71], v[144:147], v[48:63]\n" \
  "ds_read_b128 a[212:215], v250 offset:32768\n" \
  "v_exp_f32 v170, v170\n" \
  "v_add_f32 v224, v224, v168\n" \
  "v_exp_f32 v171, v171\n" \
  "v_mfma_f32_32x32x16_bf16 v[80:95], a[72:75], v[144:147], v[80:95]\n" \
  "ds_read_b128 a[216:219], v251 offset:32768\n" \
  "v_add_f32 v225, v225, v169\n" \
  "v_cvt_pk_bf16_f32 v168, v168, v169\n" \
  "v_mfma_f32_32x32x16_bf16 v[112:127], a[76:79], v[144:147], v[112:127]\n" \
  "ds_read_b128 a[220:223], v252 offset:32768\n" \
  "v_exp_f32 v172, v172\n" \
  "v_add_f32 v224, v224, v170\n" \
  "v_exp_f32 v173, v173\n" \
  "v_mfma_f32_32x32x16_bf16 v[16:31], a[80:83], v[152:155], v[16:31]\n" \
  "v_add_f32 v225, v225, v171\n" \
  "v_cvt_pk_bf16_f32 v169, v170, v171\n" \
  "v_mfma_f32_32x32x16_bf16 v[48:63], a[84:87], v[152:155], v[48:63]\n" \
  "v_exp_f32 v174, v174\n" \
  "v_add_f32 v224, v224, v172\n" \
  "v_exp_f32 v175, v175\n" \
  "v_mfma_f32_32x32x16_bf16 v[80:95], a[88:91], v[152:155], v[80:95]\n" \
  "v_add_f32 v225, v225, v173\n" \
  "v_cvt_pk_bf16_f32 v170, v172, v173\n" \
  "v_mfma_f32_32x32x16_bf16 v[112:127], a[92:95], v[152:155], v[112:127]\n" \
  "v_add_f32 v224, v224, v174\n" \
  "v_add_f32 v225, v225, v175\n" \
  "v_cvt_pk_bf16_f32 v171, v174, v175\n" \
  "global_load_dwordx4 a[184:187], v232, %[fmod] sc1\n" \
  "s_waitcnt lgkmcnt(0)\n" \
  "s_waitcnt vmcnt(10)\n" \
  "s_barrier\n" \
  "v_mfma_f32_32x32x16_bf16 v[128:143], a[192:195], a[0:3], v[192:207]\n" \
  "v_exp_f32 v176, v176\n" \
  "v_exp_f32 v177, v177\n" \
  "v_mfma_f32_32x32x16_bf16 v[128:143], a[196:199], a[4:7], v[128:143]\n" \
  "s_add_u32 m0, s60, 16384\n" \
  "ds_read_b64_tr_b16 a[64:65], v243 offset:0\n" \
  "v_exp_f32 v178, v178\n" \
  "v_add_f32 v226, v226, v176\n" \
  "v_exp_f32 v179, v179\n" \
  "v_mfma_f32_32x32x16_bf16 v[128:143], a[200:203], a[8:11], v[128:143]\n" \
  "ds_read_b64_tr_b16 a[66:67], v243 offset:2048\n" \
  "v_add_f32 v227, v227, v177\n" \
  "v_cvt_pk_bf16_f32 v176, v176, v177\n" \
  "v_mfma_f32_32x32x16_bf16 v[128:143], a[204:207], a[12:15], v[128:143]\n" \
  "s_mov_b32 s40, 81920\n" \
  "ds_read_b64_tr_b16 a[68:69], v243 offset:512\n" \
  "v_exp_f32 v180, v180\n" \
  "v_add_f32 v226, v226, v178\n" \
  "v_exp_f32 v181, v181\n" \
  "v_mfma_f32_32x32x16_bf16 v[128:143], a[208:211], a[16:19], v[128:143]\n" \
  "ds_read_b64_tr_b16 a[70:71], v243 offset:2560\n" \
  "v_add_f32 v227, v227, v179\n" \
  "v_cvt_pk_bf16_f32 v177, v178, v179\n" \
  "v_mfma_f32_32x32x16_bf16 v[128:143], a[212:215], a[20:23], v[128:143]\n" \
  "buffer_load_dwordx4 v233, %[srdK], s40 offen offset:0 lds\n" \
  "ds_read_b64_tr_b16 a[72:73], v243 offset:1024\n" \
  "v_exp_f32 v182, v182\n" \
  "v_add_f32 v226, v226, v180\n" \
  "v_exp_f32 v183, v183\n" \
  "v_mfma_f32_32x32x16_bf16 v[128:143], a[216:219], a[24:27], v[128:143]\n" \
  "ds_read_b64_tr_b16 a[74:75], v243 offset:3072\n" \
  "v_add_f32 v227, v227, v181\n" \
  "v_cvt_pk_bf16_f32 v178, v180, v181\n" \
  "v_mfma_f32_32x32x16_bf16 v[128:143], a[220:223], a[28:31], v[128:143]\n" \
  "buffer_load_dwordx4 v234, %[srdK], s40 offen offset:1024 lds\n" \
  "v_exp_f32 v184, v184\n" \
  "v_add_f32 v226, v226, v182\n" \
  "v_exp_f32 v185, v185\n" \
  "v_mfma_f32_32x32x16_bf16 v[144:159], a[192:195], a[32:35], v[208:223]\n" \
  "ds_read_b64_tr_b16 a[76:77], v243 offset:1536\n" \
  "v_add_f32 v227, v227, v183\n" \
  "v_cvt_pk_bf16_f32 v179, v182, v183\n" \
  "v_mfma_f32_32x32x16_bf16 v[144:159], a[196:199], a[36:39], v[144:159]\n" \
  "buffer_load_dwordx4 v235, %[srdK], s40 offen offset:2048 lds\n" \
  "ds_read_b64_tr_b16 a[78:79], v243 offset:3584\n" \
  "v_exp_f32 v186, v186\n" \
  "v_add_f32 v226, v226, v184\n" \
  "v_exp_f32 v187, v187\n" \
  "v_mfma_f32_32x32x16_bf16 v[144:159], a[200:203], a[40:43], v[144:159]\n" \
  "ds_read_b64_tr_b16 a[80:81], v243 offset:4096\n" \
  "v_add_f32 v227, v227, v185\n" \
  "v_cvt_pk_bf16_f32 v184, v184, v185\n" \
  "v_mfma_f32_32x32x16_bf16 v[144:159], a[204:207], a[44:47], v[144:159]\n" \
  "buffer_load_dwordx4 v236, %[srdK], s40 offen offset:3072 lds\n" \
  "ds_read_b64_tr_b16 a[82:83], v243 offset:6144\n" \
  "v_exp_f32 v188, v188\n" \
  "v_add_f32 v226, v226, v186\n" \
  "v_exp_f32 v189, v189\n" \
  "v_mfma_f32_32x32x16_bf16 v[144:159], a[208:211], a[48:51], v[144:159]\n" \
  "ds_read_b64_tr_b16 a[84:85], v243 offset:4608\n" \
  "v_add_f32 v227, v227, v187\n" \
  "v_cvt_pk_bf16_f32 v185, v186, v187\n" \
  "v_mfma_f32_32x32x16_bf16 v[144:159], a[212:215], a[52:55], v[144:159]\n" \
  "s_add_u32 m0, s60, 98304\n" \
  "ds_read_b64_tr_b16 a[86:87], v243 offset:6656\n" \
  "v_exp_f32 v190, v190\n" \
  "v_add_f32 v226, v226, v188\n" \
  "v_exp_f32 v191, v191\n" \
  "v_mfma_f32_32x32x16_bf16 v[144:159], a[216:219], a[56:59], v[144:159]\n" \
  "v_add_f32 v227, v227, v189\n" \
  "v_cvt_pk_bf16_f32 v186, v188, v189\n" \
  "v_mfma_f32_32x32x16_bf16 v[144:159], a[220:223], a[60:63], v[144:159]\n" \
  "s_mov_b32 s40, 65536\n" \
  "ds_read_b64_tr_b16 a[88:89], v243 offset:5120\n" \
  "v_add_f32 v226, v226, v190\n" \
  "v_add_f32 v227, v227, v191\n" \
  "v_cvt_pk_bf16_f32 v187, v190, v191\n" \
  "v_mfma_f32_32x32x16_bf16 v[0:15], a[96:99], v[160:163], v[0:15]\n" \
  "ds_read_b64_tr_b16 a[90:91], v243 offset:7168\n" \
  "v_exp_f32 v128, v128\n" \
  "v_exp_f32 v129, v129\n" \
  "v_mfma_f32_32x32x16_bf16 v[32:47], a[100:103], v[160:163], v[32:47]\n" \
  "buffer_load_dwordx4 v237, %[srdV], s40 offen offset:0 lds\n" \
  "ds_read_b64_tr_b16 a[92:93], v243 offset:5632\n" \
  "v_exp_f32 v130, v130\n" \
  "v_add_f32 v224, v224, v128\n" \
  "v_exp_f32 v131, v131\n" \
  "v_mfma_f32_32x32x16_bf16 v[64:79], a[104:107], v[160:163], v[64:79]\n" \
  "ds_read_b64_tr_b16 a[94:95], v243 offset:7680\n" \
  "v_add_f32 v225, v225, v129\n" \
  "v_cvt_pk_bf16_f32 v128, v128, v129\n" \
  "v_mfma_f32_32x32x16_bf16 v[96:111], a[108:111], v[160:163], v[96:111]\n" \
  "buffer_load_dwordx4 v238, %[srdV], s40 offen offset:1024 lds\n" \
  "ds_read_b128 a[224:227], v245 offset:40960\n" \
  "v_exp_f32 v132, v132\n" \
  "v_add_f32 v224, v224, v130\n" \
  "v_exp_f32 v133, v133\n" \
  "v_mfma_f32_32x32x16_bf16 v[0:15], a[112:115], v[168:171], v[0:15]\n" \
  "ds_read_b128 a[228:231], v246 offset:40960\n" \
  "v_add_f32 v225, v225, v131\n" \
  "v_cvt_pk_bf16_f32 v129, v130, v131\n" \
  "v_mfma_f32_32x32x16_bf16 v[32:47], a[116:119], v[168:171], v[32:47]\n" \
  "buffer_load_dwordx4 v237, %[srdV], s40 offen offset:2048 lds\n" \
  "v_exp_f32 v134, v134\n" \
  "v_add_f32 v224, v224, v132\n" \
  "v_exp_f32 v135, v135\n" \
  "v_mfma_f32_32x32x16_bf16 v[64:79], a[120:123], v[168:171], v[64:79]\n" \
  "ds_read_b128 a[232:235], v247 offset:40960\n" \
  "v_add_f32 v225, v225, v133\n" \
  "v_cvt_pk_bf16_f32 v130, v132, v133\n" \
  "v_mfma_f32_32x32x16_bf16 v[96:111], a[124:127], v[168:171], v[96:111]\n" \
  "buffer_load_dwordx4 v238, %[srdV], s40 offen offset:3072 lds\n" \
  "ds_read_b128 a[236:239], v248 offset:40960\n" \
  "v_exp_f32 v136, v136\n" \
  "v_add_f32 v224, v224, v134\n" \
  "v_exp_f32 v137, v137\n" \
  "v_mfma_f32_32x32x16_bf16 v[16:31], a[96:99], v[176:179], v[16:31]\n" \
  "ds_read_b128 a[240:243], v249 offset:40960\n" \
  "v_add_f32 v225, v225, v135\n" \
  "v_cvt_pk_bf16_f32 v131, v134, v135\n" \
  "v_mfma_f32_32x32x16_bf16 v[48:63], a[100:103], v[176:179], v[48:63]\n" \
  "ds_read_b128 a[244:247], v250 offset:40960\n" \
  "v_exp_f32 v138, v138\n" \
  "v_add_f32 v224, v224, v136\n" \
  "v_exp_f32 v139, v139\n" \
  "v_mfma_f32_32x32x16_bf16 v[80:95], a[104:107], v[176:179], v[80:95]\n" \
  "ds_read_b128 a[248:251], v251 offset:40960\n" \
  "v_add_f32 v225, v225, v137\n" \
  "v_cvt_pk_bf16_f32 v136, v136, v137\n" \
  "v_mfma_f32_32x32x16_bf16 v[112:127], a[108:111], v[176:179], v[112:127]\n" \
  "ds_read_b128 a[252:255], v252 offset:40960\n" \
  "v_exp_f32 v140, v140\n" \
  "v_add_f32 v224, v224, v138\n" \
  "v_exp_f32 v141, v141\n" \
  "v_mfma_f32_32x32x16_bf16 v[16:31], a[112:115], v[184:187], v[16:31]\n" \
  "v_add_f32 v225, v225, v139\n" \
  "v_cvt_pk_bf16_f32 v137, v138, v139\n" \
  "v_mfma_f32_32x32x16_bf16 v[48:63], a[116:119], v[184:187], v[48:63]\n" \
  "v_exp_f32 v142, v142\n" \
  "v_add_f32 v224, v224, v140\n" \
  "v_exp_f32 v143, v143\n" \
  "v_mfma_f32_32x32x16_bf16 v[80:95], a[120:123], v[184:187], v[80:95]\n" \
  "v_add_f32 v225, v225, v141\n" \
  "v_cvt_pk_bf16_f32 v138, v140, v141\n" \
  "v_mfma_f32_32x32x16_bf16 v[112:127], a[124:127], v[184:187], v[112:127]\n" \
  "v_add_f32 v224, v224, v142\n" \
  "v_add_f32 v225, v225, v143\n" \
  "v_cvt_pk_bf16_f32 v139, v142, v143\n" \
  "s_waitcnt lgkmcnt(1)\n" \
  "v_mfma_f32_32x32x16_bf16 v[160:175], a[224:227], a[0:3], v[192:207]\n" \
  "v_exp_f32 v144, v144\n" \
  "v_exp_f32 v145, v145\n" \
  "v_mfma_f32_32x32x16_bf16 v[160:175], a[228:231], a[4:7], v[160:175]\n" \
  "ds_read_b64_tr_b16 a[96:97], v243 offset:8192\n" \
  "v_exp_f32 v146, v146\n" \
  "v_add_f32 v226, v226, v144\n" \
  "v_exp_f32 v147, v147\n" \
  "v_mfma_f32_32x32x16_bf16 v[160:175], a[232:235], a[8:11], v[160:175]\n" \
  "ds_read_b64_tr_b16 a[98:99], v243 offset:10240\n" \
  "v_add_f32 v227, v227, v145\n" \
  "v_cvt_pk_bf16_f32 v144, v144, v145\n" \
  "v_mfma_f32_32x32x16_bf16 v[160:175], a[236:239], a[12:15], v[160:175]\n" \
  "ds_read_b64_tr_b16 a[100:101], v243 offset:8704\n" \
  "v_exp_f32 v148, v148\n" \
  "v_add_f32 v226, v226, v146\n" \
  "v_exp_f32 v149, v149\n" \
  "v_mfma_f32_32x32x16_bf16 v[160:175], a[240:243], a[16:19], v[160:175]\n" \
  "ds_read_b64_tr_b16 a[102:103], v243 offset:10752\n" \
  "v_add_f32 v227, v227, v147\n" \
  "v_cvt_pk_bf16_f32 v145, v146, v147\n" \
  "v_mfma_f32_32x32x16_bf16 v[160:175], a[244:247], a[20:23], v[160:175]\n" \
  "ds_read_b64_tr_b16 a[104:105], v243 offset:9216\n" \
  "v_exp_f32 v150, v150\n" \
  "v_add_f32 v226, v226, v148\n" \
  "v_exp_f32 v151, v151\n" \
  "v_mfma_f32_32x32x16_bf16 v[160:175], a[248:251], a[24:27], v[160:175]\n" \
  "ds_read_b64_tr_b16 a[106:107], v243 offset:11264\n" \
  "v_add_f32 v227, v227, v149\n" \
  "v_cvt_pk_bf16_f32 v146, v148, v149\n" \
  "s_waitcnt lgkmcnt(5)\n" \
  "v_mfma_f32_32x32x16_bf16 v[160:175], a[252:255], a[28:31], v[160:175]\n" \
  "v_exp_f32 v152, v152\n" \
  "v_add_f32 v226, v226, v150\n" \
  "v_exp_f32 v153, v153\n" \
  "v_mfma_f32_32x32x16_bf16 v[176:191], a[224:227], a[32:35], v[208:223]\n" \
  "ds_read_b64_tr_b16 a[108:109], v243 offset:9728\n" \
  "v_add_f32 v227, v227, v151\n" \
  "v_cvt_pk_bf16_f32 v147, v150, v151\n" \
  "v_mfma_f32_32x32x16_bf16 v[176:191], a[228:231], a[36:39], v[176:191]\n" \
  "ds_read_b64_tr_b16 a[110:111], v243 offset:11776\n" \
  "v_exp_f32 v154, v154\n" \
  "v_add_f32 v226, v226, v152\n" \
  "v_exp_f32 v155, v155\n" \
  "v_mfma_f32_32x32x16_bf16 v[176:191], a[232:235], a[40:43], v[176:191]\n" \
  "ds_read_b64_tr_b16 a[112:113], v243 offset:12288\n" \
  "v_add_f32 v227, v227, v153\n" \
  "v_cvt_pk_bf16_f32 v152, v152, v153\n" \
  "v_mfma_f32_32x32x16_bf16 v[176:191], a[236:239], a[44:47], v[176:191]\n" \
  "ds_read_b64_tr_b16 a[114:115], v243 offset:14336\n" \
  "v_exp_f32 v156, v156\n" \
  "v_add_f32 v226, v226, v154\n" \
  "v_exp_f32 v157, v157\n" \
  "v_mfma_f32_32x32x16_bf16 v[176:191], a[240:243], a[48:51], v[176:191]\n" \
  "ds_read_b64_tr_b16 a[116:117], v243 offset:12800\n" \
  "v_add_f32 v227, v227, v155\n" \
  "v_cvt_pk_bf16_f32 v153, v154, v155\n" \
  "v_mfma_f32_32x32x16_bf16 v[176:191], a[244:247], a[52:55], v[176:191]\n" \
  "ds_read_b64_tr_b16 a[118:119], v243 offset:14848\n" \
  "v_exp_f32 v158, v158\n" \
  "v_add_f32 v226, v226, v156\n" \
  "v_exp_f32 v159, v159\n" \
  "v_mfma_f32_32x32x16_bf16 v[176:191], a[248:251], a[56:59], v[176:191]\n" \
  "v_add_f32 v227, v227, v157\n" \
  "v_cvt_pk_bf16_f32 v154, v156, v157\n" \
  "v_mfma_f32_32x32x16_bf16 v[176:191], a[252:255], a[60:63], v[176:191]\n" \
  "ds_read_b64_tr_b16 a[120:121], v243 offset:13312\n" \
  "v_add_f32 v226, v226, v158\n" \
  "v_add_f32 v227, v227, v159\n" \
  "v_cvt_pk_bf16_f32 v155, v158, v159\n" \
  "v_mfma_f32_32x32x16_bf16 v[0:15], a[64:67], v[128:131], v[0:15]\n" \
  "ds_read_b64_tr_b16 a[122:123], v243 offset:15360\n" \
  "v_exp_f32 v160, v160\n" \
  "v_exp_f32 v161, v161\n" \
  "v_mfma_f32_32x32x16_bf16 v[32:47], a[68:71], v[128:131], v[32:47]\n" \
  "ds_read_b64_tr_b16 a[124:125], v243 offset:13824\n" \
  "v_exp_f32 v162, v162\n" \
  "v_add_f32 v224, v224, v160\n" \
  "v_exp_f32 v163, v163\n" \
  "v_mfma_f32_32x32x16_bf16 v[64:79], a[72:75], v[128:131], v[64:79]\n" \
  "ds_read_b64_tr_b16 a[126:127], v243 offset:15872\n" \
  "v_add_f32 v225, v225, v161\n" \
  "v_cvt_pk_bf16_f32 v160, v160, v161\n" \
  "v_mfma_f32_32x32x16_bf16 v[96:111], a[76:79], v[128:131], v[96:111]\n" \
  "ds_read_b128 a[192:195], v245 offset:49152\n" \
  "v_exp_f32 v164, v164\n" \
  "v_add_f32 v224, v224, v162\n" \
  "v_exp_f32 v165, v165\n" \
  "v_mfma_f32_32x32x16_bf16 v[0:15], a[80:83], v[136:139], v[0:15]\n" \
  "ds_read_b128 a[196:199], v246 offset:49152\n" \
  "v_add_f32 v225, v225, v163\n" \
  "v_cvt_pk_bf16_f32 v161, v162, v163\n" \
  "v_mfma_f32_32x32x16_bf16 v[32:47], a[84:87], v[136:139], v[32:47]\n" \
  "v_exp_f32 v166, v166\n" \
  "v_add_f32 v224, v224, v164\n" \
  "v_exp_f32 v167, v167\n" \
  "v_mfma_f32_32x32x16_bf16 v[64:79], a[88:91], v[136:139], v[64:79]\n" \
  "ds_read_b128 a[200:203], v247 offset:49152\n" \
  "v_add_f32 v225, v225, v165\n" \
  "v_cvt_pk_bf16_f32 v162, v164, v165\n" \
  "v_mfma_f32_32x32x16_bf16 v[96:111], a[92:95], v[136:139], v[96:111]\n" \
  "ds_read_b128 a[204:207], v248 offset:49152\n" \
  "v_exp_f32 v168, v168\n" \
  "v_add_f32 v224, v224, v166\n" \
  "v_exp_f32 v169, v169\n" \
  "v_mfma_f32_32x32x16_bf16 v[16:31], a[64:67], v[144:147], v[16:31]\n" \
  "ds_read_b128 a[208:211], v249 offset:49152\n" \
  "v_add_f32 v225, v225, v167\n" \
  "v_cvt_pk_bf16_f32 v163, v166, v167\n" \
  "v_mfma_f32_32x32x16_bf16 v[48:63], a[68:71], v[144:147], v[48:63]\n" \
  "ds_read_b128 a[212:215], v250 offset:49152\n" \
  "v_exp_f32 v170, v170\n" \
  "v_add_f32 v224, v224, v168\n" \
  "v_exp_f32 v171, v171\n" \
  "v_mfma_f32_32x32x16_bf16 v[80:95], a[72:75], v[144:147], v[80:95]\n" \
  "ds_read_b128 a[216:219], v251 offset:49152\n" \
  "v_add_f32 v225, v225, v169\n" \
  "v_cvt_pk_bf16_f32 v168, v168, v169\n" \
  "v_mfma_f32_32x32x16_bf16 v[112:127], a[76:79], v[144:147], v[112:127]\n" \
  "ds_read_b128 a[220:223], v252 offset:49152\n" \
  "v_exp_f32 v172, v172\n" \
  "v_add_f32 v224, v224, v170\n" \
  "v_exp_f32 v173, v173\n" \
  "v_mfma_f32_32x32x16_bf16 v[16:31], a[80:83], v[152:155], v[16:31]\n" \
  "v_add_f32 v225, v225, v171\n" \
  "v_cvt_pk_bf16_f32 v169, v170, v171\n" \
  "v_mfma_f32_32x32x16_bf16 v[48:63], a[84:87], v[152:155], v[48:63]\n" \
  "v_exp_f32 v174, v174\n" \
  "v_add_f32 v224, v224, v172\n" \
  "v_exp_f32 v175, v175\n" \
  "v_mfma_f32_32x32x16_bf16 v[80:95], a[88:91], v[152:155], v[80:95]\n" \
  "v_add_f32 v225, v225, v173\n" \
  "v_cvt_pk_bf16_f32 v170, v172, v173\n" \
  "v_mfma_f32_32x32x16_bf16 v[112:127], a[92:95], v[152:155], v[112:127]\n" \
  "v_add_f32 v224, v224, v174\n" \
  "v_add_f32 v225, v225, v175\n" \
  "v_cvt_pk_bf16_f32 v171, v174, v175\n" \
  "s_waitcnt vmcnt(17)\n" \
  "v_accvgpr_read_b32 v228, a188\n" \
  "v_accvgpr_read_b32 v229, a189\n" \
  "v_accvgpr_read_b32 v230, a190\n" \
  "v_accvgpr_read_b32 v241, a191\n" \
  "s_cmp_lg_u32 %[w], 0\n" \
  "s_cbranch_scc1 .Lrel_skip_%=\n" \
  "s_mov_b32 s57, 0\n" \
  ".Lrel_chk_%=:\n" \
  "s_nop 1\n" \
  "v_cmp_ne_u32 vcc, %[tag], v228\n" \
  "s_nop 4\n" \
  "s_mov_b64 s[58:59], vcc\n" \
  "s_nop 1\n" \
  "v_cmp_ne_u32 vcc, %[tag], v229\n" \
  "s_nop 4\n" \
  "s_or_b64 s[58:59], s[58:59], vcc\n" \
  "s_nop 1\n" \
  "v_cmp_ne_u32 vcc, %[tag], v230\n" \
  "s_nop 4\n" \
  "s_or_b64 s[58:59], s[58:59], vcc\n" \
  "s_nop 1\n" \
  "v_cmp_ne_u32 vcc, %[tag], v241\n" \
  "s_nop 4\n" \
  "s_or_b64 s[58:59], s[58:59], vcc\n" \
  "s_cmp_eq_u64 s[58:59], 0\n" \
  "s_cbranch_scc1 .Lrel_done_%=\n" \
  "s_add_u32 s57, s57, 1\n" \
  "s_cmp_gt_u32 s57, 0x10000\n" \
  "s_cbranch_scc1 .Lrel_done_%=\n" \
  "s_sleep 1\n" \
  "global_load_dwordx4 a[188:191], v232, %[frel] sc1\n" \
  "s_waitcnt vmcnt(0)\n" \
  "v_accvgpr_read_b32 v228, a188\n" \
  "v_accvgpr_read_b32 v229, a189\n" \
  "v_accvgpr_read_b32 v230, a190\n" \
  "v_accvgpr_read_b32 v241, a191\n" \
  "s_branch .Lrel_chk_%=\n" \
  ".Lrel_done_%=:\n" \
  ".Lrel_skip_%=:\n" \
  "s_waitcnt lgkmcnt(0)\n" \
  "s_waitcnt vmcnt(9)\n" \
  "s_barrier\n" \
  "v_mfma_f32_32x32x16_bf16 v[128:143], a[192:195], a[0:3], v[192:207]\n" \
  "v_exp_f32 v176, v176\n" \
  "v_exp_f32 v177, v177\n" \
  "v_mfma_f32_32x32x16_bf16 v[128:143], a[196:199], a[4:7], v[128:143]\n" \
  "s_add_u32 m0, s60, 32768\n" \
  "ds_read_b64_tr_b16 a[64:65], v243 offset:16384\n" \
  "v_exp_f32 v178, v178\n" \
  "v_add_f32 v226, v226, v176\n" \
  "v_exp_f32 v179, v179\n" \
  "v_mfma_f32_32x32x16_bf16 v[128:143], a[200:203], a[8:11], v[128:143]\n" \
  "ds_read_b64_tr_b16 a[66:67], v243 offset:18432\n" \
  "v_add_f32 v227, v227, v177\n" \
  "v_cvt_pk_bf16_f32 v176, v176, v177\n" \
  "v_mfma_f32_32x32x16_bf16 v[128:143], a[204:207], a[12:15], v[128:143]\n" \
  "s_mov_b32 s40, 98304\n" \
  "ds_read_b64_tr_b16 a[68:69], v243 offset:16896\n" \
  "v_exp_f32 v180, v180\n" \
  "v_add_f32 v226, v226, v178\n" \
  "v_exp_f32 v181, v181\n" \
  "v_mfma_f32_32x32x16_bf16 v[128:143], a[208:211], a[16:19], v[128:143]\n" \
  "ds_read_b64_tr_b16 a[70:71], v243 offset:18944\n" \
  "v_add_f32 v227, v227, v179\n" \
  "v_cvt_pk_bf16_f32 v177, v178, v179\n" \
  "v_mfma_f32_32x32x16_bf16 v[128:143], a[212:215], a[20:23], v[128:143]\n" \
  "buffer_load_dwordx4 v233, %[srdK], s40 offen offset:0 lds\n" \
  "ds_read_b64_tr_b16 a[72:73], v243 offset:17408\n" \
  "v_exp_f32 v182, v182\n" \
  "v_add_f32 v226, v226, v180\n" \
  "v_exp_f32 v183, v183\n" \
  "v_mfma_f32_32x32x16_bf16 v[128:143], a[216:219], a[24:27], v[128:143]\n" \
  "ds_read_b64_tr_b16 a[74:75], v243 offset:19456\n" \
  "v_add_f32 v227, v227, v181\n" \
  "v_cvt_pk_bf16_f32 v178, v180, v181\n" \
  "v_mfma_f32_32x32x16_bf16 v[128:143], a[220:223], a[28:31], v[128:143]\n" \
  "buffer_load_dwordx4 v234, %[srdK], s40 offen offset:1024 lds\n" \
  "v_exp_f32 v184, v184\n" \
  "v_add_f32 v226, v226, v182\n" \
  "v_exp_f32 v185, v185\n" \
  "v_mfma_f32_32x32x16_bf16 v[144:159], a[192:195], a[32:35], v[208:223]\n" \
  "ds_read_b64_tr_b16 a[76:77], v243 offset:17920\n" \
  "v_add_f32 v227, v227, v183\n" \
  "v_cvt_pk_bf16_f32 v179, v182, v183\n" \
  "v_mfma_f32_32x32x16_bf16 v[144:159], a[196:199], a[36:39], v[144:159]\n" \
  "buffer_load_dwordx4 v235, %[srdK], s40 offen offset:2048 lds\n" \
  "ds_read_b64_tr_b16 a[78:79], v243 offset:19968\n" \
  "v_exp_f32 v186, v186\n" \
  "v_add_f32 v226, v226, v184\n" \
  "v_exp_f32 v187, v187\n" \
  "v_mfma_f32_32x32x16_bf16 v[144:159], a[200:203], a[40:43], v[144:159]\n" \
  "ds_read_b64_tr_b16 a[80:81], v243 offset:20480\n" \
  "v_add_f32 v227, v227, v185\n" \
  "v_cvt_pk_bf16_f32 v184, v184, v185\n" \
  "v_mfma_f32_32x32x16_bf16 v[144:159], a[204:207], a[44:47], v[144:159]\n" \
  "buffer_load_dwordx4 v236, %[srdK], s40 offen offset:3072 lds\n" \
  "ds_read_b64_tr_b16 a[82:83], v243 offset:22528\n" \
  "v_exp_f32 v188, v188\n" \
  "v_add_f32 v226, v226, v186\n" \
  "v_exp_f32 v189, v189\n" \
  "v_mfma_f32_32x32x16_bf16 v[144:159], a[208:211], a[48:51], v[144:159]\n" \
  "ds_read_b64_tr_b16 a[84:85], v243 offset:20992\n" \
  "v_add_f32 v227, v227, v187\n" \
  "v_cvt_pk_bf16_f32 v185, v186, v187\n" \
  "v_mfma_f32_32x32x16_bf16 v[144:159], a[212:215], a[52:55], v[144:159]\n" \
  "s_add_u32 m0, s60, 114688\n" \
  "ds_read_b64_tr_b16 a[86:87], v243 offset:23040\n" \
  "v_exp_f32 v190, v190\n" \
  "v_add_f32 v226, v226, v188\n" \
  "v_exp_f32 v191, v191\n" \
  "v_mfma_f32_32x32x16_bf16 v[144:159], a[216:219], a[56:59], v[144:159]\n" \
  "v_add_f32 v227, v227, v189\n" \
  "v_cvt_pk_bf16_f32 v186, v188, v189\n" \
  "v_mfma_f32_32x32x16_bf16 v[144:159], a[220:223], a[60:63], v[144:159]\n" \
  "s_mov_b32 s40, 81920\n" \
  "ds_read_b64_tr_b16 a[88:89], v243 offset:21504\n" \
  "v_add_f32 v226, v226, v190\n" \
  "v_add_f32 v227, v227, v191\n" \
  "v_cvt_pk_bf16_f32 v187, v190, v191\n" \
  "v_mfma_f32_32x32x16_bf16 v[0:15], a[96:99], v[160:163], v[0:15]\n" \
  "ds_read_b64_tr_b16 a[90:91], v243 offset:23552\n" \
  "v_exp_f32 v128, v128\n" \
  "v_exp_f32 v129, v129\n" \
  "v_mfma_f32_32x32x16_bf16 v[32:47], a[100:103], v[160:163], v[32:47]\n" \
  "buffer_load_dwordx4 v237, %[srdV], s40 offen offset:0 lds\n" \
  "ds_read_b64_tr_b16 a[92:93], v243 offset:22016\n" \
  "v_exp_f32 v130, v130\n" \
  "v_add_f32 v224, v224, v128\n" \
  "v_exp_f32 v131, v131\n" \
  "v_mfma_f32_32x32x16_bf16 v[64:79], a[104:107], v[160:163], v[64:79]\n" \
  "ds_read_b64_tr_b16 a[94:95], v243 offset:24064\n" \
  "v_add_f32 v225, v225, v129\n" \
  "v_cvt_pk_bf16_f32 v128, v128, v129\n" \
  "v_mfma_f32_32x32x16_bf16 v[96:111], a[108:111], v[160:163], v[96:111]\n" \
  "buffer_load_dwordx4 v238, %[srdV], s40 offen offset:1024 lds\n" \
  "ds_read_b128 a[224:227], v245 offset:57344\n" \
  "v_exp_f32 v132, v132\n" \
  "v_add_f32 v224, v224, v130\n" \
  "v_exp_f32 v133, v133\n" \
  "v_mfma_f32_32x32x16_bf16 v[0:15], a[112:115], v[168:171], v[0:15]\n" \
  "ds_read_b128 a[228:231], v246 offset:57344\n" \
  "v_add_f32 v225, v225, v131\n" \
  "v_cvt_pk_bf16_f32 v129, v130, v131\n" \
  "v_mfma_f32_32x32x16_bf16 v[32:47], a[116:119], v[168:171], v[32:47]\n" \
  "buffer_load_dwordx4 v237, %[srdV], s40 offen offset:2048 lds\n" \
  "v_exp_f32 v134, v134\n" \
  "v_add_f32 v224, v224, v132\n" \
  "v_exp_f32 v135, v135\n" \
  "v_mfma_f32_32x32x16_bf16 v[64:79], a[120:123], v[168:171], v[64:79]\n" \
  "ds_read_b128 a[232:235], v247 offset:57344\n" \
  "v_add_f32 v225, v225, v133\n" \
  "v_cvt_pk_bf16_f32 v130, v132, v133\n" \
  "v_mfma_f32_32x32x16_bf16 v[96:111], a[124:127], v[168:171], v[96:111]\n" \
  "buffer_load_dwordx4 v238, %[srdV], s40 offen offset:3072 lds\n" \
  "ds_read_b128 a[236:239], v248 offset:57344\n" \
  "v_exp_f32 v136, v136\n" \
  "v_add_f32 v224, v224, v134\n" \
  "v_exp_f32 v137, v137\n" \
  "v_mfma_f32_32x32x16_bf16 v[16:31], a[96:99], v[176:179], v[16:31]\n" \
  "ds_read_b128 a[240:243], v249 offset:57344\n" \
  "v_add_f32 v225, v225, v135\n" \
  "v_cvt_pk_bf16_f32 v131, v134, v135\n" \
  "v_mfma_f32_32x32x16_bf16 v[48:63], a[100:103], v[176:179], v[48:63]\n" \
  "ds_read_b128 a[244:247], v250 offset:57344\n" \
  "v_exp_f32 v138, v138\n" \
  "v_add_f32 v224, v224, v136\n" \
  "v_exp_f32 v139, v139\n" \
  "v_mfma_f32_32x32x16_bf16 v[80:95], a[104:107], v[176:179], v[80:95]\n" \
  "ds_read_b128 a[248:251], v251 offset:57344\n" \
  "v_add_f32 v225, v225, v137\n" \
  "v_cvt_pk_bf16_f32 v136, v136, v137\n" \
  "v_mfma_f32_32x32x16_bf16 v[112:127], a[108:111], v[176:179], v[112:127]\n" \
  "ds_read_b128 a[252:255], v252 offset:57344\n" \
  "v_exp_f32 v140, v140\n" \
  "v_add_f32 v224, v224, v138\n" \
  "v_exp_f32 v141, v141\n" \
  "v_mfma_f32_32x32x16_bf16 v[16:31], a[112:115], v[184:187], v[16:31]\n" \
  "v_add_f32 v225, v225, v139\n" \
  "v_cvt_pk_bf16_f32 v137, v138, v139\n" \
  "v_mfma_f32_32x32x16_bf16 v[48:63], a[116:119], v[184:187], v[48:63]\n" \
  "v_exp_f32 v142, v142\n" \
  "v_add_f32 v224, v224, v140\n" \
  "v_exp_f32 v143, v143\n" \
  "v_mfma_f32_32x32x16_bf16 v[80:95], a[120:123], v[184:187], v[80:95]\n" \
  "v_add_f32 v225, v225, v141\n" \
  "v_cvt_pk_bf16_f32 v138, v140, v141\n" \
  "v_mfma_f32_32x32x16_bf16 v[112:127], a[124:127], v[184:187], v[112:127]\n" \
  "v_add_f32 v224, v224, v142\n" \
  "v_add_f32 v225, v225, v143\n" \
  "v_cvt_pk_bf16_f32 v139, v142, v143\n" \
  "s_waitcnt lgkmcnt(1)\n" \
  "v_mfma_f32_32x32x16_bf16 v[160:175], a[224:227], a[0:3], v[192:207]\n" \
  "v_exp_f32 v144, v144\n" \
  "v_exp_f32 v145, v145\n" \
  "v_mfma_f32_32x32x16_bf16 v[160:175], a[228:231], a[4:7], v[160:175]\n" \
  "ds_read_b64_tr_b16 a[96:97], v243 offset:24576\n" \
  "v_exp_f32 v146, v146\n" \
  "v_add_f32 v226, v226, v144\n" \
  "v_exp_f32 v147, v147\n" \
  "v_mfma_f32_32x32x16_bf16 v[160:175], a[232:235], a[8:11], v[160:175]\n" \
  "ds_read_b64_tr_b16 a[98:99], v243 offset:26624\n" \
  "v_add_f32 v227, v227, v145\n" \
  "v_cvt_pk_bf16_f32 v144, v144, v145\n" \
  "v_mfma_f32_32x32x16_bf16 v[160:175], a[236:239], a[12:15], v[160:175]\n" \
  "ds_read_b64_tr_b16 a[100:101], v243 offset:25088\n" \
  "v_exp_f32 v148, v148\n" \
  "v_add_f32 v226, v226, v146\n" \
  "v_exp_f32 v149, v149\n" \
  "v_mfma_f32_32x32x16_bf16 v[160:175], a[240:243], a[16:19], v[160:175]\n" \
  "ds_read_b64_tr_b16 a[102:103], v243 offset:27136\n" \
  "v_add_f32 v227, v227, v147\n" \
  "v_cvt_pk_bf16_f32 v145, v146, v147\n" \
  "v_mfma_f32_32x32x16_bf16 v[160:175], a[244:247], a[20:23], v[160:175]\n" \
  "ds_read_b64_tr_b16 a[104:105], v243 offset:25600\n" \
  "v_exp_f32 v150, v150\n" \
  "v_add_f32 v226, v226, v148\n" \
  "v_exp_f32 v151, v151\n" \
  "v_mfma_f32_32x32x16_bf16 v[160:175], a[248:251], a[24:27], v[160:175]\n" \
  "ds_read_b64_tr_b16 a[106:107], v243 offset:27648\n" \
  "v_add_f32 v227, v227, v149\n" \
  "v_cvt_pk_bf16_f32 v146, v148, v149\n" \
  "s_waitcnt lgkmcnt(5)\n" \
  "v_mfma_f32_32x32x16_bf16 v[160:175], a[252:255], a[28:31], v[160:175]\n" \
  "v_exp_f32 v152, v152\n" \
  "v_add_f32 v226, v226, v150\n" \
  "v_exp_f32 v153, v153\n" \
  "v_mfma_f32_32x32x16_bf16 v[176:191], a[224:227], a[32:35], v[208:223]\n" \
  "ds_read_b64_tr_b16 a[108:109], v243 offset:26112\n" \
  "v_add_f32 v227, v227, v151\n" \
  "v_cvt_pk_bf16_f32 v147, v150, v151\n" \
  "v_mfma_f32_32x32x16_bf16 v[176:191], a[228:231], a[36:39], v[176:191]\n" \
  "ds_read_b64_tr_b16 a[110:111], v243 offset:28160\n" \
  "v_exp_f32 v154, v154\n" \
  "v_add_f32 v226, v226, v152\n" \
  "v_exp_f32 v155, v155\n" \
  "v_mfma_f32_32x32x16_bf16 v[176:191], a[232:235], a[40:43], v[176:191]\n" \
  "ds_read_b64_tr_b16 a[112:113], v243 offset:28672\n" \
  "v_add_f32 v227, v227, v153\n" \
  "v_cvt_pk_bf16_f32 v152, v152, v153\n" \
  "v_mfma_f32_32x32x16_bf16 v[176:191], a[236:239], a[44:47], v[176:191]\n" \
  "ds_read_b64_tr_b16 a[114:115], v243 offset:30720\n" \
  "v_exp_f32 v156, v156\n" \
  "v_add_f32 v226, v226, v154\n" \
  "v_exp_f32 v157, v157\n" \
  "v_mfma_f32_32x32x16_bf16 v[176:191], a[240:243], a[48:51], v[176:191]\n" \
  "ds_read_b64_tr_b16 a[116:117], v243 offset:29184\n" \
  "v_add_f32 v227, v227, v155\n" \
  "v_cvt_pk_bf16_f32 v153, v154, v155\n" \
  "v_mfma_f32_32x32x16_bf16 v[176:191], a[244:247], a[52:55], v[176:191]\n" \
  "ds_read_b64_tr_b16 a[118:119], v243 offset:31232\n" \
  "v_exp_f32 v158, v158\n" \
  "v_add_f32 v226, v226, v156\n" \
  "v_exp_f32 v159, v159\n" \
  "v_mfma_f32_32x32x16_bf16 v[176:191], a[248:251], a[56:59], v[176:191]\n" \
  "v_add_f32 v227, v227, v157\n" \
  "v_cvt_pk_bf16_f32 v154, v156, v157\n" \
  "v_mfma_f32_32x32x16_bf16 v[176:191], a[252:255], a[60:63], v[176:191]\n" \
  "ds_read_b64_tr_b16 a[120:121], v243 offset:29696\n" \
  "v_add_f32 v226, v226, v158\n" \
  "v_add_f32 v227, v227, v159\n" \
  "v_cvt_pk_bf16_f32 v155, v158, v159\n" \
  "v_mfma_f32_32x32x16_bf16 v[0:15], a[64:67], v[128:131], v[0:15]\n" \
  "ds_read_b64_tr_b16 a[122:123], v243 offset:31744\n" \
  "v_exp_f32 v160, v160\n" \
  "v_exp_f32 v161, v161\n" \
  "v_mfma_f32_32x32x16_bf16 v[32:47], a[68:71], v[128:131], v[32:47]\n" \
  "ds_read_b64_tr_b16 a[124:125], v243 offset:30208\n" \
  "v_exp_f32 v162, v162\n" \
  "v_add_f32 v224, v224, v160\n" \
  "v_exp_f32 v163, v163\n" \
  "v_mfma_f32_32x32x16_bf16 v[64:79], a[72:75], v[128:131], v[64:79]\n" \
  "ds_read_b64_tr_b16 a[126:127], v243 offset:32256\n" \
  "v_add_f32 v225, v225, v161\n" \
  "v_cvt_pk_bf16_f32 v160, v160, v161\n" \
  "v_mfma_f32_32x32x16_bf16 v[96:111], a[76:79], v[128:131], v[96:111]\n" \
  "ds_read_b128 a[192:195], v245 offset:0\n" \
  "v_exp_f32 v164, v164\n" \
  "v_add_f32 v224, v224, v162\n" \
  "v_exp_f32 v165, v165\n" \
  "v_mfma_f32_32x32x16_bf16 v[0:15], a[80:83], v[136:139], v[0:15]\n" \
  "ds_read_b128 a[196:199], v246 offset:0\n" \
  "v_add_f32 v225, v225, v163\n" \
  "v_cvt_pk_bf16_f32 v161, v162, v163\n" \
  "v_mfma_f32_32x32x16_bf16 v[32:47], a[84:87], v[136:139], v[32:47]\n" \
  "v_exp_f32 v166, v166\n" \
  "v_add_f32 v224, v224, v164\n" \
  "v_exp_f32 v167, v167\n" \
  "v_mfma_f32_32x32x16_bf16 v[64:79], a[88:91], v[136:139], v[64:79]\n" \
  "ds_read_b128 a[200:203], v247 offset:0\n" \
  "v_add_f32 v225, v225, v165\n" \
  "v_cvt_pk_bf16_f32 v162, v164, v165\n" \
  "v_mfma_f32_32x32x16_bf16 v[96:111], a[92:95], v[136:139], v[96:111]\n" \
  "ds_read_b128 a[204:207], v248 offset:0\n" \
  "v_exp_f32 v168, v168\n" \
  "v_add_f32 v224, v224, v166\n" \
  "v_exp_f32 v169, v169\n" \
  "v_mfma_f32_32x32x16_bf16 v[16:31], a[64:67], v[144:147], v[16:31]\n" \
  "ds_read_b128 a[208:211], v249 offset:0\n" \
  "v_add_f32 v225, v225, v167\n" \
  "v_cvt_pk_bf16_f32 v163, v166, v167\n" \
  "v_mfma_f32_32x32x16_bf16 v[48:63], a[68:71], v[144:147], v[48:63]\n" \
  "ds_read_b128 a[212:215], v250 offset:0\n" \
  "v_exp_f32 v170, v170\n" \
  "v_add_f32 v224, v224, v168\n" \
  "v_exp_f32 v171, v171\n" \
  "v_mfma_f32_32x32x16_bf16 v[80:95], a[72:75], v[144:147], v[80:95]\n" \
  "ds_read_b128 a[216:219], v251 offset:0\n" \
  "v_add_f32 v225, v225, v169\n" \
  "v_cvt_pk_bf16_f32 v168, v168, v169\n" \
  "v_mfma_f32_32x32x16_bf16 v[112:127], a[76:79], v[144:147], v[112:127]\n" \
  "ds_read_b128 a[220:223], v252 offset:0\n" \
  "v_exp_f32 v172, v172\n" \
  "v_add_f32 v224, v224, v170\n" \
  "v_exp_f32 v173, v173\n" \
  "v_mfma_f32_32x32x16_bf16 v[16:31], a[80:83], v[152:155], v[16:31]\n" \
  "v_add_f32 v225, v225, v171\n" \
  "v_cvt_pk_bf16_f32 v169, v170, v171\n" \
  "v_mfma_f32_32x32x16_bf16 v[48:63], a[84:87], v[152:155], v[48:63]\n" \
  "v_exp_f32 v174, v174\n" \
  "v_add_f32 v224, v224, v172\n" \
  "v_exp_f32 v175, v175\n" \
  "v_mfma_f32_32x32x16_bf16 v[80:95], a[88:91], v[152:155], v[80:95]\n" \
  "v_add_f32 v225, v225, v173\n" \
  "v_cvt_pk_bf16_f32 v170, v172, v173\n" \
  "v_mfma_f32_32x32x16_bf16 v[112:127], a[92:95], v[152:155], v[112:127]\n" \
  "v_add_f32 v224, v224, v174\n" \
  "v_add_f32 v225, v225, v175\n" \
  "v_cvt_pk_bf16_f32 v171, v174, v175\n" \
  "s_waitcnt vmcnt(16)\n" \
  "v_accvgpr_read_b32 v228, a184\n" \
  "v_accvgpr_read_b32 v229, a185\n" \
  "v_accvgpr_read_b32 v230, a186\n" \
  "v_accvgpr_read_b32 v241, a187\n" \
  "s_cmp_lg_u32 %[w], 0\n" \
  "s_cbranch_scc1 .Lmod_skip_%=\n" \
  "s_mov_b32 exec_hi, 0xff\n" \
  "s_mov_b32 s57, 0\n" \
  ".Lmod_chk_%=:\n" \
  "s_nop 1\n" \
  "v_cmp_ne_u32 vcc, %[tag], v228\n" \
  "s_nop 4\n" \
  "s_mov_b64 s[58:59], vcc\n" \
  "s_nop 1\n" \
  "v_cmp_ne_u32 vcc, %[tag], v229\n" \
  "s_nop 4\n" \
  "s_or_b64 s[58:59], s[58:59], vcc\n" \
  "s_nop 1\n" \
  "v_cmp_ne_u32 vcc, %[tag], v230\n" \
  "s_nop 4\n" \
  "s_or_b64 s[58:59], s[58:59], vcc\n" \
  "s_nop 1\n" \
  "v_cmp_ne_u32 vcc, %[tag], v241\n" \
  "s_nop 4\n" \
  "s_or_b64 s[58:59], s[58:59], vcc\n" \
  "s_cmp_eq_u64 s[58:59], 0\n" \
  "s_cbranch_scc1 .Lmod_done_%=\n" \
  "s_add_u32 s57, s57, 1\n" \
  "s_cmp_gt_u32 s57, 0x10000\n" \
  "s_cbranch_scc1 .Lmod_done_%=\n" \
  "s_sleep 1\n" \
  "global_load_dwordx4 a[184:187], v232, %[fmod] sc1\n" \
  "s_waitcnt vmcnt(0)\n" \
  "v_accvgpr_read_b32 v228, a184\n" \
  "v_accvgpr_read_b32 v229, a185\n" \
  "v_accvgpr_read_b32 v230, a186\n" \
  "v_accvgpr_read_b32 v241, a187\n" \
  "s_branch .Lmod_chk_%=\n" \
  ".Lmod_done_%=:\n" \
  "s_mov_b64 exec, -1\n" \
  ".Lmod_skip_%=:\n" \
  "s_waitcnt lgkmcnt(0)\n" \
  "s_waitcnt vmcnt(8)\n" \
  "s_barrier\n" \
  "v_mfma_f32_32x32x16_bf16 v[128:143], a[192:195], a[0:3], v[192:207]\n" \
  "v_exp_f32 v176, v176\n" \
  "v_exp_f32 v177, v177\n" \
  "v_mfma_f32_32x32x16_bf16 v[128:143], a[196:199], a[4:7], v[128:143]\n" \
  "s_add_u32 m0, s60, 49152\n" \
  "ds_read_b64_tr_b16 a[64:65], v243 offset:32768\n" \
  "v_exp_f32 v178, v178\n" \
  "v_add_f32 v226, v226, v176\n" \
  "v_exp_f32 v179, v179\n" \
  "v_mfma_f32_32x32x16_bf16 v[128:143], a[200:203], a[8:11], v[128:143]\n" \
  "ds_read_b64_tr_b16 a[66:67], v243 offset:34816\n" \
  "v_add_f32 v227, v227, v177\n" \
  "v_cvt_pk_bf16_f32 v176, v176, v177\n" \
  "v_mfma_f32_32x32x16_bf16 v[128:143], a[204:207], a[12:15], v[128:143]\n" \
  "s_mov_b32 s40, 114688\n" \
  "ds_read_b64_tr_b16 a[68:69], v243 offset:33280\n" \
  "v_exp_f32 v180, v180\n" \
  "v_add_f32 v226, v226, v178\n" \
  "v_exp_f32 v181, v181\n" \
  "v_mfma_f32_32x32x16_bf16 v[128:143], a[208:211], a[16:19], v[128:143]\n" \
  "ds_read_b64_tr_b16 a[70:71], v243 offset:35328\n" \
  "v_add_f32 v227, v227, v179\n" \
  "v_cvt_pk_bf16_f32 v177, v178, v179\n" \
  "v_mfma_f32_32x32x16_bf16 v[128:143], a[212:215], a[20:23], v[128:143]\n" \
  "buffer_load_dwordx4 v233, %[srdK], s40 offen offset:0 lds\n" \
  "ds_read_b64_tr_b16 a[72:73], v243 offset:33792\n" \
  "v_exp_f32 v182, v182\n" \
  "v_add_f32 v226, v226, v180\n" \
  "v_exp_f32 v183, v183\n" \
  "v_mfma_f32_32x32x16_bf16 v[128:143], a[216:219], a[24:27], v[128:143]\n" \
  "ds_read_b64_tr_b16 a[74:75], v243 offset:35840\n" \
  "v_add_f32 v227, v227, v181\n" \
  "v_cvt_pk_bf16_f32 v178, v180, v181\n" \
  "v_mfma_f32_32x32x16_bf16 v[128:143], a[220:223], a[28:31], v[128:143]\n" \
  "buffer_load_dwordx4 v234, %[srdK], s40 offen offset:1024 lds\n" \
  "v_exp_f32 v184, v184\n" \
  "v_add_f32 v226, v226, v182\n" \
  "v_exp_f32 v185, v185\n" \
  "v_mfma_f32_32x32x16_bf16 v[144:159], a[192:195], a[32:35], v[208:223]\n" \
  "ds_read_b64_tr_b16 a[76:77], v243 offset:34304\n" \
  "v_add_f32 v227, v227, v183\n" \
  "v_cvt_pk_bf16_f32 v179, v182, v183\n" \
  "v_mfma_f32_32x32x16_bf16 v[144:159], a[196:199], a[36:39], v[144:159]\n" \
  "buffer_load_dwordx4 v235, %[srdK], s40 offen offset:2048 lds\n" \
  "ds_read_b64_tr_b16 a[78:79], v243 offset:36352\n" \
  "v_exp_f32 v186, v186\n" \
  "v_add_f32 v226, v226, v184\n" \
  "v_exp_f32 v187, v187\n" \
  "v_mfma_f32_32x32x16_bf16 v[144:159], a[200:203], a[40:43], v[144:159]\n" \
  "ds_read_b64_tr_b16 a[80:81], v243 offset:36864\n" \
  "v_add_f32 v227, v227, v185\n" \
  "v_cvt_pk_bf16_f32 v184, v184, v185\n" \
  "v_mfma_f32_32x32x16_bf16 v[144:159], a[204:207], a[44:47], v[144:159]\n" \
  "buffer_load_dwordx4 v236, %[srdK], s40 offen offset:3072 lds\n" \
  "ds_read_b64_tr_b16 a[82:83], v243 offset:38912\n" \
  "v_exp_f32 v188, v188\n" \
  "v_add_f32 v226, v226, v186\n" \
  "v_exp_f32 v189, v189\n" \
  "v_mfma_f32_32x32x16_bf16 v[144:159], a[208:211], a[48:51], v[144:159]\n" \
  "ds_read_b64_tr_b16 a[84:85], v243 offset:37376\n" \
  "v_add_f32 v227, v227, v187\n" \
  "v_cvt_pk_bf16_f32 v185, v186, v187\n" \
  "v_mfma_f32_32x32x16_bf16 v[144:159], a[212:215], a[52:55], v[144:159]\n" \
  "s_add_u32 m0, s60, 131072\n" \
  "ds_read_b64_tr_b16 a[86:87], v243 offset:39424\n" \
  "v_exp_f32 v190, v190\n" \
  "v_add_f32 v226, v226, v188\n" \
  "v_exp_f32 v191, v191\n" \
  "v_mfma_f32_32x32x16_bf16 v[144:159], a[216:219], a[56:59], v[144:159]\n" \
  "v_add_f32 v227, v227, v189\n" \
  "v_cvt_pk_bf16_f32 v186, v188, v189\n" \
  "v_mfma_f32_32x32x16_bf16 v[144:159], a[220:223], a[60:63], v[144:159]\n" \
  "s_mov_b32 s40, 98304\n" \
  "ds_read_b64_tr_b16 a[88:89], v243 offset:37888\n" \
  "v_add_f32 v226, v226, v190\n" \
  "v_add_f32 v227, v227, v191\n" \
  "v_cvt_pk_bf16_f32 v187, v190, v191\n" \
  "v_mfma_f32_32x32x16_bf16 v[0:15], a[96:99], v[160:163], v[0:15]\n" \
  "ds_read_b64_tr_b16 a[90:91], v243 offset:39936\n" \
  "v_exp_f32 v128, v128\n" \
  "v_exp_f32 v129, v129\n" \
  "v_mfma_f32_32x32x16_bf16 v[32:47], a[100:103], v[160:163], v[32:47]\n" \
  "buffer_load_dwordx4 v237, %[srdV], s40 offen offset:0 lds\n" \
  "ds_read_b64_tr_b16 a[92:93], v243 offset:38400\n" \
  "v_exp_f32 v130, v130\n" \
  "v_add_f32 v224, v224, v128\n" \
  "v_exp_f32 v131, v131\n" \
  "v_mfma_f32_32x32x16_bf16 v[64:79], a[104:107], v[160:163], v[64:79]\n" \
  "ds_read_b64_tr_b16 a[94:95], v243 offset:40448\n" \
  "v_add_f32 v225, v225, v129\n" \
  "v_cvt_pk_bf16_f32 v128, v128, v129\n" \
  "v_mfma_f32_32x32x16_bf16 v[96:111], a[108:111], v[160:163], v[96:111]\n" \
  "buffer_load_dwordx4 v238, %[srdV], s40 offen offset:1024 lds\n" \
  "ds_read_b128 a[224:227], v245 offset:8192\n" \
  "v_exp_f32 v132, v132\n" \
  "v_add_f32 v224, v224, v130\n" \
  "v_exp_f32 v133, v133\n" \
  "v_mfma_f32_32x32x16_bf16 v[0:15], a[112:115], v[168:171], v[0:15]\n" \
  "ds_read_b128 a[228:231], v246 offset:8192\n" \
  "v_add_f32 v225, v225, v131\n" \
  "v_cvt_pk_bf16_f32 v129, v130, v131\n" \
  "v_mfma_f32_32x32x16_bf16 v[32:47], a[116:119], v[168:171], v[32:47]\n" \
  "buffer_load_dwordx4 v237, %[srdV], s40 offen offset:2048 lds\n" \
  "v_exp_f32 v134, v134\n" \
  "v_add_f32 v224, v224, v132\n" \
  "v_exp_f32 v135, v135\n" \
  "v_mfma_f32_32x32x16_bf16 v[64:79], a[120:123], v[168:171], v[64:79]\n" \
  "ds_read_b128 a[232:235], v247 offset:8192\n" \
  "v_add_f32 v225, v225, v133\n" \
  "v_cvt_pk_bf16_f32 v130, v132, v133\n" \
  "v_mfma_f32_32x32x16_bf16 v[96:111], a[124:127], v[168:171], v[96:111]\n" \
  "buffer_load_dwordx4 v238, %[srdV], s40 offen offset:3072 lds\n" \
  "ds_read_b128 a[236:239], v248 offset:8192\n" \
  "v_exp_f32 v136, v136\n" \
  "v_add_f32 v224, v224, v134\n" \
  "v_exp_f32 v137, v137\n" \
  "v_mfma_f32_32x32x16_bf16 v[16:31], a[96:99], v[176:179], v[16:31]\n" \
  "ds_read_b128 a[240:243], v249 offset:8192\n" \
  "v_add_f32 v225, v225, v135\n" \
  "v_cvt_pk_bf16_f32 v131, v134, v135\n" \
  "v_mfma_f32_32x32x16_bf16 v[48:63], a[100:103], v[176:179], v[48:63]\n" \
  "ds_read_b128 a[244:247], v250 offset:8192\n" \
  "v_exp_f32 v138, v138\n" \
  "v_add_f32 v224, v224, v136\n" \
  "v_exp_f32 v139, v139\n" \
  "v_mfma_f32_32x32x16_bf16 v[80:95], a[104:107], v[176:179], v[80:95]\n" \
  "ds_read_b128 a[248:251], v251 offset:8192\n" \
  "v_add_f32 v225, v225, v137\n" \
  "v_cvt_pk_bf16_f32 v136, v136, v137\n" \
  "v_mfma_f32_32x32x16_bf16 v[112:127], a[108:111], v[176:179], v[112:127]\n" \
  "ds_read_b128 a[252:255], v252 offset:8192\n" \
  "v_exp_f32 v140, v140\n" \
  "v_add_f32 v224, v224, v138\n" \
  "v_exp_f32 v141, v141\n" \
  "v_mfma_f32_32x32x16_bf16 v[16:31], a[112:115], v[184:187], v[16:31]\n" \
  "v_add_f32 v225, v225, v139\n" \
  "v_cvt_pk_bf16_f32 v137, v138, v139\n" \
  "v_mfma_f32_32x32x16_bf16 v[48:63], a[116:119], v[184:187], v[48:63]\n" \
  "v_exp_f32 v142, v142\n" \
  "v_add_f32 v224, v224, v140\n" \
  "v_exp_f32 v143, v143\n" \
  "v_mfma_f32_32x32x16_bf16 v[80:95], a[120:123], v[184:187], v[80:95]\n" \
  "v_add_f32 v225, v225, v141\n" \
  "v_cvt_pk_bf16_f32 v138, v140, v141\n" \
  "v_mfma_f32_32x32x16_bf16 v[112:127], a[124:127], v[184:187], v[112:127]\n" \
  "v_add_f32 v224, v224, v142\n" \
  "v_add_f32 v225, v225, v143\n" \
  "v_cvt_pk_bf16_f32 v139, v142, v143\n" \
  "s_waitcnt lgkmcnt(1)\n" \
  "v_mfma_f32_32x32x16_bf16 v[160:175], a[224:227], a[0:3], v[192:207]\n" \
  "v_exp_f32 v144, v144\n" \
  "v_exp_f32 v145, v145\n" \
  "v_mfma_f32_32x32x16_bf16 v[160:175], a[228:231], a[4:7], v[160:175]\n" \
  "ds_read_b64_tr_b16 a[96:97], v243 offset:40960\n" \
  "v_exp_f32 v146, v146\n" \
  "v_add_f32 v226, v226, v144\n" \
  "v_exp_f32 v147, v147\n" \
  "v_mfma_f32_32x32x16_bf16 v[160:175], a[232:235], a[8:11], v[160:175]\n" \
  "ds_read_b64_tr_b16 a[98:99], v243 offset:43008\n" \
  "v_add_f32 v227, v227, v145\n" \
  "v_cvt_pk_bf16_f32 v144, v144, v145\n" \
  "v_mfma_f32_32x32x16_bf16 v[160:175], a[236:239], a[12:15], v[160:175]\n" \
  "ds_read_b64_tr_b16 a[100:101], v243 offset:41472\n" \
  "v_exp_f32 v148, v148\n" \
  "v_add_f32 v226, v226, v146\n" \
  "v_exp_f32 v149, v149\n" \
  "v_mfma_f32_32x32x16_bf16 v[160:175], a[240:243], a[16:19], v[160:175]\n" \
  "ds_read_b64_tr_b16 a[102:103], v243 offset:43520\n" \
  "v_add_f32 v227, v227, v147\n" \
  "v_cvt_pk_bf16_f32 v145, v146, v147\n" \
  "v_mfma_f32_32x32x16_bf16 v[160:175], a[244:247], a[20:23], v[160:175]\n" \
  "ds_read_b64_tr_b16 a[104:105], v243 offset:41984\n" \
  "v_exp_f32 v150, v150\n" \
  "v_add_f32 v226, v226, v148\n" \
  "v_exp_f32 v151, v151\n" \
  "v_mfma_f32_32x32x16_bf16 v[160:175], a[248:251], a[24:27], v[160:175]\n" \
  "ds_read_b64_tr_b16 a[106:107], v243 offset:44032\n" \
  "v_add_f32 v227, v227, v149\n" \
  "v_cvt_pk_bf16_f32 v146, v148, v149\n" \
  "s_waitcnt lgkmcnt(5)\n" \
  "v_mfma_f32_32x32x16_bf16 v[160:175], a[252:255], a[28:31], v[160:175]\n" \
  "v_exp_f32 v152, v152\n" \
  "v_add_f32 v226, v226, v150\n" \
  "v_exp_f32 v153, v153\n" \
  "v_mfma_f32_32x32x16_bf16 v[176:191], a[224:227], a[32:35], v[208:223]\n" \
  "ds_read_b64_tr_b16 a[108:109], v243 offset:42496\n" \
  "v_add_f32 v227, v227, v151\n" \
  "v_cvt_pk_bf16_f32 v147, v150, v151\n" \
  "v_mfma_f32_32x32x16_bf16 v[176:191], a[228:231], a[36:39], v[176:191]\n" \
  "ds_read_b64_tr_b16 a[110:111], v243 offset:44544\n" \
  "v_exp_f32 v154, v154\n" \
  "v_add_f32 v226, v226, v152\n" \
  "v_exp_f32 v155, v155\n" \
  "v_mfma_f32_32x32x16_bf16 v[176:191], a[232:235], a[40:43], v[176:191]\n" \
  "ds_read_b64_tr_b16 a[112:113], v243 offset:45056\n" \
  "v_add_f32 v227, v227, v153\n" \
  "v_cvt_pk_bf16_f32 v152, v152, v153\n" \
  "v_mfma_f32_32x32x16_bf16 v[176:191], a[236:239], a[44:47], v[176:191]\n" \
  "ds_read_b64_tr_b16 a[114:115], v243 offset:47104\n" \
  "v_exp_f32 v156, v156\n" \
  "v_add_f32 v226, v226, v154\n" \
  "v_exp_f32 v157, v157\n" \
  "v_mfma_f32_32x32x16_bf16 v[176:191], a[240:243], a[48:51], v[176:191]\n" \
  "ds_read_b64_tr_b16 a[116:117], v243 offset:45568\n" \
  "v_add_f32 v227, v227, v155\n" \
  "v_cvt_pk_bf16_f32 v153, v154, v155\n" \
  "v_mfma_f32_32x32x16_bf16 v[176:191], a[244:247], a[52:55], v[176:191]\n" \
  "ds_read_b64_tr_b16 a[118:119], v243 offset:47616\n" \
  "v_exp_f32 v158, v158\n" \
  "v_add_f32 v226, v226, v156\n" \
  "v_exp_f32 v159, v159\n" \
  "v_mfma_f32_32x32x16_bf16 v[176:191], a[248:251], a[56:59], v[176:191]\n" \
  "v_add_f32 v227, v227, v157\n" \
  "v_cvt_pk_bf16_f32 v154, v156, v157\n" \
  "v_mfma_f32_32x32x16_bf16 v[176:191], a[252:255], a[60:63], v[176:191]\n" \
  "ds_read_b64_tr_b16 a[120:121], v243 offset:46080\n" \
  "v_add_f32 v226, v226, v158\n" \
  "v_add_f32 v227, v227, v159\n" \
  "v_cvt_pk_bf16_f32 v155, v158, v159\n" \
  "v_mfma_f32_32x32x16_bf16 v[0:15], a[64:67], v[128:131], v[0:15]\n" \
  "ds_read_b64_tr_b16 a[122:123], v243 offset:48128\n" \
  "v_exp_f32 v160, v160\n" \
  "v_exp_f32 v161, v161\n" \
  "v_mfma_f32_32x32x16_bf16 v[32:47], a[68:71], v[128:131], v[32:47]\n" \
  "ds_read_b64_tr_b16 a[124:125], v243 offset:46592\n" \
  "v_exp_f32 v162, v162\n" \
  "v_add_f32 v224, v224, v160\n" \
  "v_exp_f32 v163, v163\n" \
  "v_mfma_f32_32x32x16_bf16 v[64:79], a[72:75], v[128:131], v[64:79]\n" \
  "ds_read_b64_tr_b16 a[126:127], v243 offset:48640\n" \
  "v_add_f32 v225, v225, v161\n" \
  "v_cvt_pk_bf16_f32 v160, v160, v161\n" \
  "v_mfma_f32_32x32x16_bf16 v[96:111], a[76:79], v[128:131], v[96:111]\n" \
  "ds_read_b128 a[192:195], v245 offset:16384\n" \
  "v_exp_f32 v164, v164\n" \
  "v_add_f32 v224, v224, v162\n" \
  "v_exp_f32 v165, v165\n" \
  "v_mfma_f32_32x32x16_bf16 v[0:15], a[80:83], v[136:139], v[0:15]\n" \
  "ds_read_b128 a[196:199], v246 offset:16384\n" \
  "v_add_f32 v225, v225, v163\n" \
  "v_cvt_pk_bf16_f32 v161, v162, v163\n" \
  "v_mfma_f32_32x32x16_bf16 v[32:47], a[84:87], v[136:139], v[32:47]\n" \
  "v_exp_f32 v166, v166\n" \
  "v_add_f32 v224, v224, v164\n" \
  "v_exp_f32 v167, v167\n" \
  "v_mfma_f32_32x32x16_bf16 v[64:79], a[88:91], v[136:139], v[64:79]\n" \
  "ds_read_b128 a[200:203], v247 offset:16384\n" \
  "v_add_f32 v225, v225, v165\n" \
  "v_cvt_pk_bf16_f32 v162, v164, v165\n" \
  "v_mfma_f32_32x32x16_bf16 v[96:111], a[92:95], v[136:139], v[96:111]\n" \
  "ds_read_b128 a[204:207], v248 offset:16384\n" \
  "v_exp_f32 v168, v168\n" \
  "v_add_f32 v224, v224, v166\n" \
  "v_exp_f32 v169, v169\n" \
  "v_mfma_f32_32x32x16_bf16 v[16:31], a[64:67], v[144:147], v[16:31]\n" \
  "ds_read_b128 a[208:211], v249 offset:16384\n" \
  "v_add_f32 v225, v225, v167\n" \
  "v_cvt_pk_bf16_f32 v163, v166, v167\n" \
  "v_mfma_f32_32x32x16_bf16 v[48:63], a[68:71], v[144:147], v[48:63]\n" \
  "ds_read_b128 a[212:215], v250 offset:16384\n" \
  "v_exp_f32 v170, v170\n" \
  "v_add_f32 v224, v224, v168\n" \
  "v_exp_f32 v171, v171\n" \
  "v_mfma_f32_32x32x16_bf16 v[80:95], a[72:75], v[144:147], v[80:95]\n" \
  "ds_read_b128 a[216:219], v251 offset:16384\n" \
  "v_add_f32 v225, v225, v169\n" \
  "v_cvt_pk_bf16_f32 v168, v168, v169\n" \
  "v_mfma_f32_32x32x16_bf16 v[112:127], a[76:79], v[144:147], v[112:127]\n" \
  "ds_read_b128 a[220:223], v252 offset:16384\n" \
  "v_exp_f32 v172, v172\n" \
  "v_add_f32 v224, v224, v170\n" \
  "v_exp_f32 v173, v173\n" \
  "v_mfma_f32_32x32x16_bf16 v[16:31], a[80:83], v[152:155], v[16:31]\n" \
  "v_add_f32 v225, v225, v171\n" \
  "v_cvt_pk_bf16_f32 v169, v170, v171\n" \
  "v_mfma_f32_32x32x16_bf16 v[48:63], a[84:87], v[152:155], v[48:63]\n" \
  "v_exp_f32 v174, v174\n" \
  "v_add_f32 v224, v224, v172\n" \
  "v_exp_f32 v175, v175\n" \
  "v_mfma_f32_32x32x16_bf16 v[80:95], a[88:91], v[152:155], v[80:95]\n" \
  "v_add_f32 v225, v225, v173\n" \
  "v_cvt_pk_bf16_f32 v170, v172, v173\n" \
  "v_mfma_f32_32x32x16_bf16 v[112:127], a[92:95], v[152:155], v[112:127]\n" \
  "v_add_f32 v224, v224, v174\n" \
  "v_add_f32 v225, v225, v175\n" \
  "v_cvt_pk_bf16_f32 v171, v174, v175\n" \
  "s_waitcnt lgkmcnt(0)\n" \
  "s_waitcnt vmcnt(8)\n" \
  "s_barrier\n" \
  "v_mfma_f32_32x32x16_bf16 v[128:143], a[192:195], a[0:3], v[192:207]\n" \
  "v_exp_f32 v176, v176\n" \
  "v_exp_f32 v177, v177\n" \
  "v_mfma_f32_32x32x16_bf16 v[128:143], a[196:199], a[4:7], v[128:143]\n" \
  "ds_read_b64_tr_b16 a[64:65], v243 offset:49152\n" \
  "v_exp_f32 v178, v178\n" \
  "v_add_f32 v226, v226, v176\n" \
  "v_exp_f32 v179, v179\n" \
  "v_mfma_f32_32x32x16_bf16 v[128:143], a[200:203], a[8:11], v[128:143]\n" \
  "ds_read_b64_tr_b16 a[66:67], v243 offset:51200\n" \
  "v_add_f32 v227, v227, v177\n" \
  "v_cvt_pk_bf16_f32 v176, v176, v177\n" \
  "v_mfma_f32_32x32x16_bf16 v[128:143], a[204:207], a[12:15], v[128:143]\n" \
  "s_add_u32 m0, s60, 147456\n" \
  "ds_read_b64_tr_b16 a[68:69], v243 offset:49664\n" \
  "v_exp_f32 v180, v180\n" \
  "v_add_f32 v226, v226, v178\n" \
  "v_exp_f32 v181, v181\n" \
  "v_mfma_f32_32x32x16_bf16 v[128:143], a[208:211], a[16:19], v[128:143]\n" \
  "ds_read_b64_tr_b16 a[70:71], v243 offset:51712\n" \
  "v_add_f32 v227, v227, v179\n" \
  "v_cvt_pk_bf16_f32 v177, v178, v179\n" \
  "v_mfma_f32_32x32x16_bf16 v[128:143], a[212:215], a[20:23], v[128:143]\n" \
  "ds_read_b64_tr_b16 a[72:73], v243 offset:50176\n" \
  "v_exp_f32 v182, v182\n" \
  "v_add_f32 v226, v226, v180\n" \
  "v_exp_f32 v183, v183\n" \
  "v_mfma_f32_32x32x16_bf16 v[128:143], a[216:219], a[24:27], v[128:143]\n" \
  "ds_read_b64_tr_b16 a[74:75], v243 offset:52224\n" \
  "v_add_f32 v227, v227, v181\n" \
  "v_cvt_pk_bf16_f32 v178, v180, v181\n" \
  "v_mfma_f32_32x32x16_bf16 v[128:143], a[220:223], a[28:31], v[128:143]\n" \
  "s_mov_b32 s40, 114688\n" \
  "v_exp_f32 v184, v184\n" \
  "v_add_f32 v226, v226, v182\n" \
  "v_exp_f32 v185, v185\n" \
  "v_mfma_f32_32x32x16_bf16 v[144:159], a[192:195], a[32:35], v[208:223]\n" \
  "ds_read_b64_tr_b16 a[76:77], v243 offset:50688\n" \
  "v_add_f32 v227, v227, v183\n" \
  "v_cvt_pk_bf16_f32 v179, v182, v183\n" \
  "v_mfma_f32_32x32x16_bf16 v[144:159], a[196:199], a[36:39], v[144:159]\n" \
  "ds_read_b64_tr_b16 a[78:79], v243 offset:52736\n" \
  "v_exp_f32 v186, v186\n" \
  "v_add_f32 v226, v226, v184\n" \
  "v_exp_f32 v187, v187\n" \
  "v_mfma_f32_32x32x16_bf16 v[144:159], a[200:203], a[40:43], v[144:159]\n" \
  "ds_read_b64_tr_b16 a[80:81], v243 offset:53248\n" \
  "v_add_f32 v227, v227, v185\n" \
  "v_cvt_pk_bf16_f32 v184, v184, v185\n" \
  "v_mfma_f32_32x32x16_bf16 v[144:159], a[204:207], a[44:47], v[144:159]\n" \
  "buffer_load_dwordx4 v237, %[srdV], s40 offen offset:0 lds\n" \
  "ds_read_b64_tr_b16 a[82:83], v243 offset:55296\n" \
  "v_exp_f32 v188, v188\n" \
  "v_add_f32 v226, v226, v186\n" \
  "v_exp_f32 v189, v189\n" \
  "v_mfma_f32_32x32x16_bf16 v[144:159], a[208:211], a[48:51], v[144:159]\n" \
  "ds_read_b64_tr_b16 a[84:85], v243 offset:53760\n" \
  "v_add_f32 v227, v227, v187\n" \
  "v_cvt_pk_bf16_f32 v185, v186, v187\n" \
  "v_mfma_f32_32x32x16_bf16 v[144:159], a[212:215], a[52:55], v[144:159]\n" \
  "ds_read_b64_tr_b16 a[86:87], v243 offset:55808\n" \
  "v_exp_f32 v190, v190\n" \
  "v_add_f32 v226, v226, v188\n" \
  "v_exp_f32 v191, v191\n" \
  "v_mfma_f32_32x32x16_bf16 v[144:159], a[216:219], a[56:59], v[144:159]\n" \
  "v_add_f32 v227, v227, v189\n" \
  "v_cvt_pk_bf16_f32 v186, v188, v189\n" \
  "v_mfma_f32_32x32x16_bf16 v[144:159], a[220:223], a[60:63], v[144:159]\n" \
  "buffer_load_dwordx4 v238, %[srdV], s40 offen offset:1024 lds\n" \
  "ds_read_b64_tr_b16 a[88:89], v243 offset:54272\n" \
  "v_add_f32 v226, v226, v190\n" \
  "v_add_f32 v227, v227, v191\n" \
  "v_cvt_pk_bf16_f32 v187, v190, v191\n" \
  "v_mfma_f32_32x32x16_bf16 v[0:15], a[96:99], v[160:163], v[0:15]\n" \
  "ds_read_b64_tr_b16 a[90:91], v243 offset:56320\n" \
  "v_exp_f32 v128, v128\n" \
  "v_exp_f32 v129, v129\n" \
  "v_mfma_f32_32x32x16_bf16 v[32:47], a[100:103], v[160:163], v[32:47]\n" \
  "ds_read_b64_tr_b16 a[92:93], v243 offset:54784\n" \
  "v_exp_f32 v130, v130\n" \
  "v_add_f32 v224, v224, v128\n" \
  "v_exp_f32 v131, v131\n" \
  "v_mfma_f32_32x32x16_bf16 v[64:79], a[104:107], v[160:163], v[64:79]\n" \
  "ds_read_b64_tr_b16 a[94:95], v243 offset:56832\n" \
  "v_add_f32 v225, v225, v129\n" \
  "v_cvt_pk_bf16_f32 v128, v128, v129\n" \
  "v_mfma_f32_32x32x16_bf16 v[96:111], a[108:111], v[160:163], v[96:111]\n" \
  "buffer_load_dwordx4 v237, %[srdV], s40 offen offset:2048 lds\n" \
  "ds_read_b128 a[224:227], v245 offset:24576\n" \
  "v_exp_f32 v132, v132\n" \
  "v_add_f32 v224, v224, v130\n" \
  "v_exp_f32 v133, v133\n" \
  "v_mfma_f32_32x32x16_bf16 v[0:15], a[112:115], v[168:171], v[0:15]\n" \
  "ds_read_b128 a[228:231], v246 offset:24576\n" \
  "v_add_f32 v225, v225, v131\n" \
  "v_cvt_pk_bf16_f32 v129, v130, v131\n" \
  "v_mfma_f32_32x32x16_bf16 v[32:47], a[116:119], v[168:171], v[32:47]\n" \
  "v_exp_f32 v134, v134\n" \
  "v_add_f32 v224, v224, v132\n" \
  "v_exp_f32 v135, v135\n" \
  "v_mfma_f32_32x32x16_bf16 v[64:79], a[120:123], v[168:171], v[64:79]\n" \
  "ds_read_b128 a[232:235], v247 offset:24576\n" \
  "v_add_f32 v225, v225, v133\n" \
  "v_cvt_pk_bf16_f32 v130, v132, v133\n" \
  "v_mfma_f32_32x32x16_bf16 v[96:111], a[124:127], v[168:171], v[96:111]\n" \
  "buffer_load_dwordx4 v238, %[srdV], s40 offen offset:3072 lds\n" \
  "ds_read_b128 a[236:239], v248 offset:24576\n" \
  "v_exp_f32 v136, v136\n" \
  "v_add_f32 v224, v224, v134\n" \
  "v_exp_f32 v137, v137\n" \
  "v_mfma_f32_32x32x16_bf16 v[16:31], a[96:99], v[176:179], v[16:31]\n" \
  "ds_read_b128 a[240:243], v249 offset:24576\n" \
  "v_add_f32 v225, v225, v135\n" \
  "v_cvt_pk_bf16_f32 v131, v134, v135\n" \
  "v_mfma_f32_32x32x16_bf16 v[48:63], a[100:103], v[176:179], v[48:63]\n" \
  "ds_read_b128 a[244:247], v250 offset:24576\n" \
  "v_exp_f32 v138, v138\n" \
  "v_add_f32 v224, v224, v136\n" \
  "v_exp_f32 v139, v139\n" \
  "v_mfma_f32_32x32x16_bf16 v[80:95], a[104:107], v[176:179], v[80:95]\n" \
  "ds_read_b128 a[248:251], v251 offset:24576\n" \
  "v_add_f32 v225, v225, v137\n" \
  "v_cvt_pk_bf16_f32 v136, v136, v137\n" \
  "v_mfma_f32_32x32x16_bf16 v[112:127], a[108:111], v[176:179], v[112:127]\n" \
  "ds_read_b128 a[252:255], v252 offset:24576\n" \
  "v_exp_f32 v140, v140\n" \
  "v_add_f32 v224, v224, v138\n" \
  "v_exp_f32 v141, v141\n" \
  "v_mfma_f32_32x32x16_bf16 v[16:31], a[112:115], v[184:187], v[16:31]\n" \
  "v_add_f32 v225, v225, v139\n" \
  "v_cvt_pk_bf16_f32 v137, v138, v139\n" \
  "v_mfma_f32_32x32x16_bf16 v[48:63], a[116:119], v[184:187], v[48:63]\n" \
  "v_exp_f32 v142, v142\n" \
  "v_add_f32 v224, v224, v140\n" \
  "v_exp_f32 v143, v143\n" \
  "v_mfma_f32_32x32x16_bf16 v[80:95], a[120:123], v[184:187], v[80:95]\n" \
  "v_add_f32 v225, v225, v141\n" \
  "v_cvt_pk_bf16_f32 v138, v140, v141\n" \
  "v_mfma_f32_32x32x16_bf16 v[112:127], a[124:127], v[184:187], v[112:127]\n" \
  "v_add_f32 v224, v224, v142\n" \
  "v_add_f32 v225, v225, v143\n" \
  "v_cvt_pk_bf16_f32 v139, v142, v143\n" \
  "s_waitcnt lgkmcnt(1)\n" \
  "v_mfma_f32_32x32x16_bf16 v[160:175], a[224:227], a[0:3], v[192:207]\n" \
  "v_exp_f32 v144, v144\n" \
  "v_exp_f32 v145, v145\n" \
  "v_mfma_f32_32x32x16_bf16 v[160:175], a[228:231], a[4:7], v[160:175]\n" \
  "ds_read_b64_tr_b16 a[96:97], v243 offset:57344\n" \
  "v_exp_f32 v146, v146\n" \
  "v_add_f32 v226, v226, v144\n" \
  "v_exp_f32 v147, v147\n" \
  "v_mfma_f32_32x32x16_bf16 v[160:175], a[232:235], a[8:11], v[160:175]\n" \
  "ds_read_b64_tr_b16 a[98:99], v243 offset:59392\n" \
  "v_add_f32 v227, v227, v145\n" \
  "v_cvt_pk_bf16_f32 v144, v144, v145\n" \
  "v_mfma_f32_32x32x16_bf16 v[160:175], a[236:239], a[12:15], v[160:175]\n" \
  "ds_read_b64_tr_b16 a[100:101], v243 offset:57856\n" \
  "v_exp_f32 v148, v148\n" \
  "v_add_f32 v226, v226, v146\n" \
  "v_exp_f32 v149, v149\n" \
  "v_mfma_f32_32x32x16_bf16 v[160:175], a[240:243], a[16:19], v[160:175]\n" \
  "ds_read_b64_tr_b16 a[102:103], v243 offset:59904\n" \
  "v_add_f32 v227, v227, v147\n" \
  "v_cvt_pk_bf16_f32 v145, v146, v147\n" \
  "v_mfma_f32_32x32x16_bf16 v[160:175], a[244:247], a[20:23], v[160:175]\n" \
  "ds_read_b64_tr_b16 a[104:105], v243 offset:58368\n" \
  "v_exp_f32 v150, v150\n" \
  "v_add_f32 v226, v226, v148\n" \
  "v_exp_f32 v151, v151\n" \
  "v_mfma_f32_32x32x16_bf16 v[160:175], a[248:251], a[24:27], v[160:175]\n" \
  "ds_read_b64_tr_b16 a[106:107], v243 offset:60416\n" \
  "v_add_f32 v227, v227, v149\n" \
  "v_cvt_pk_bf16_f32 v146, v148, v149\n" \
  "s_waitcnt lgkmcnt(5)\n" \
  "v_mfma_f32_32x32x16_bf16 v[160:175], a[252:255], a[28:31], v[160:175]\n" \
  "v_exp_f32 v152, v152\n" \
  "v_add_f32 v226, v226, v150\n" \
  "v_exp_f32 v153, v153\n" \
  "v_mfma_f32_32x32x16_bf16 v[176:191], a[224:227], a[32:35], v[208:223]\n" \
  "ds_read_b64_tr_b16 a[108:109], v243 offset:58880\n" \
  "v_add_f32 v227, v227, v151\n" \
  "v_cvt_pk_bf16_f32 v147, v150, v151\n" \
  "v_mfma_f32_32x32x16_bf16 v[176:191], a[228:231], a[36:39], v[176:191]\n" \
  "ds_read_b64_tr_b16 a[110:111], v243 offset:60928\n" \
  "v_exp_f32 v154, v154\n" \
  "v_add_f32 v226, v226, v152\n" \
  "v_exp_f32 v155, v155\n" \
  "v_mfma_f32_32x32x16_bf16 v[176:191], a[232:235], a[40:43], v[176:191]\n" \
  "ds_read_b64_tr_b16 a[112:113], v243 offset:61440\n" \
  "v_add_f32 v227, v227, v153\n" \
  "v_cvt_pk_bf16_f32 v152, v152, v153\n" \
  "v_mfma_f32_32x32x16_bf16 v[176:191], a[236:239], a[44:47], v[176:191]\n" \
  "ds_read_b64_tr_b16 a[114:115], v243 offset:63488\n" \
  "v_exp_f32 v156, v156\n" \
  "v_add_f32 v226, v226, v154\n" \
  "v_exp_f32 v157, v157\n" \
  "v_mfma_f32_32x32x16_bf16 v[176:191], a[240:243], a[48:51], v[176:191]\n" \
  "ds_read_b64_tr_b16 a[116:117], v243 offset:61952\n" \
  "v_add_f32 v227, v227, v155\n" \
  "v_cvt_pk_bf16_f32 v153, v154, v155\n" \
  "v_mfma_f32_32x32x16_bf16 v[176:191], a[244:247], a[52:55], v[176:191]\n" \
  "ds_read_b64_tr_b16 a[118:119], v243 offset:64000\n" \
  "v_exp_f32 v158, v158\n" \
  "v_add_f32 v226, v226, v156\n" \
  "v_exp_f32 v159, v159\n" \
  "v_mfma_f32_32x32x16_bf16 v[176:191], a[248:251], a[56:59], v[176:191]\n" \
  "v_add_f32 v227, v227, v157\n" \
  "v_cvt_pk_bf16_f32 v154, v156, v157\n" \
  "v_mfma_f32_32x32x16_bf16 v[176:191], a[252:255], a[60:63], v[176:191]\n" \
  "ds_read_b64_tr_b16 a[120:121], v243 offset:62464\n" \
  "v_add_f32 v226, v226, v158\n" \
  "v_add_f32 v227, v227, v159\n" \
  "v_cvt_pk_bf16_f32 v155, v158, v159\n" \
  "v_mfma_f32_32x32x16_bf16 v[0:15], a[64:67], v[128:131], v[0:15]\n" \
  "ds_read_b64_tr_b16 a[122:123], v243 offset:64512\n" \
  "v_exp_f32 v160, v160\n" \
  "v_exp_f32 v161, v161\n" \
  "v_mfma_f32_32x32x16_bf16 v[32:47], a[68:71], v[128:131], v[32:47]\n" \
  "ds_read_b64_tr_b16 a[124:125], v243 offset:62976\n" \
  "v_exp_f32 v162, v162\n" \
  "v_add_f32 v224, v224, v160\n" \
  "v_exp_f32 v163, v163\n" \
  "v_mfma_f32_32x32x16_bf16 v[64:79], a[72:75], v[128:131], v[64:79]\n" \
  "ds_read_b64_tr_b16 a[126:127], v243 offset:65024\n" \
  "v_add_f32 v225, v225, v161\n" \
  "v_cvt_pk_bf16_f32 v160, v160, v161\n" \
  "v_mfma_f32_32x32x16_bf16 v[96:111], a[76:79], v[128:131], v[96:111]\n" \
  "ds_read_b128 a[192:195], v245 offset:32768\n" \
  "v_exp_f32 v164, v164\n" \
  "v_add_f32 v224, v224, v162\n" \
  "v_exp_f32 v165, v165\n" \
  "v_mfma_f32_32x32x16_bf16 v[0:15], a[80:83], v[136:139], v[0:15]\n" \
  "ds_read_b128 a[196:199], v246 offset:32768\n" \
  "v_add_f32 v225, v225, v163\n" \
  "v_cvt_pk_bf16_f32 v161, v162, v163\n" \
  "v_mfma_f32_32x32x16_bf16 v[32:47], a[84:87], v[136:139], v[32:47]\n" \
  "v_exp_f32 v166, v166\n" \
  "v_add_f32 v224, v224, v164\n" \
  "v_exp_f32 v167, v167\n" \
  "v_mfma_f32_32x32x16_bf16 v[64:79], a[88:91], v[136:139], v[64:79]\n" \
  "ds_read_b128 a[200:203], v247 offset:32768\n" \
  "v_add_f32 v225, v225, v165\n" \
  "v_cvt_pk_bf16_f32 v162, v164, v165\n" \
  "v_mfma_f32_32x32x16_bf16 v[96:111], a[92:95], v[136:139], v[96:111]\n" \
  "ds_read_b128 a[204:207], v248 offset:32768\n" \
  "v_exp_f32 v168, v168\n" \
  "v_add_f32 v224, v224, v166\n" \
  "v_exp_f32 v169, v169\n" \
  "v_mfma_f32_32x32x16_bf16 v[16:31], a[64:67], v[144:147], v[16:31]\n" \
  "ds_read_b128 a[208:211], v249 offset:32768\n" \
  "v_add_f32 v225, v225, v167\n" \
  "v_cvt_pk_bf16_f32 v163, v166, v167\n" \
  "v_mfma_f32_32x32x16_bf16 v[48:63], a[68:71], v[144:147], v[48:63]\n" \
  "ds_read_b128 a[212:215], v250 offset:32768\n" \
  "v_exp_f32 v170, v170\n" \
  "v_add_f32 v224, v224, v168\n" \
  "v_exp_f32 v171, v171\n" \
  "v_mfma_f32_32x32x16_bf16 v[80:95], a[72:75], v[144:147], v[80:95]\n" \
  "ds_read_b128 a[216:219], v251 offset:32768\n" \
  "v_add_f32 v225, v225, v169\n" \
  "v_cvt_pk_bf16_f32 v168, v168, v169\n" \
  "v_mfma_f32_32x32x16_bf16 v[112:127], a[76:79], v[144:147], v[112:127]\n" \
  "ds_read_b128 a[220:223], v252 offset:32768\n" \
  "v_exp_f32 v172, v172\n" \
  "v_add_f32 v224, v224, v170\n" \
  "v_exp_f32 v173, v173\n" \
  "v_mfma_f32_32x32x16_bf16 v[16:31], a[80:83], v[152:155], v[16:31]\n" \
  "v_add_f32 v225, v225, v171\n" \
  "v_cvt_pk_bf16_f32 v169, v170, v171\n" \
  "v_mfma_f32_32x32x16_bf16 v[48:63], a[84:87], v[152:155], v[48:63]\n" \
  "v_exp_f32 v174, v174\n" \
  "v_add_f32 v224, v224, v172\n" \
  "v_exp_f32 v175, v175\n" \
  "v_mfma_f32_32x32x16_bf16 v[80:95], a[88:91], v[152:155], v[80:95]\n" \
  "v_add_f32 v225, v225, v173\n" \
  "v_cvt_pk_bf16_f32 v170, v172, v173\n" \
  "v_mfma_f32_32x32x16_bf16 v[112:127], a[92:95], v[152:155], v[112:127]\n" \
  "v_add_f32 v224, v224, v174\n" \
  "v_add_f32 v225, v225, v175\n" \
  "v_cvt_pk_bf16_f32 v171, v174, v175\n" \
  "s_waitcnt lgkmcnt(0)\n" \
  "s_waitcnt vmcnt(4)\n" \
  "s_barrier\n" \
  "v_mfma_f32_32x32x16_bf16 v[128:143], a[192:195], a[0:3], v[192:207]\n" \
  "v_exp_f32 v176, v176\n" \
  "v_exp_f32 v177, v177\n" \
  "v_mfma_f32_32x32x16_bf16 v[128:143], a[196:199], a[4:7], v[128:143]\n" \
  "s_add_u32 m0, s61, 81920\n" \
  "ds_read_b64_tr_b16 a[64:65], v244 offset:0\n" \
  "v_exp_f32 v178, v178\n" \
  "v_add_f32 v226, v226, v176\n" \
  "v_exp_f32 v179, v179\n" \
  "v_mfma_f32_32x32x16_bf16 v[128:143], a[200:203], a[8:11], v[128:143]\n" \
  "s_mov_b32 s40, 0\n" \
  "ds_read_b64_tr_b16 a[66:67], v244 offset:2048\n" \
  "v_add_f32 v227, v227, v177\n" \
  "v_cvt_pk_bf16_f32 v176, v176, v177\n" \
  "v_mfma_f32_32x32x16_bf16 v[128:143], a[204:207], a[12:15], v[128:143]\n" \
  "ds_read_b64_tr_b16 a[68:69], v244 offset:512\n" \
  "v_exp_f32 v180, v180\n" \
  "v_add_f32 v226, v226, v178\n" \
  "v_exp_f32 v181, v181\n" \
  "v_mfma_f32_32x32x16_bf16 v[128:143], a[208:211], a[16:19], v[128:143]\n" \
  "buffer_load_dwordx4 v253, %[srdA], s40 offen offset:0 sc1 lds\n" \
  "ds_read_b64_tr_b16 a[70:71], v244 offset:2560\n" \
  "v_add_f32 v227, v227, v179\n" \
  "v_cvt_pk_bf16_f32 v177, v178, v179\n" \
  "v_mfma_f32_32x32x16_bf16 v[128:143], a[212:215], a[20:23], v[128:143]\n" \
  "buffer_load_dwordx4 v253, %[srdA], s40 offen offset:1024 sc1 lds\n" \
  "ds_read_b64_tr_b16 a[72:73], v244 offset:1024\n" \
  "v_exp_f32 v182, v182\n" \
  "v_add_f32 v226, v226, v180\n" \
  "v_exp_f32 v183, v183\n" \
  "v_mfma_f32_32x32x16_bf16 v[128:143], a[216:219], a[24:27], v[128:143]\n" \
  "ds_read_b64_tr_b16 a[74:75], v244 offset:3072\n" \
  "v_add_f32 v227, v227, v181\n" \
  "v_cvt_pk_bf16_f32 v178, v180, v181\n" \
  "v_mfma_f32_32x32x16_bf16 v[128:143], a[220:223], a[28:31], v[128:143]\n" \
  "buffer_load_dwordx4 v253, %[srdA], s40 offen offset:2048 sc1 lds\n" \
  "v_exp_f32 v184, v184\n" \
  "v_add_f32 v226, v226, v182\n" \
  "v_exp_f32 v185, v185\n" \
  "v_mfma_f32_32x32x16_bf16 v[144:159], a[192:195], a[32:35], v[208:223]\n" \
  "buffer_load_dwordx4 v253, %[srdA], s40 offen offset:3072 sc1 lds\n" \
  "ds_read_b64_tr_b16 a[76:77], v244 offset:1536\n" \
  "v_add_f32 v227, v227, v183\n" \
  "v_cvt_pk_bf16_f32 v179, v182, v183\n" \
  "v_mfma_f32_32x32x16_bf16 v[144:159], a[196:199], a[36:39], v[144:159]\n" \
  "ds_read_b64_tr_b16 a[78:79], v244 offset:3584\n" \
  "v_exp_f32 v186, v186\n" \
  "v_add_f32 v226, v226, v184\n" \
  "v_exp_f32 v187, v187\n" \
  "v_mfma_f32_32x32x16_bf16 v[144:159], a[200:203], a[40:43], v[144:159]\n" \
  "s_add_u32 m0, s61, 86016\n" \
  "ds_read_b64_tr_b16 a[80:81], v244 offset:4096\n" \
  "v_add_f32 v227, v227, v185\n" \
  "v_cvt_pk_bf16_f32 v184, v184, v185\n" \
  "v_mfma_f32_32x32x16_bf16 v[144:159], a[204:207], a[44:47], v[144:159]\n" \
  "s_mov_b32 s40, 4096\n" \
  "ds_read_b64_tr_b16 a[82:83], v244 offset:6144\n" \
  "v_exp_f32 v188, v188\n" \
  "v_add_f32 v226, v226, v186\n" \
  "v_exp_f32 v189, v189\n" \
  "v_mfma_f32_32x32x16_bf16 v[144:159], a[208:211], a[48:51], v[144:159]\n" \
  "ds_read_b64_tr_b16 a[84:85], v244 offset:4608\n" \
  "v_add_f32 v227, v227, v187\n" \
  "v_cvt_pk_bf16_f32 v185, v186, v187\n" \
  "v_mfma_f32_32x32x16_bf16 v[144:159], a[212:215], a[52:55], v[144:159]\n" \
  "buffer_load_dwordx4 v253, %[srdA], s40 offen offset:0 sc1 lds\n" \
  "ds_read_b64_tr_b16 a[86:87], v244 offset:6656\n" \
  "v_exp_f32 v190, v190\n" \
  "v_add_f32 v226, v226, v188\n" \
  "v_exp_f32 v191, v191\n" \
  "v_mfma_f32_32x32x16_bf16 v[144:159], a[216:219], a[56:59], v[144:159]\n" \
  "buffer_load_dwordx4 v253, %[srdA], s40 offen offset:1024 sc1 lds\n" \
  "v_add_f32 v227, v227, v189\n" \
  "v_cvt_pk_bf16_f32 v186, v188, v189\n" \
  "v_mfma_f32_32x32x16_bf16 v[144:159], a[220:223], a[60:63], v[144:159]\n" \
  "ds_read_b64_tr_b16 a[88:89], v244 offset:5120\n" \
  "v_add_f32 v226, v226, v190\n" \
  "v_add_f32 v227, v227, v191\n" \
  "v_cvt_pk_bf16_f32 v187, v190, v191\n" \
  "v_mfma_f32_32x32x16_bf16 v[0:15], a[96:99], v[160:163], v[0:15]\n" \
  "buffer_load_dwordx4 v253, %[srdA], s40 offen offset:2048 sc1 lds\n" \
  "ds_read_b64_tr_b16 a[90:91], v244 offset:7168\n" \
  "v_exp_f32 v128, v128\n" \
  "v_exp_f32 v129, v129\n" \
  "v_mfma_f32_32x32x16_bf16 v[32:47], a[100:103], v[160:163], v[32:47]\n" \
  "buffer_load_dwordx4 v253, %[srdA], s40 offen offset:3072 sc1 lds\n" \
  "ds_read_b64_tr_b16 a[92:93], v244 offset:5632\n" \
  "v_exp_f32 v130, v130\n" \
  "v_add_f32 v224, v224, v128\n" \
  "v_exp_f32 v131, v131\n" \
  "v_mfma_f32_32x32x16_bf16 v[64:79], a[104:107], v[160:163], v[64:79]\n" \
  "ds_read_b64_tr_b16 a[94:95], v244 offset:7680\n" \
  "v_add_f32 v225, v225, v129\n" \
  "v_cvt_pk_bf16_f32 v128, v128, v129\n" \
  "v_mfma_f32_32x32x16_bf16 v[96:111], a[108:111], v[160:163], v[96:111]\n" \
  "s_add_u32 m0, s61, 90112\n" \
  "ds_read_b128 a[224:227], v245 offset:40960\n" \
  "v_exp_f32 v132, v132\n" \
  "v_add_f32 v224, v224, v130\n" \
  "v_exp_f32 v133, v133\n" \
  "v_mfma_f32_32x32x16_bf16 v[0:15], a[112:115], v[168:171], v[0:15]\n" \
  "s_mov_b32 s40, 8192\n" \
  "ds_read_b128 a[228:231], v246 offset:40960\n" \
  "v_add_f32 v225, v225, v131\n" \
  "v_cvt_pk_bf16_f32 v129, v130, v131\n" \
  "v_mfma_f32_32x32x16_bf16 v[32:47], a[116:119], v[168:171], v[32:47]\n" \
  "v_exp_f32 v134, v134\n" \
  "v_add_f32 v224, v224, v132\n" \
  "v_exp_f32 v135, v135\n" \
  "v_mfma_f32_32x32x16_bf16 v[64:79], a[120:123], v[168:171], v[64:79]\n" \
  "buffer_load_dwordx4 v253, %[srdA], s40 offen offset:0 sc1 lds\n" \
  "ds_read_b128 a[232:235], v247 offset:40960\n" \
  "v_add_f32 v225, v225, v133\n" \
  "v_cvt_pk_bf16_f32 v130, v132, v133\n" \
  "v_mfma_f32_32x32x16_bf16 v[96:111], a[124:127], v[168:171], v[96:111]\n" \
  "buffer_load_dwordx4 v253, %[srdA], s40 offen offset:1024 sc1 lds\n" \
  "ds_read_b128 a[236:239], v248 offset:40960\n" \
  "v_exp_f32 v136, v136\n" \
  "v_add_f32 v224, v224, v134\n" \
  "v_exp_f32 v137, v137\n" \
  "v_mfma_f32_32x32x16_bf16 v[16:31], a[96:99], v[176:179], v[16:31]\n" \
  "ds_read_b128 a[240:243], v249 offset:40960\n" \
  "v_add_f32 v225, v225, v135\n" \
  "v_cvt_pk_bf16_f32 v131, v134, v135\n" \
  "v_mfma_f32_32x32x16_bf16 v[48:63], a[100:103], v[176:179], v[48:63]\n" \
  "ds_read_b128 a[244:247], v250 offset:40960\n" \
  "v_exp_f32 v138, v138\n" \
  "v_add_f32 v224, v224, v136\n" \
  "v_exp_f32 v139, v139\n" \
  "v_mfma_f32_32x32x16_bf16 v[80:95], a[104:107], v[176:179], v[80:95]\n" \
  "ds_read_b128 a[248:251], v251 offset:40960\n" \
  "v_add_f32 v225, v225, v137\n" \
  "v_cvt_pk_bf16_f32 v136, v136, v137\n" \
  "v_mfma_f32_32x32x16_bf16 v[112:127], a[108:111], v[176:179], v[112:127]\n" \
  "ds_read_b128 a[252:255], v252 offset:40960\n" \
  "v_exp_f32 v140, v140\n" \
  "v_add_f32 v224, v224, v138\n" \
  "v_exp_f32 v141, v141\n" \
  "v_mfma_f32_32x32x16_bf16 v[16:31], a[112:115], v[184:187], v[16:31]\n" \
  "v_add_f32 v225, v225, v139\n" \
  "v_cvt_pk_bf16_f32 v137, v138, v139\n" \
  "v_mfma_f32_32x32x16_bf16 v[48:63], a[116:119], v[184:187], v[48:63]\n" \
  "v_exp_f32 v142, v142\n" \
  "v_add_f32 v224, v224, v140\n" \
  "v_exp_f32 v143, v143\n" \
  "v_mfma_f32_32x32x16_bf16 v[80:95], a[120:123], v[184:187], v[80:95]\n" \
  "v_add_f32 v225, v225, v141\n" \
  "v_cvt_pk_bf16_f32 v138, v140, v141\n" \
  "v_mfma_f32_32x32x16_bf16 v[112:127], a[124:127], v[184:187], v[112:127]\n" \
  "v_add_f32 v224, v224, v142\n" \
  "v_add_f32 v225, v225, v143\n" \
  "v_cvt_pk_bf16_f32 v139, v142, v143\n" \
  "s_waitcnt lgkmcnt(1)\n" \
  "v_mfma_f32_32x32x16_bf16 v[160:175], a[224:227], a[0:3], v[192:207]\n" \
  "v_exp_f32 v144, v144\n" \
  "v_exp_f32 v145, v145\n" \
  "v_mfma_f32_32x32x16_bf16 v[160:175], a[228:231], a[4:7], v[160:175]\n" \
  "ds_read_b64_tr_b16 a[96:97], v244 offset:8192\n" \
  "v_exp_f32 v146, v146\n" \
  "v_add_f32 v226, v226, v144\n" \
  "v_exp_f32 v147, v147\n" \
  "v_mfma_f32_32x32x16_bf16 v[160:175], a[232:235], a[8:11], v[160:175]\n" \
  "ds_read_b64_tr_b16 a[98:99], v244 offset:10240\n" \
  "v_add_f32 v227, v227, v145\n" \
  "v_cvt_pk_bf16_f32 v144, v144, v145\n" \
  "v_mfma_f32_32x32x16_bf16 v[160:175], a[236:239], a[12:15], v[160:175]\n" \
  "ds_read_b64_tr_b16 a[100:101], v244 offset:8704\n" \
  "v_exp_f32 v148, v148\n" \
  "v_add_f32 v226, v226, v146\n" \
  "v_exp_f32 v149, v149\n" \
  "v_mfma_f32_32x32x16_bf16 v[160:175], a[240:243], a[16:19], v[160:175]\n" \
  "ds_read_b64_tr_b16 a[102:103], v244 offset:10752\n" \
  "v_add_f32 v227, v227, v147\n" \
  "v_cvt_pk_bf16_f32 v145, v146, v147\n" \
  "v_mfma_f32_32x32x16_bf16 v[160:175], a[244:247], a[20:23], v[160:175]\n" \
  "ds_read_b64_tr_b16 a[104:105], v244 offset:9216\n" \
  "v_exp_f32 v150, v150\n" \
  "v_add_f32 v226, v226, v148\n" \
  "v_exp_f32 v151, v151\n" \
  "v_mfma_f32_32x32x16_bf16 v[160:175], a[248:251], a[24:27], v[160:175]\n" \
  "ds_read_b64_tr_b16 a[106:107], v244 offset:11264\n" \
  "v_add_f32 v227, v227, v149\n" \
  "v_cvt_pk_bf16_f32 v146, v148, v149\n" \
  "s_waitcnt lgkmcnt(5)\n" \
  "v_mfma_f32_32x32x16_bf16 v[160:175], a[252:255], a[28:31], v[160:175]\n" \
  "v_exp_f32 v152, v152\n" \
  "v_add_f32 v226, v226, v150\n" \
  "v_exp_f32 v153, v153\n" \
  "v_mfma_f32_32x32x16_bf16 v[176:191], a[224:227], a[32:35], v[208:223]\n" \
  "ds_read_b64_tr_b16 a[108:109], v244 offset:9728\n" \
  "v_add_f32 v227, v227, v151\n" \
  "v_cvt_pk_bf16_f32 v147, v150, v151\n" \
  "v_mfma_f32_32x32x16_bf16 v[176:191], a[228:231], a[36:39], v[176:191]\n" \
  "ds_read_b64_tr_b16 a[110:111], v244 offset:11776\n" \
  "v_exp_f32 v154, v154\n" \
  "v_add_f32 v226, v226, v152\n" \
  "v_exp_f32 v155, v155\n" \
  "v_mfma_f32_32x32x16_bf16 v[176:191], a[232:235], a[40:43], v[176:191]\n" \
  "ds_read_b64_tr_b16 a[112:113], v244 offset:12288\n" \
  "v_add_f32 v227, v227, v153\n" \
  "v_cvt_pk_bf16_f32 v152, v152, v153\n" \
  "v_mfma_f32_32x32x16_bf16 v[176:191], a[236:239], a[44:47], v[176:191]\n" \
  "ds_read_b64_tr_b16 a[114:115], v244 offset:14336\n" \
  "v_exp_f32 v156, v156\n" \
  "v_add_f32 v226, v226, v154\n" \
  "v_exp_f32 v157, v157\n" \
  "v_mfma_f32_32x32x16_bf16 v[176:191], a[240:243], a[48:51], v[176:191]\n" \
  "ds_read_b64_tr_b16 a[116:117], v244 offset:12800\n" \
  "v_add_f32 v227, v227, v155\n" \
  "v_cvt_pk_bf16_f32 v153, v154, v155\n" \
  "v_mfma_f32_32x32x16_bf16 v[176:191], a[244:247], a[52:55], v[176:191]\n" \
  "ds_read_b64_tr_b16 a[118:119], v244 offset:14848\n" \
  "v_exp_f32 v158, v158\n" \
  "v_add_f32 v226, v226, v156\n" \
  "v_exp_f32 v159, v159\n" \
  "v_mfma_f32_32x32x16_bf16 v[176:191], a[248:251], a[56:59], v[176:191]\n" \
  "v_add_f32 v227, v227, v157\n" \
  "v_cvt_pk_bf16_f32 v154, v156, v157\n" \
  "v_mfma_f32_32x32x16_bf16 v[176:191], a[252:255], a[60:63], v[176:191]\n" \
  "ds_read_b64_tr_b16 a[120:121], v244 offset:13312\n" \
  "v_add_f32 v226, v226, v158\n" \
  "v_add_f32 v227, v227, v159\n" \
  "v_cvt_pk_bf16_f32 v155, v158, v159\n" \
  "v_mfma_f32_32x32x16_bf16 v[0:15], a[64:67], v[128:131], v[0:15]\n" \
  "ds_read_b64_tr_b16 a[122:123], v244 offset:15360\n" \
  "v_exp_f32 v160, v160\n" \
  "v_exp_f32 v161, v161\n" \
  "v_mfma_f32_32x32x16_bf16 v[32:47], a[68:71], v[128:131], v[32:47]\n" \
  "ds_read_b64_tr_b16 a[124:125], v244 offset:13824\n" \
  "v_exp_f32 v162, v162\n" \
  "v_add_f32 v224, v224, v160\n" \
  "v_exp_f32 v163, v163\n" \
  "v_mfma_f32_32x32x16_bf16 v[64:79], a[72:75], v[128:131], v[64:79]\n" \
  "ds_read_b64_tr_b16 a[126:127], v244 offset:15872\n" \
  "v_add_f32 v225, v225, v161\n" \
  "v_cvt_pk_bf16_f32 v160, v160, v161\n" \
  "v_mfma_f32_32x32x16_bf16 v[96:111], a[76:79], v[128:131], v[96:111]\n" \
  "ds_read_b128 a[192:195], v245 offset:49152\n" \
  "v_exp_f32 v164, v164\n" \
  "v_add_f32 v224, v224, v162\n" \
  "v_exp_f32 v165, v165\n" \
  "v_mfma_f32_32x32x16_bf16 v[0:15], a[80:83], v[136:139], v[0:15]\n" \
  "ds_read_b128 a[196:199], v246 offset:49152\n" \
  "v_add_f32 v225, v225, v163\n" \
  "v_cvt_pk_bf16_f32 v161, v162, v163\n" \
  "v_mfma_f32_32x32x16_bf16 v[32:47], a[84:87], v[136:139], v[32:47]\n" \
  "v_exp_f32 v166, v166\n" \
  "v_add_f32 v224, v224, v164\n" \
  "v_exp_f32 v167, v167\n" \
  "v_mfma_f32_32x32x16_bf16 v[64:79], a[88:91], v[136:139], v[64:79]\n" \
  "ds_read_b128 a[200:203], v247 offset:49152\n" \
  "v_add_f32 v225, v225, v165\n" \
  "v_cvt_pk_bf16_f32 v162, v164, v165\n" \
  "v_mfma_f32_32x32x16_bf16 v[96:111], a[92:95], v[136:139], v[96:111]\n" \
  "ds_read_b128 a[204:207], v248 offset:49152\n" \
  "v_exp_f32 v168, v168\n" \
  "v_add_f32 v224, v224, v166\n" \
  "v_exp_f32 v169, v169\n" \
  "v_mfma_f32_32x32x16_bf16 v[16:31], a[64:67], v[144:147], v[16:31]\n" \
  "ds_read_b128 a[208:211], v249 offset:49152\n" \
  "v_add_f32 v225, v225, v167\n" \
  "v_cvt_pk_bf16_f32 v163, v166, v167\n" \
  "v_mfma_f32_32x32x16_bf16 v[48:63], a[68:71], v[144:147], v[48:63]\n" \
  "ds_read_b128 a[212:215], v250 offset:49152\n" \
  "v_exp_f32 v170, v170\n" \
  "v_add_f32 v224, v224, v168\n" \
  "v_exp_f32 v171, v171\n" \
  "v_mfma_f32_32x32x16_bf16 v[80:95], a[72:75], v[144:147], v[80:95]\n" \
  "ds_read_b128 a[216:219], v251 offset:49152\n" \
  "v_add_f32 v225, v225, v169\n" \
  "v_cvt_pk_bf16_f32 v168, v168, v169\n" \
  "v_mfma_f32_32x32x16_bf16 v[112:127], a[76:79], v[144:147], v[112:127]\n" \
  "ds_read_b128 a[220:223], v252 offset:49152\n" \
  "v_exp_f32 v172, v172\n" \
  "v_add_f32 v224, v224, v170\n" \
  "v_exp_f32 v173, v173\n" \
  "v_mfma_f32_32x32x16_bf16 v[16:31], a[80:83], v[152:155], v[16:31]\n" \
  "v_add_f32 v225, v225, v171\n" \
  "v_cvt_pk_bf16_f32 v169, v170, v171\n" \
  "v_mfma_f32_32x32x16_bf16 v[48:63], a[84:87], v[152:155], v[48:63]\n" \
  "v_exp_f32 v174, v174\n" \
  "v_add_f32 v224, v224, v172\n" \
  "v_exp_f32 v175, v175\n" \
  "v_mfma_f32_32x32x16_bf16 v[80:95], a[88:91], v[152:155], v[80:95]\n" \
  "v_add_f32 v225, v225, v173\n" \
  "v_cvt_pk_bf16_f32 v170, v172, v173\n" \
  "v_mfma_f32_32x32x16_bf16 v[112:127], a[92:95], v[152:155], v[112:127]\n" \
  "v_add_f32 v224, v224, v174\n" \
  "v_add_f32 v225, v225, v175\n" \
  "v_cvt_pk_bf16_f32 v171, v174, v175\n" \
  "s_waitcnt lgkmcnt(0)\n" \
  "s_waitcnt vmcnt(10)\n" \
  "s_barrier\n" \
  "v_mfma_f32_32x32x16_bf16 v[128:143], a[192:195], a[0:3], v[192:207]\n" \
  "v_exp_f32 v176, v176\n" \
  "v_exp_f32 v177, v177\n" \
  "v_mfma_f32_32x32x16_bf16 v[128:143], a[196:199], a[4:7], v[128:143]\n" \
  "s_add_u32 m0, s61, 0\n" \
  "ds_read_b64_tr_b16 a[64:65], v244 offset:16384\n" \
  "v_exp_f32 v178, v178\n" \
  "v_add_f32 v226, v226, v176\n" \
  "v_exp_f32 v179, v179\n" \
  "v_mfma_f32_32x32x16_bf16 v[128:143], a[200:203], a[8:11], v[128:143]\n" \
  "s_mov_b32 s40, 40960\n" \
  "ds_read_b64_tr_b16 a[66:67], v244 offset:18432\n" \
  "v_add_f32 v227, v227, v177\n" \
  "v_cvt_pk_bf16_f32 v176, v176, v177\n" \
  "v_mfma_f32_32x32x16_bf16 v[128:143], a[204:207], a[12:15], v[128:143]\n" \
  "ds_read_b64_tr_b16 a[68:69], v244 offset:16896\n" \
  "v_exp_f32 v180, v180\n" \
  "v_add_f32 v226, v226, v178\n" \
  "v_exp_f32 v181, v181\n" \
  "v_mfma_f32_32x32x16_bf16 v[128:143], a[208:211], a[16:19], v[128:143]\n" \
  "buffer_load_dwordx4 v253, %[srdA], s40 offen offset:0 sc1 lds\n" \
  "ds_read_b64_tr_b16 a[70:71], v244 offset:18944\n" \
  "v_add_f32 v227, v227, v179\n" \
  "v_cvt_pk_bf16_f32 v177, v178, v179\n" \
  "v_mfma_f32_32x32x16_bf16 v[128:143], a[212:215], a[20:23], v[128:143]\n" \
  "buffer_load_dwordx4 v253, %[srdA], s40 offen offset:1024 sc1 lds\n" \
  "ds_read_b64_tr_b16 a[72:73], v244 offset:17408\n" \
  "v_exp_f32 v182, v182\n" \
  "v_add_f32 v226, v226, v180\n" \
  "v_exp_f32 v183, v183\n" \
  "v_mfma_f32_32x32x16_bf16 v[128:143], a[216:219], a[24:27], v[128:143]\n" \
  "ds_read_b64_tr_b16 a[74:75], v244 offset:19456\n" \
  "v_add_f32 v227, v227, v181\n" \
  "v_cvt_pk_bf16_f32 v178, v180, v181\n" \
  "v_mfma_f32_32x32x16_bf16 v[128:143], a[220:223], a[28:31], v[128:143]\n" \
  "buffer_load_dwordx4 v253, %[srdA], s40 offen offset:2048 sc1 lds\n" \
  "v_exp_f32 v184, v184\n" \
  "v_add_f32 v226, v226, v182\n" \
  "v_exp_f32 v185, v185\n" \
  "v_mfma_f32_32x32x16_bf16 v[144:159], a[192:195], a[32:35], v[208:223]\n" \
  "buffer_load_dwordx4 v253, %[srdA], s40 offen offset:3072 sc1 lds\n" \
  "ds_read_b64_tr_b16 a[76:77], v244 offset:17920\n" \
  "v_add_f32 v227, v227, v183\n" \
  "v_cvt_pk_bf16_f32 v179, v182, v183\n" \
  "v_mfma_f32_32x32x16_bf16 v[144:159], a[196:199], a[36:39], v[144:159]\n" \
  "ds_read_b64_tr_b16 a[78:79], v244 offset:19968\n" \
  "v_exp_f32 v186, v186\n" \
  "v_add_f32 v226, v226, v184\n" \
  "v_exp_f32 v187, v187\n" \
  "v_mfma_f32_32x32x16_bf16 v[144:159], a[200:203], a[40:43], v[144:159]\n" \
  "s_add_u32 m0, s61, 4096\n" \
  "ds_read_b64_tr_b16 a[80:81], v244 offset:20480\n" \
  "v_add_f32 v227, v227, v185\n" \
  "v_cvt_pk_bf16_f32 v184, v184, v185\n" \
  "v_mfma_f32_32x32x16_bf16 v[144:159], a[204:207], a[44:47], v[144:159]\n" \
  "s_mov_b32 s40, 45056\n" \
  "ds_read_b64_tr_b16 a[82:83], v244 offset:22528\n" \
  "v_exp_f32 v188, v188\n" \
  "v_add_f32 v226, v226, v186\n" \
  "v_exp_f32 v189, v189\n" \
  "v_mfma_f32_32x32x16_bf16 v[144:159], a[208:211], a[48:51], v[144:159]\n" \
  "ds_read_b64_tr_b16 a[84:85], v244 offset:20992\n" \
  "v_add_f32 v227, v227, v187\n" \
  "v_cvt_pk_bf16_f32 v185, v186, v187\n" \
  "v_mfma_f32_32x32x16_bf16 v[144:159], a[212:215], a[52:55], v[144:159]\n" \
  "buffer_load_dwordx4 v253, %[srdA], s40 offen offset:0 sc1 lds\n" \
  "ds_read_b64_tr_b16 a[86:87], v244 offset:23040\n" \
  "v_exp_f32 v190, v190\n" \
  "v_add_f32 v226, v226, v188\n" \
  "v_exp_f32 v191, v191\n" \
  "v_mfma_f32_32x32x16_bf16 v[144:159], a[216:219], a[56:59], v[144:159]\n" \
  "buffer_load_dwordx4 v253, %[srdA], s40 offen offset:1024 sc1 lds\n" \
  "v_add_f32 v227, v227, v189\n" \
  "v_cvt_pk_bf16_f32 v186, v188, v189\n" \
  "v_mfma_f32_32x32x16_bf16 v[144:159], a[220:223], a[60:63], v[144:159]\n" \
  "ds_read_b64_tr_b16 a[88:89], v244 offset:21504\n" \
  "v_add_f32 v226, v226, v190\n" \
  "v_add_f32 v227, v227, v191\n" \
  "v_cvt_pk_bf16_f32 v187, v190, v191\n" \
  "v_mfma_f32_32x32x16_bf16 v[0:15], a[96:99], v[160:163], v[0:15]\n" \
  "buffer_load_dwordx4 v253, %[srdA], s40 offen offset:2048 sc1 lds\n" \
  "ds_read_b64_tr_b16 a[90:91], v244 offset:23552\n" \
  "v_exp_f32 v128, v128\n" \
  "v_exp_f32 v129, v129\n" \
  "v_mfma_f32_32x32x16_bf16 v[32:47], a[100:103], v[160:163], v[32:47]\n" \
  "buffer_load_dwordx4 v253, %[srdA], s40 offen offset:3072 sc1 lds\n" \
  "ds_read_b64_tr_b16 a[92:93], v244 offset:22016\n" \
  "v_exp_f32 v130, v130\n" \
  "v_add_f32 v224, v224, v128\n" \
  "v_exp_f32 v131, v131\n" \
  "v_mfma_f32_32x32x16_bf16 v[64:79], a[104:107], v[160:163], v[64:79]\n" \
  "ds_read_b64_tr_b16 a[94:95], v244 offset:24064\n" \
  "v_add_f32 v225, v225, v129\n" \
  "v_cvt_pk_bf16_f32 v128, v128, v129\n" \
  "v_mfma_f32_32x32x16_bf16 v[96:111], a[108:111], v[160:163], v[96:111]\n" \
  "s_add_u32 m0, s61, 8192\n" \
  "ds_read_b128 a[224:227], v245 offset:57344\n" \
  "v_exp_f32 v132, v132\n" \
  "v_add_f32 v224, v224, v130\n" \
  "v_exp_f32 v133, v133\n" \
  "v_mfma_f32_32x32x16_bf16 v[0:15], a[112:115], v[168:171], v[0:15]\n" \
  "s_mov_b32 s40, 49152\n" \
  "ds_read_b128 a[228:231], v246 offset:57344\n" \
  "v_add_f32 v225, v225, v131\n" \
  "v_cvt_pk_bf16_f32 v129, v130, v131\n" \
  "v_mfma_f32_32x32x16_bf16 v[32:47], a[116:119], v[168:171], v[32:47]\n" \
  "v_exp_f32 v134, v134\n" \
  "v_add_f32 v224, v224, v132\n" \
  "v_exp_f32 v135, v135\n" \
  "v_mfma_f32_32x32x16_bf16 v[64:79], a[120:123], v[168:171], v[64:79]\n" \
  "buffer_load_dwordx4 v253, %[srdA], s40 offen offset:0 sc1 lds\n" \
  "ds_read_b128 a[232:235], v247 offset:57344\n" \
  "v_add_f32 v225, v225, v133\n" \
  "v_cvt_pk_bf16_f32 v130, v132, v133\n" \
  "v_mfma_f32_32x32x16_bf16 v[96:111], a[124:127], v[168:171], v[96:111]\n" \
  "buffer_load_dwordx4 v253, %[srdA], s40 offen offset:1024 sc1 lds\n" \
  "ds_read_b128 a[236:239], v248 offset:57344\n" \
  "v_exp_f32 v136, v136\n" \
  "v_add_f32 v224, v224, v134\n" \
  "v_exp_f32 v137, v137\n" \
  "v_mfma_f32_32x32x16_bf16 v[16:31], a[96:99], v[176:179], v[16:31]\n" \
  "ds_read_b128 a[240:243], v249 offset:57344\n" \
  "v_add_f32 v225, v225, v135\n" \
  "v_cvt_pk_bf16_f32 v131, v134, v135\n" \
  "v_mfma_f32_32x32x16_bf16 v[48:63], a[100:103], v[176:179], v[48:63]\n" \
  "ds_read_b128 a[244:247], v250 offset:57344\n" \
  "v_exp_f32 v138, v138\n" \
  "v_add_f32 v224, v224, v136\n" \
  "v_exp_f32 v139, v139\n" \
  "v_mfma_f32_32x32x16_bf16 v[80:95], a[104:107], v[176:179], v[80:95]\n" \
  "ds_read_b128 a[248:251], v251 offset:57344\n" \
  "v_add_f32 v225, v225, v137\n" \
  "v_cvt_pk_bf16_f32 v136, v136, v137\n" \
  "v_mfma_f32_32x32x16_bf16 v[112:127], a[108:111], v[176:179], v[112:127]\n" \
  "ds_read_b128 a[252:255], v252 offset:57344\n" \
  "v_exp_f32 v140, v140\n" \
  "v_add_f32 v224, v224, v138\n" \
  "v_exp_f32 v141, v141\n" \
  "v_mfma_f32_32x32x16_bf16 v[16:31], a[112:115], v[184:187], v[16:31]\n" \
  "v_add_f32 v225, v225, v139\n" \
  "v_cvt_pk_bf16_f32 v137, v138, v139\n" \
  "v_mfma_f32_32x32x16_bf16 v[48:63], a[116:119], v[184:187], v[48:63]\n" \
  "v_exp_f32 v142, v142\n" \
  "v_add_f32 v224, v224, v140\n" \
  "v_exp_f32 v143, v143\n" \
  "v_mfma_f32_32x32x16_bf16 v[80:95], a[120:123], v[184:187], v[80:95]\n" \
  "v_add_f32 v225, v225, v141\n" \
  "v_cvt_pk_bf16_f32 v138, v140, v141\n" \
  "v_mfma_f32_32x32x16_bf16 v[112:127], a[124:127], v[184:187], v[112:127]\n" \
  "v_add_f32 v224, v224, v142\n" \
  "v_add_f32 v225, v225, v143\n" \
  "v_cvt_pk_bf16_f32 v139, v142, v143\n" \
  "s_waitcnt lgkmcnt(1)\n" \
  "v_mfma_f32_32x32x16_bf16 v[160:175], a[224:227], a[0:3], v[192:207]\n" \
  "v_exp_f32 v144, v144\n" \
  "v_exp_f32 v145, v145\n" \
  "v_mfma_f32_32x32x16_bf16 v[160:175], a[228:231], a[4:7], v[160:175]\n" \
  "ds_read_b64_tr_b16 a[96:97], v244 offset:24576\n" \
  "v_exp_f32 v146, v146\n" \
  "v_add_f32 v226, v226, v144\n" \
  "v_exp_f32 v147, v147\n" \
  "v_mfma_f32_32x32x16_bf16 v[160:175], a[232:235], a[8:11], v[160:175]\n" \
  "v_add_f32 v227, v227, v145\n" \
  "v_cvt_pk_bf16_f32 v144, v144, v145\n" \
  "v_mfma_f32_32x32x16_bf16 v[160:175], a[236:239], a[12:15], v[160:175]\n" \
  "ds_read_b64_tr_b16 a[98:99], v244 offset:26624\n" \
  "v_exp_f32 v148, v148\n" \
  "v_add_f32 v226, v226, v146\n" \
  "v_exp_f32 v149, v149\n" \
  "v_mfma_f32_32x32x16_bf16 v[160:175], a[240:243], a[16:19], v[160:175]\n" \
  "v_add_f32 v227, v227, v147\n" \
  "v_cvt_pk_bf16_f32 v145, v146, v147\n" \
  "v_mfma_f32_32x32x16_bf16 v[160:175], a[244:247], a[20:23], v[160:175]\n" \
  "ds_read_b64_tr_b16 a[100:101], v244 offset:25088\n" \
  "v_exp_f32 v150, v150\n" \
  "v_add_f32 v226, v226, v148\n" \
  "v_exp_f32 v151, v151\n" \
  "v_mfma_f32_32x32x16_bf16 v[160:175], a[248:251], a[24:27], v[160:175]\n" \
  "ds_read_b64_tr_b16 a[102:103], v244 offset:27136\n" \
  "v_add_f32 v227, v227, v149\n" \
  "v_cvt_pk_bf16_f32 v146, v148, v149\n" \
  "s_waitcnt lgkmcnt(3)\n" \
  "v_mfma_f32_32x32x16_bf16 v[160:175], a[252:255], a[28:31], v[160:175]\n" \
  "v_exp_f32 v152, v152\n" \
  "v_add_f32 v226, v226, v150\n" \
  "v_exp_f32 v153, v153\n" \
  "v_mfma_f32_32x32x16_bf16 v[176:191], a[224:227], a[32:35], v[208:223]\n" \
  "ds_read_b64_tr_b16 a[104:105], v244 offset:25600\n" \
  "v_add_f32 v227, v227, v151\n" \
  "v_cvt_pk_bf16_f32 v147, v150, v151\n" \
  "v_mfma_f32_32x32x16_bf16 v[176:191], a[228:231], a[36:39], v[176:191]\n" \
  "v_exp_f32 v154, v154\n" \
  "v_add_f32 v226, v226, v152\n" \
  "v_exp_f32 v155, v155\n" \
  "v_mfma_f32_32x32x16_bf16 v[176:191], a[232:235], a[40:43], v[176:191]\n" \
  "ds_read_b64_tr_b16 a[106:107], v244 offset:27648\n" \
  "v_add_f32 v227, v227, v153\n" \
  "v_cvt_pk_bf16_f32 v152, v152, v153\n" \
  "v_mfma_f32_32x32x16_bf16 v[176:191], a[236:239], a[44:47], v[176:191]\n" \
  "v_exp_f32 v156, v156\n" \
  "v_add_f32 v226, v226, v154\n" \
  "v_exp_f32 v157, v157\n" \
  "v_mfma_f32_32x32x16_bf16 v[176:191], a[240:243], a[48:51], v[176:191]\n" \
  "ds_read_b64_tr_b16 a[108:109], v244 offset:26112\n" \
  "v_add_f32 v227, v227, v155\n" \
  "v_cvt_pk_bf16_f32 v153, v154, v155\n" \
  "v_mfma_f32_32x32x16_bf16 v[176:191], a[244:247], a[52:55], v[176:191]\n" \
  "ds_read_b64_tr_b16 a[110:111], v244 offset:28160\n" \
  "v_exp_f32 v158, v158\n" \
  "v_add_f32 v226, v226, v156\n" \
  "v_exp_f32 v159, v159\n" \
  "v_mfma_f32_32x32x16_bf16 v[176:191], a[248:251], a[56:59], v[176:191]\n" \
  "v_add_f32 v227, v227, v157\n" \
  "v_cvt_pk_bf16_f32 v154, v156, v157\n" \
  "v_mfma_f32_32x32x16_bf16 v[176:191], a[252:255], a[60:63], v[176:191]\n" \
  "ds_read_b64_tr_b16 a[112:113], v244 offset:28672\n" \
  "v_add_f32 v226, v226, v158\n" \
  "v_add_f32 v227, v227, v159\n" \
  "v_cvt_pk_bf16_f32 v155, v158, v159\n" \
  "v_mfma_f32_32x32x16_bf16 v[0:15], a[64:67], v[128:131], v[0:15]\n" \
  "v_exp_f32 v160, v160\n" \
  "v_exp_f32 v161, v161\n" \
  "v_mfma_f32_32x32x16_bf16 v[32:47], a[68:71], v[128:131], v[32:47]\n" \
  "ds_read_b64_tr_b16 a[114:115], v244 offset:30720\n" \
  "v_exp_f32 v162, v162\n" \
  "v_add_f32 v224, v224, v160\n" \
  "v_exp_f32 v163, v163\n" \
  "v_mfma_f32_32x32x16_bf16 v[64:79], a[72:75], v[128:131], v[64:79]\n" \
  "v_add_f32 v225, v225, v161\n" \
  "v_cvt_pk_bf16_f32 v160, v160, v161\n" \
  "v_mfma_f32_32x32x16_bf16 v[96:111], a[76:79], v[128:131], v[96:111]\n" \
  "ds_read_b64_tr_b16 a[116:117], v244 offset:29184\n" \
  "v_exp_f32 v164, v164\n" \
  "v_add_f32 v224, v224, v162\n" \
  "v_exp_f32 v165, v165\n" \
  "v_mfma_f32_32x32x16_bf16 v[0:15], a[80:83], v[136:139], v[0:15]\n" \
  "ds_read_b64_tr_b16 a[118:119], v244 offset:31232\n" \
  "v_add_f32 v225, v225, v163\n" \
  "v_cvt_pk_bf16_f32 v161, v162, v163\n" \
  "v_mfma_f32_32x32x16_bf16 v[32:47], a[84:87], v[136:139], v[32:47]\n" \
  "v_exp_f32 v166, v166\n" \
  "v_add_f32 v224, v224, v164\n" \
  "v_exp_f32 v167, v167\n" \
  "v_mfma_f32_32x32x16_bf16 v[64:79], a[88:91], v[136:139], v[64:79]\n" \
  "ds_read_b64_tr_b16 a[120:121], v244 offset:29696\n" \
  "v_add_f32 v225, v225, v165\n" \
  "v_cvt_pk_bf16_f32 v162, v164, v165\n" \
  "v_mfma_f32_32x32x16_bf16 v[96:111], a[92:95], v[136:139], v[96:111]\n" \
  "v_exp_f32 v168, v168\n" \
  "v_add_f32 v224, v224, v166\n" \
  "v_exp_f32 v169, v169\n" \
  "v_mfma_f32_32x32x16_bf16 v[16:31], a[64:67], v[144:147], v[16:31]\n" \
  "ds_read_b64_tr_b16 a[122:123], v244 offset:31744\n" \
  "v_add_f32 v225, v225, v167\n" \
  "v_cvt_pk_bf16_f32 v163, v166, v167\n" \
  "v_mfma_f32_32x32x16_bf16 v[48:63], a[68:71], v[144:147], v[48:63]\n" \
  "v_exp_f32 v170, v170\n" \
  "v_add_f32 v224, v224, v168\n" \
  "v_exp_f32 v171, v171\n" \
  "v_mfma_f32_32x32x16_bf16 v[80:95], a[72:75], v[144:147], v[80:95]\n" \
  "ds_read_b64_tr_b16 a[124:125], v244 offset:30208\n" \
  "v_add_f32 v225, v225, v169\n" \
  "v_cvt_pk_bf16_f32 v168, v168, v169\n" \
  "v_mfma_f32_32x32x16_bf16 v[112:127], a[76:79], v[144:147], v[112:127]\n" \
  "ds_read_b64_tr_b16 a[126:127], v244 offset:32256\n" \
  "v_exp_f32 v172, v172\n" \
  "v_add_f32 v224, v224, v170\n" \
  "v_exp_f32 v173, v173\n" \
  "v_mfma_f32_32x32x16_bf16 v[16:31], a[80:83], v[152:155], v[16:31]\n" \
  "v_add_f32 v225, v225, v171\n" \
  "v_cvt_pk_bf16_f32 v169, v170, v171\n" \
  "v_mfma_f32_32x32x16_bf16 v[48:63], a[84:87], v[152:155], v[48:63]\n" \
  "v_exp_f32 v174, v174\n" \
  "v_add_f32 v224, v224, v172\n" \
  "v_exp_f32 v175, v175\n" \
  "v_mfma_f32_32x32x16_bf16 v[80:95], a[88:91], v[152:155], v[80:95]\n" \
  "v_add_f32 v225, v225, v173\n" \
  "v_cvt_pk_bf16_f32 v170, v172, v173\n" \
  "v_mfma_f32_32x32x16_bf16 v[112:127], a[92:95], v[152:155], v[112:127]\n" \
  "v_add_f32 v224, v224, v174\n" \
  "v_add_f32 v225, v225, v175\n" \
  "v_cvt_pk_bf16_f32 v171, v174, v175\n" \
  "v_add_f32 v228, v224, v225\n" \
  "v_mov_b32 v229, v228\n" \
  "s_nop 1\n" \
  "v_permlane32_swap_b32 v228, v229\n" \
  "v_add_f32 v224, v228, v229\n" \
  "v_rcp_f32 v228, v224\n" \
  "s_nop 0\n" \
  "v_mov_b32 v229, v228\n" \
  "s_waitcnt lgkmcnt(1)\n" \
  "v_mfma_f32_32x32x16_bf16 v[0:15], a[96:99], v[160:163], v[0:15]\n" \
  "v_exp_f32 v176, v176\n" \
  "v_exp_f32 v177, v177\n" \
  "v_exp_f32 v178, v178\n" \
  "v_add_f32 v226, v226, v176\n" \
  "v_exp_f32 v179, v179\n" \
  "v_mfma_f32_32x32x16_bf16 v[0:15], a[112:115], v[168:171], v[0:15]\n" \
  "v_add_f32 v227, v227, v177\n" \
  "v_cvt_pk_bf16_f32 v176, v176, v177\n" \
  "v_exp_f32 v180, v180\n" \
  "v_add_f32 v226, v226, v178\n" \
  "v_exp_f32 v181, v181\n" \
  "v_mfma_f32_32x32x16_bf16 v[32:47], a[100:103], v[160:163], v[32:47]\n" \
  "v_add_f32 v227, v227, v179\n" \
  "v_cvt_pk_bf16_f32 v177, v178, v179\n" \
  "v_exp_f32 v182, v182\n" \
  "v_add_f32 v226, v226, v180\n" \
  "v_exp_f32 v183, v183\n" \
  "v_mfma_f32_32x32x16_bf16 v[32:47], a[116:119], v[168:171], v[32:47]\n" \
  "v_add_f32 v227, v227, v181\n" \
  "v_cvt_pk_bf16_f32 v178, v180, v181\n" \
  "v_exp_f32 v184, v184\n" \
  "v_add_f32 v226, v226, v182\n" \
  "v_exp_f32 v185, v185\n" \
  "v_mfma_f32_32x32x16_bf16 v[64:79], a[104:107], v[160:163], v[64:79]\n" \
  "v_add_f32 v227, v227, v183\n" \
  "v_cvt_pk_bf16_f32 v179, v182, v183\n" \
  "v_exp_f32 v186, v186\n" \
  "v_add_f32 v226, v226, v184\n" \
  "v_exp_f32 v187, v187\n" \
  "v_mfma_f32_32x32x16_bf16 v[64:79], a[120:123], v[168:171], v[64:79]\n" \
  "v_add_f32 v227, v227, v185\n" \
  "v_cvt_pk_bf16_f32 v184, v184, v185\n" \
  "v_exp_f32 v188, v188\n" \
  "v_add_f32 v226, v226, v186\n" \
  "v_exp_f32 v189, v189\n" \
  "v_mfma_f32_32x32x16_bf16 v[96:111], a[108:111], v[160:163], v[96:111]\n" \
  "v_add_f32 v227, v227, v187\n" \
  "v_cvt_pk_bf16_f32 v185, v186, v187\n" \
  "v_exp_f32 v190, v190\n" \
  "v_add_f32 v226, v226, v188\n" \
  "v_exp_f32 v191, v191\n" \
  "s_waitcnt lgkmcnt(0)\n" \
  "v_mfma_f32_32x32x16_bf16 v[96:111], a[124:127], v[168:171], v[96:111]\n" \
  "v_add_f32 v227, v227, v189\n" \
  "v_cvt_pk_bf16_f32 v186, v188, v189\n" \
  "v_add_f32 v226, v226, v190\n" \
  "v_add_f32 v227, v227, v191\n" \
  "v_cvt_pk_bf16_f32 v187, v190, v191\n" \
  "v_mfma_f32_32x32x16_bf16 v[16:31], a[96:99], v[176:179], v[16:31]\n" \
  "v_add_f32 v230, v226, v227\n" \
  "v_mov_b32 v231, v230\n" \
  "s_nop 1\n" \
  "v_permlane32_swap_b32 v230, v231\n" \
  "v_add_f32 v226, v230, v231\n" \
  "v_rcp_f32 v230, v226\n" \
  "s_nop 0\n" \
  "v_mov_b32 v231, v230\n" \
  "v_mul_f32 v0, v0, v228\n" \
  "v_mul_f32 v1, v1, v228\n" \
  "v_mul_f32 v2, v2, v228\n" \
  "v_mul_f32 v3, v3, v228\n" \
  "v_mul_f32 v4, v4, v228\n" \
  "v_mul_f32 v5, v5, v228\n" \
  "v_mul_f32 v6, v6, v228\n" \
  "v_mul_f32 v7, v7, v228\n" \
  "v_mfma_f32_32x32x16_bf16 v[16:31], a[112:115], v[184:187], v[16:31]\n" \
  "s_mov_b32 s43, 0\n" \
  "v_mul_f32 v8, v8, v228\n" \
  "v_mul_f32 v9, v9, v228\n" \
  "v_mul_f32 v10, v10, v228\n" \
  "v_mul_f32 v11, v11, v228\n" \
  "v_mul_f32 v12, v12, v228\n" \
  "v_mul_f32 v13, v13, v228\n" \
  "v_mul_f32 v14, v14, v228\n" \
  "v_mul_f32 v15, v15, v228\n" \
  "v_cvt_pk_bf16_f32 v0, v0, v1\n" \
  "v_cvt_pk_bf16_f32 v1, v2, v3\n" \
  "v_cvt_pk_bf16_f32 v2, v4, v5\n" \
  "v_cvt_pk_bf16_f32 v3, v6, v7\n" \
  "buffer_store_dwordx4 v[0:3], v232, %[srdO], s43 offen offset:0 sc1\n" \
  "v_cvt_pk_bf16_f32 v8, v8, v9\n" \
  "v_mfma_f32_32x32x16_bf16 v[48:63], a[100:103], v[176:179], v[48:63]\n" \
  "s_mov_b32 s43, 0\n" \
  "v_cvt_pk_bf16_f32 v9, v10, v11\n" \
  "v_cvt_pk_bf16_f32 v10, v12, v13\n" \
  "v_cvt_pk_bf16_f32 v11, v14, v15\n" \
  "buffer_store_dwordx4 v[8:11], v232, %[srdO], s43 offen offset:1024 sc1\n" \
  "v_mul_f32 v32, v32, v228\n" \
  "v_mul_f32 v33, v33, v228\n" \
  "v_mul_f32 v34, v34, v228\n" \
  "v_mul_f32 v35, v35, v228\n" \
  "v_mul_f32 v36, v36, v228\n" \
  "v_mul_f32 v37, v37, v228\n" \
  "v_mul_f32 v38, v38, v228\n" \
  "v_mul_f32 v39, v39, v228\n" \
  "v_mul_f32 v40, v40, v228\n" \
  "v_mul_f32 v41, v41, v228\n" \
  "v_mfma_f32_32x32x16_bf16 v[48:63], a[116:119], v[184:187], v[48:63]\n" \
  "s_mov_b32 s43, 0\n" \
  "v_mul_f32 v42, v42, v228\n" \
  "v_mul_f32 v43, v43, v228\n" \
  "v_mul_f32 v44, v44, v228\n" \
  "v_mul_f32 v45, v45, v228\n" \
  "v_mul_f32 v46, v46, v228\n" \
  "v_mul_f32 v47, v47, v228\n" \
  "v_cvt_pk_bf16_f32 v32, v32, v33\n" \
  "v_cvt_pk_bf16_f32 v33, v34, v35\n" \
  "v_cvt_pk_bf16_f32 v34, v36, v37\n" \
  "v_cvt_pk_bf16_f32 v35, v38, v39\n" \
  "buffer_store_dwordx4 v[32:35], v232, %[srdO], s43 offen offset:2048 sc1\n" \
  "v_cvt_pk_bf16_f32 v40, v40, v41\n" \
  "v_cvt_pk_bf16_f32 v41, v42, v43\n" \
  "v_cvt_pk_bf16_f32 v42, v44, v45\n" \
  "v_mfma_f32_32x32x16_bf16 v[80:95], a[104:107], v[176:179], v[80:95]\n" \
  "s_mov_b32 s43, 0\n" \
  "v_cvt_pk_bf16_f32 v43, v46, v47\n" \
  "buffer_store_dwordx4 v[40:43], v232, %[srdO], s43 offen offset:3072 sc1\n" \
  "v_mul_f32 v64, v64, v228\n" \
  "v_mul_f32 v65, v65, v228\n" \
  "v_mul_f32 v66, v66, v228\n" \
  "v_mul_f32 v67, v67, v228\n" \
  "v_mul_f32 v68, v68, v228\n" \
  "v_mul_f32 v69, v69, v228\n" \
  "v_mul_f32 v70, v70, v228\n" \
  "v_mul_f32 v71, v71, v228\n" \
  "v_mul_f32 v72, v72, v228\n" \
  "v_mul_f32 v73, v73, v228\n" \
  "v_mul_f32 v74, v74, v228\n" \
  "v_mfma_f32_32x32x16_bf16 v[80:95], a[120:123], v[184:187], v[80:95]\n" \
  "s_mov_b32 s43, 4096\n" \
  "v_mul_f32 v75, v75, v228\n" \
  "v_mul_f32 v76, v76, v228\n" \
  "v_mul_f32 v77, v77, v228\n" \
  "v_mul_f32 v78, v78, v228\n" \
  "v_mul_f32 v79, v79, v228\n" \
  "v_cvt_pk_bf16_f32 v64, v64, v65\n" \
  "v_cvt_pk_bf16_f32 v65, v66, v67\n" \
  "v_cvt_pk_bf16_f32 v66, v68, v69\n" \
  "v_cvt_pk_bf16_f32 v67, v70, v71\n" \
  "buffer_store_dwordx4 v[64:67], v232, %[srdO], s43 offen offset:0 sc1\n" \
  "v_cvt_pk_bf16_f32 v72, v72, v73\n" \
  "v_cvt_pk_bf16_f32 v73, v74, v75\n" \
  "v_cvt_pk_bf16_f32 v74, v76, v77\n" \
  "v_cvt_pk_bf16_f32 v75, v78, v79\n" \
  "v_mfma_f32_32x32x16_bf16 v[112:127], a[108:111], v[176:179], v[112:127]\n" \
  "s_mov_b32 s43, 4096\n" \
  "buffer_store_dwordx4 v[72:75], v232, %[srdO], s43 offen offset:1024 sc1\n" \
  "v_mul_f32 v96, v96, v228\n" \
  "v_mul_f32 v97, v97, v228\n" \
  "v_mul_f32 v98, v98, v228\n" \
  "v_mul_f32 v99, v99, v228\n" \
  "v_mul_f32 v100, v100, v228\n" \
  "v_mul_f32 v101, v101, v228\n" \
  "v_mul_f32 v102, v102, v228\n" \
  "v_mul_f32 v103, v103, v228\n" \
  "v_mul_f32 v104, v104, v228\n" \
  "v_mul_f32 v105, v105, v228\n" \
  "v_mul_f32 v106, v106, v228\n" \
  "v_mul_f32 v107, v107, v228\n" \
  "v_mul_f32 v108, v108, v228\n" \
  "v_mfma_f32_32x32x16_bf16 v[112:127], a[124:127], v[184:187], v[112:127]\n" \
  "s_mov_b32 s43, 4096\n" \
  "s_mov_b32 s43, 4096\n" \
  "v_mul_f32 v109, v109, v228\n" \
  "v_mul_f32 v110, v110, v228\n" \
  "v_mul_f32 v111, v111, v228\n" \
  "v_cvt_pk_bf16_f32 v96, v96, v97\n" \
  "v_cvt_pk_bf16_f32 v97, v98, v99\n" \
  "v_cvt_pk_bf16_f32 v98, v100, v101\n" \
  "v_cvt_pk_bf16_f32 v99, v102, v103\n" \
  "buffer_store_dwordx4 v[96:99], v232, %[srdO], s43 offen offset:2048 sc1\n" \
  "v_cvt_pk_bf16_f32 v104, v104, v105\n" \
  "v_cvt_pk_bf16_f32 v105, v106, v107\n" \
  "v_cvt_pk_bf16_f32 v106, v108, v109\n" \
  "v_cvt_pk_bf16_f32 v107, v110, v111\n" \
  "buffer_store_dwordx4 v[104:107], v232, %[srdO], s43 offen offset:3072 sc1\n" \
  "v_pk_mul_f32 v[16:17], v[16:17], v[230:231]\n" \
  "v_pk_mul_f32 v[18:19], v[18:19], v[230:231]\n" \
  "v_pk_mul_f32 v[20:21], v[20:21], v[230:231]\n" \
  "v_pk_mul_f32 v[22:23], v[22:23], v[230:231]\n" \
  "v_pk_mul_f32 v[24:25], v[24:25], v[230:231]\n" \
  "v_pk_mul_f32 v[26:27], v[26:27], v[230:231]\n" \
  "v_pk_mul_f32 v[28:29], v[28:29], v[230:231]\n" \
  "v_pk_mul_f32 v[30:31], v[30:31], v[230:231]\n" \
  "v_cvt_pk_bf16_f32 v16, v16, v17\n" \
  "v_cvt_pk_bf16_f32 v17, v18, v19\n" \
  "v_cvt_pk_bf16_f32 v18, v20, v21\n" \
  "v_cvt_pk_bf16_f32 v19, v22, v23\n" \
  "s_mov_b32 s43, 262144\n" \
  "buffer_store_dwordx4 v[16:19], v232, %[srdO], s43 offen offset:0 sc1\n" \
  "v_cvt_pk_bf16_f32 v24, v24, v25\n" \
  "v_cvt_pk_bf16_f32 v25, v26, v27\n" \
  "v_cvt_pk_bf16_f32 v26, v28, v29\n" \
  "v_cvt_pk_bf16_f32 v27, v30, v31\n" \
  "s_mov_b32 s43, 262144\n" \
  "buffer_store_dwordx4 v[24:27], v232, %[srdO], s43 offen offset:1024 sc1\n" \
  "v_pk_mul_f32 v[48:49], v[48:49], v[230:231]\n" \
  "v_pk_mul_f32 v[50:51], v[50:51], v[230:231]\n" \
  "v_pk_mul_f32 v[52:53], v[52:53], v[230:231]\n" \
  "v_pk_mul_f32 v[54:55], v[54:55], v[230:231]\n" \
  "v_pk_mul_f32 v[56:57], v[56:57], v[230:231]\n" \
  "v_pk_mul_f32 v[58:59], v[58:59], v[230:231]\n" \
  "v_pk_mul_f32 v[60:61], v[60:61], v[230:231]\n" \
  "v_pk_mul_f32 v[62:63], v[62:63], v[230:231]\n" \
  "v_cvt_pk_bf16_f32 v48, v48, v49\n" \
  "v_cvt_pk_bf16_f32 v49, v50, v51\n" \
  "v_cvt_pk_bf16_f32 v50, v52, v53\n" \
  "v_cvt_pk_bf16_f32 v51, v54, v55\n" \
  "s_mov_b32 s43, 262144\n" \
  "buffer_store_dwordx4 v[48:51], v232, %[srdO], s43 offen offset:2048 sc1\n" \
  "v_cvt_pk_bf16_f32 v56, v56, v57\n" \
  "v_cvt_pk_bf16_f32 v57, v58, v59\n" \
  "v_cvt_pk_bf16_f32 v58, v60, v61\n" \
  "v_cvt_pk_bf16_f32 v59, v62, v63\n" \
  "s_mov_b32 s43, 262144\n" \
  "buffer_store_dwordx4 v[56:59], v232, %[srdO], s43 offen offset:3072 sc1\n" \
  "v_pk_mul_f32 v[80:81], v[80:81], v[230:231]\n" \
  "v_pk_mul_f32 v[82:83], v[82:83], v[230:231]\n" \
  "v_pk_mul_f32 v[84:85], v[84:85], v[230:231]\n" \
  "v_pk_mul_f32 v[86:87], v[86:87], v[230:231]\n" \
  "v_pk_mul_f32 v[88:89], v[88:89], v[230:231]\n" \
  "v_pk_mul_f32 v[90:91], v[90:91], v[230:231]\n" \
  "v_pk_mul_f32 v[92:93], v[92:93], v[230:231]\n" \
  "v_pk_mul_f32 v[94:95], v[94:95], v[230:231]\n" \
  "v_cvt_pk_bf16_f32 v80, v80, v81\n" \
  "v_cvt_pk_bf16_f32 v81, v82, v83\n" \
  "v_cvt_pk_bf16_f32 v82, v84, v85\n" \
  "v_cvt_pk_bf16_f32 v83, v86, v87\n" \
  "s_mov_b32 s43, 266240\n" \
  "buffer_store_dwordx4 v[80:83], v232, %[srdO], s43 offen offset:0 sc1\n" \
  "v_cvt_pk_bf16_f32 v88, v88, v89\n" \
  "v_cvt_pk_bf16_f32 v89, v90, v91\n" \
  "v_cvt_pk_bf16_f32 v90, v92, v93\n" \
  "v_cvt_pk_bf16_f32 v91, v94, v95\n" \
  "s_mov_b32 s43, 266240\n" \
  "buffer_store_dwordx4 v[88:91], v232, %[srdO], s43 offen offset:1024 sc1\n" \
  "v_pk_mul_f32 v[112:113], v[112:113], v[230:231]\n" \
  "v_pk_mul_f32 v[114:115], v[114:115], v[230:231]\n" \
  "v_pk_mul_f32 v[116:117], v[116:117], v[230:231]\n" \
  "v_pk_mul_f32 v[118:119], v[118:119], v[230:231]\n" \
  "v_pk_mul_f32 v[120:121], v[120:121], v[230:231]\n" \
  "v_pk_mul_f32 v[122:123], v[122:123], v[230:231]\n" \
  "v_pk_mul_f32 v[124:125], v[124:125], v[230:231]\n" \
  "v_pk_mul_f32 v[126:127], v[126:127], v[230:231]\n" \
  "v_cvt_pk_bf16_f32 v112, v112, v113\n" \
  "v_cvt_pk_bf16_f32 v113, v114, v115\n" \
  "v_cvt_pk_bf16_f32 v114, v116, v117\n" \
  "v_cvt_pk_bf16_f32 v115, v118, v119\n" \
  "s_mov_b32 s43, 266240\n" \
  "buffer_store_dwordx4 v[112:115], v232, %[srdO], s43 offen offset:2048 sc1\n" \
  "v_cvt_pk_bf16_f32 v120, v120, v121\n" \
  "v_cvt_pk_bf16_f32 v121, v122, v123\n" \
  "v_cvt_pk_bf16_f32 v122, v124, v125\n" \
  "v_cvt_pk_bf16_f32 v123, v126, v127\n" \
  "s_mov_b32 s43, 266240\n" \
  "buffer_store_dwordx4 v[120:123], v232, %[srdO], s43 offen offset:3072 sc1\n" \
  "v_max_f32 v229, v224, v226\n" \
  "v_cmp_nlt_f32 vcc, v229, %[lim]\n" \
  "s_nop 4\n" \
  "s_waitcnt vmcnt(16)\n" \
  "s_cbranch_vccnz .Lfb_nopub_%=\n" \
  "s_waitcnt vmcnt(0)\n" \
  "v_mov_b32 v228, %[tag]\n" \
  "v_mov_b32 v229, 0\n" \
  "s_mov_b64 exec, 1\n" \
  "global_store_dword v229, v228, %[fo] sc1\n" \
  "s_mov_b64 exec, -1\n" \
  ".Lfb_nopub_%=:\n" \
  "s_barrier\n" \
  "s_cbranch_vccz .Lfb_skip_%=\n" \
  "s_waitcnt vmcnt(0) lgkmcnt(0)\n" \
  "v_lshrrev_b32 v236, 4, v232\n" \
  "v_and_b32 v233, 31, v236\n" \
  "v_lshlrev_b32 v233, 8, v233\n" \
  "v_lshrrev_b32 v231, 5, v236\n" \
  "v_lshl_add_u32 v233, v231, 4, v233\n" \
  "v_bfe_u32 v234, v236, 2, 3\n" \
  "v_lshlrev_b32 v234, 8, v234\n" \
  "v_lshl_add_u32 v234, v231, 6, v234\n" \
  "v_and_b32 v230, 3, v236\n" \
  "v_lshl_add_u32 v234, v230, 4, v234\n" \
  "v_add_u32 v234, 1024, v234\n" \
  "v_lshlrev_b32 v235, 8, v231\n" \
  "v_bfe_u32 v231, v236, 2, 2\n" \
  "v_lshl_add_u32 v235, v231, 6, v235\n" \
  "v_bfe_u32 v231, v236, 4, 1\n" \
  "v_lshl_add_u32 v235, v231, 5, v235\n" \
  "v_lshl_add_u32 v235, v230, 3, v235\n" \
  "s_lshl_b32 s47, %[w], 13\n" \
  "s_add_u32 s47, s47, 122880\n" \
  "v_add_u32 v235, s47, v235\n" \
  "v_mov_b32 v228, 0xff7fffff\n" \
  "v_mov_b32 v224, 0\n" \
  "v_mov_b32 v0, 0\n" \
  "v_mov_b32 v1, 0\n" \
  "v_mov_b32 v2, 0\n" \
  "v_mov_b32 v3, 0\n" \
  "v_mov_b32 v4, 0\n" \
  "v_mov_b32 v5, 0\n" \
  "v_mov_b32 v6, 0\n" \
  "v_mov_b32 v7, 0\n" \
  "v_mov_b32 v8, 0\n" \
  "v_mov_b32 v9, 0\n" \
  "v_mov_b32 v10, 0\n" \
  "v_mov_b32 v11, 0\n" \
  "v_mov_b32 v12, 0\n" \
  "v_mov_b32 v13, 0\n" \
  "v_mov_b32 v14, 0\n" \
  "v_mov_b32 v15, 0\n" \
  "v_mov_b32 v32, 0\n" \
  "v_mov_b32 v33, 0\n" \
  "v_mov_b32 v34, 0\n" \
  "v_mov_b32 v35, 0\n" \
  "v_mov_b32 v36, 0\n" \
  "v_mov_b32 v37, 0\n" \
  "v_mov_b32 v38, 0\n" \
  "v_mov_b32 v39, 0\n" \
  "v_mov_b32 v40, 0\n" \
  "v_mov_b32 v41, 0\n" \
  "v_mov_b32 v42, 0\n" \
  "v_mov_b32 v43, 0\n" \
  "v_mov_b32 v44, 0\n" \
  "v_mov_b32 v45, 0\n" \
  "v_mov_b32 v46, 0\n" \
  "v_mov_b32 v47, 0\n" \
  "v_mov_b32 v64, 0\n" \
  "v_mov_b32 v65, 0\n" \
  "v_mov_b32 v66, 0\n" \
  "v_mov_b32 v67, 0\n" \
  "v_mov_b32 v68, 0\n" \
  "v_mov_b32 v69, 0\n" \
  "v_mov_b32 v70, 0\n" \
  "v_mov_b32 v71, 0\n" \
  "v_mov_b32 v72, 0\n" \
  "v_mov_b32 v73, 0\n" \
  "v_mov_b32 v74, 0\n" \
  "v_mov_b32 v75, 0\n" \
  "v_mov_b32 v76, 0\n" \
  "v_mov_b32 v77, 0\n" \
  "v_mov_b32 v78, 0\n" \
  "v_mov_b32 v79, 0\n" \
  "v_mov_b32 v96, 0\n" \
  "v_mov_b32 v97, 0\n" \
  "v_mov_b32 v98, 0\n" \
  "v_mov_b32 v99, 0\n" \
  "v_mov_b32 v100, 0\n" \
  "v_mov_b32 v101, 0\n" \
  "v_mov_b32 v102, 0\n" \
  "v_mov_b32 v103, 0\n" \
  "v_mov_b32 v104, 0\n" \
  "v_mov_b32 v105, 0\n" \
  "v_mov_b32 v106, 0\n" \
  "v_mov_b32 v107, 0\n" \
  "v_mov_b32 v108, 0\n" \
  "v_mov_b32 v109, 0\n" \
  "v_mov_b32 v110, 0\n" \
  "v_mov_b32 v111, 0\n" \
  "v_mov_b32 v229, 0xff7fffff\n" \
  "v_mov_b32 v226, 0\n" \
  "v_mov_b32 v16, 0\n" \
  "v_mov_b32 v17, 0\n" \
  "v_mov_b32 v18, 0\n" \
  "v_mov_b32 v19, 0\n" \
  "v_mov_b32 v20, 0\n" \
  "v_mov_b32 v21, 0\n" \
  "v_mov_b32 v22, 0\n" \
  "v_mov_b32 v23, 0\n" \
  "v_mov_b32 v24, 0\n" \
  "v_mov_b32 v25, 0\n" \
  "v_mov_b32 v26, 0\n" \
  "v_mov_b32 v27, 0\n" \
  "v_mov_b32 v28, 0\n" \
  "v_mov_b32 v29, 0\n" \
  "v_mov_b32 v30, 0\n" \
  "v_mov_b32 v31, 0\n" \
  "v_mov_b32 v48, 0\n" \
  "v_mov_b32 v49, 0\n" \
  "v_mov_b32 v50, 0\n" \
  "v_mov_b32 v51, 0\n" \
  "v_mov_b32 v52, 0\n" \
  "v_mov_b32 v53, 0\n" \
  "v_mov_b32 v54, 0\n" \
  "v_mov_b32 v55, 0\n" \
  "v_mov_b32 v56, 0\n" \
  "v_mov_b32 v57, 0\n" \
  "v_mov_b32 v58, 0\n" \
  "v_mov_b32 v59, 0\n" \
  "v_mov_b32 v60, 0\n" \
  "v_mov_b32 v61, 0\n" \
  "v_mov_b32 v62, 0\n" \
  "v_mov_b32 v63, 0\n" \
  "v_mov_b32 v80, 0\n" \
  "v_mov_b32 v81, 0\n" \
  "v_mov_b32 v82, 0\n" \
  "v_mov_b32 v83, 0\n" \
  "v_mov_b32 v84, 0\n" \
  "v_mov_b32 v85, 0\n" \
  "v_mov_b32 v86, 0\n" \
  "v_mov_b32 v87, 0\n" \
  "v_mov_b32 v88, 0\n" \
  "v_mov_b32 v89, 0\n" \
  "v_mov_b32 v90, 0\n" \
  "v_mov_b32 v91, 0\n" \
  "v_mov_b32 v92, 0\n" \
  "v_mov_b32 v93, 0\n" \
  "v_mov_b32 v94, 0\n" \
  "v_mov_b32 v95, 0\n" \
  "v_mov_b32 v112, 0\n" \
  "v_mov_b32 v113, 0\n" \
  "v_mov_b32 v114, 0\n" \
  "v_mov_b32 v115, 0\n" \
  "v_mov_b32 v116, 0\n" \
  "v_mov_b32 v117, 0\n" \
  "v_mov_b32 v118, 0\n" \
  "v_mov_b32 v119, 0\n" \
  "v_mov_b32 v120, 0\n" \
  "v_mov_b32 v121, 0\n" \
  "v_mov_b32 v122, 0\n" \
  "v_mov_b32 v123, 0\n" \
  "v_mov_b32 v124, 0\n" \
  "v_mov_b32 v125, 0\n" \
  "v_mov_b32 v126, 0\n" \
  "v_mov_b32 v127, 0\n" \
  "s_mov_b32 s46, 0\n" \
  "s_nop 7\n" \
  ".Lfb_loop_%=:\n" \
  "s_add_u32 m0, s47, 0\n" \
  "s_add_u32 s40, s46, 0\n" \
  "buffer_load_dwordx4 v234, %[srdV], s40 offen offset:0 lds\n" \
  "s_add_u32 m0, s47, 1024\n" \
  "s_add_u32 s40, s46, 128\n" \
  "buffer_load_dwordx4 v234, %[srdV], s40 offen offset:0 lds\n" \
  "s_add_u32 m0, s47, 2048\n" \
  "s_add_u32 s40, s46, 2048\n" \
  "buffer_load_dwordx4 v234, %[srdV], s40 offen offset:0 lds\n" \
  "s_add_u32 m0, s47, 3072\n" \
  "s_add_u32 s40, s46, 2176\n" \
  "buffer_load_dwordx4 v234, %[srdV], s40 offen offset:0 lds\n" \
  "s_add_u32 m0, s47, 4096\n" \
  "s_add_u32 s40, s46, 4096\n" \
  "buffer_load_dwordx4 v234, %[srdV], s40 offen offset:0 lds\n" \
  "s_add_u32 m0, s47, 5120\n" \
  "s_add_u32 s40, s46, 4224\n" \
  "buffer_load_dwordx4 v234, %[srdV], s40 offen offset:0 lds\n" \
  "s_add_u32 m0, s47, 6144\n" \
  "s_add_u32 s40, s46, 6144\n" \
  "buffer_load_dwordx4 v234, %[srdV], s40 offen offset:0 lds\n" \
  "s_add_u32 m0, s47, 7168\n" \
  "s_add_u32 s40, s46, 6272\n" \
  "buffer_load_dwordx4 v234, %[srdV], s40 offen offset:0 lds\n" \
  "buffer_load_dwordx4 a[192:195], v233, %[srdK], s46 offen offset:0\n" \
  "buffer_load_dwordx4 a[196:199], v233, %[srdK], s46 offen offset:32\n" \
  "buffer_load_dwordx4 a[200:203], v233, %[srdK], s46 offen offset:64\n" \
  "buffer_load_dwordx4 a[204:207], v233, %[srdK], s46 offen offset:96\n" \
  "buffer_load_dwordx4 a[208:211], v233, %[srdK], s46 offen offset:128\n" \
  "buffer_load_dwordx4 a[212:215], v233, %[srdK], s46 offen offset:160\n" \
  "buffer_load_dwordx4 a[216:219], v233, %[srdK], s46 offen offset:192\n" \
  "buffer_load_dwordx4 a[220:223], v233, %[srdK], s46 offen offset:224\n" \
  "s_waitcnt vmcnt(7)\n" \
  "v_mfma_f32_32x32x16_bf16 v[128:143], a[192:195], a[0:3], 0\n" \
  "s_waitcnt vmcnt(6)\n" \
  "v_mfma_f32_32x32x16_bf16 v[128:143], a[196:199], a[4:7], v[128:143]\n" \
  "s_waitcnt vmcnt(5)\n" \
  "v_mfma_f32_32x32x16_bf16 v[128:143], a[200:203], a[8:11], v[128:143]\n" \
  "s_waitcnt vmcnt(4)\n" \
  "v_mfma_f32_32x32x16_bf16 v[128:143], a[204:207], a[12:15], v[128:143]\n" \
  "s_waitcnt vmcnt(3)\n" \
  "v_mfma_f32_32x32x16_bf16 v[128:143], a[208:211], a[16:19], v[128:143]\n" \
  "s_waitcnt vmcnt(2)\n" \
  "v_mfma_f32_32x32x16_bf16 v[128:143], a[212:215], a[20:23], v[128:143]\n" \
  "s_waitcnt vmcnt(1)\n" \
  "v_mfma_f32_32x32x16_bf16 v[128:143], a[216:219], a[24:27], v[128:143]\n" \
  "s_waitcnt vmcnt(0)\n" \
  "v_mfma_f32_32x32x16_bf16 v[128:143], a[220:223], a[28:31], v[128:143]\n" \
  "v_mfma_f32_32x32x16_bf16 v[144:159], a[192:195], a[32:35], 0\n" \
  "v_mfma_f32_32x32x16_bf16 v[144:159], a[196:199], a[36:39], v[144:159]\n" \
  "v_mfma_f32_32x32x16_bf16 v[144:159], a[200:203], a[40:43], v[144:159]\n" \
  "v_mfma_f32_32x32x16_bf16 v[144:159], a[204:207], a[44:47], v[144:159]\n" \
  "v_mfma_f32_32x32x16_bf16 v[144:159], a[208:211], a[48:51], v[144:159]\n" \
  "v_mfma_f32_32x32x16_bf16 v[144:159], a[212:215], a[52:55], v[144:159]\n" \
  "v_mfma_f32_32x32x16_bf16 v[144:159], a[216:219], a[56:59], v[144:159]\n" \
  "v_mfma_f32_32x32x16_bf16 v[144:159], a[220:223], a[60:63], v[144:159]\n" \
  "ds_read_b64_tr_b16 a[64:65], v235 offset:0\n" \
  "ds_read_b64_tr_b16 a[66:67], v235 offset:2048\n" \
  "ds_read_b64_tr_b16 a[68:69], v235 offset:512\n" \
  "ds_read_b64_tr_b16 a[70:71], v235 offset:2560\n" \
  "ds_read_b64_tr_b16 a[72:73], v235 offset:1024\n" \
  "ds_read_b64_tr_b16 a[74:75], v235 offset:3072\n" \
  "ds_read_b64_tr_b16 a[76:77], v235 offset:1536\n" \
  "ds_read_b64_tr_b16 a[78:79], v235 offset:3584\n" \
  "ds_read_b64_tr_b16 a[80:81], v235 offset:4096\n" \
  "ds_read_b64_tr_b16 a[82:83], v235 offset:6144\n" \
  "ds_read_b64_tr_b16 a[84:85], v235 offset:4608\n" \
  "ds_read_b64_tr_b16 a[86:87], v235 offset:6656\n" \
  "ds_read_b64_tr_b16 a[88:89], v235 offset:5120\n" \
  "ds_read_b64_tr_b16 a[90:91], v235 offset:7168\n" \
  "ds_read_b64_tr_b16 a[92:93], v235 offset:5632\n" \
  "ds_read_b64_tr_b16 a[94:95], v235 offset:7680\n" \
  "v_max3_f32 v160, v128, v129, v130\n" \
  "v_max3_f32 v160, v160, v131, v132\n" \
  "v_max3_f32 v160, v160, v133, v134\n" \
  "v_max3_f32 v160, v160, v135, v136\n" \
  "v_max3_f32 v160, v160, v137, v138\n" \
  "v_max3_f32 v160, v160, v139, v140\n" \
  "v_max3_f32 v160, v160, v141, v142\n" \
  "v_max_f32 v160, v160, v143\n" \
  "v_mov_b32 v161, v160\n" \
  "s_nop 1\n" \
  "v_permlane32_swap_b32 v160, v161\n" \
  "v_max_f32 v160, v160, v161\n" \
  "v_max_f32 v160, v160, v228\n" \
  "v_sub_f32 v230, v228, v160\n" \
  "v_exp_f32 v230, v230\n" \
  "v_mov_b32 v228, v160\n" \
  "v_sub_f32 v128, v128, v160\n" \
  "v_sub_f32 v129, v129, v160\n" \
  "v_sub_f32 v130, v130, v160\n" \
  "v_sub_f32 v131, v131, v160\n" \
  "v_sub_f32 v132, v132, v160\n" \
  "v_sub_f32 v133, v133, v160\n" \
  "v_sub_f32 v134, v134, v160\n" \
  "v_sub_f32 v135, v135, v160\n" \
  "v_sub_f32 v136, v136, v160\n" \
  "v_sub_f32 v137, v137, v160\n" \
  "v_sub_f32 v138, v138, v160\n" \
  "v_sub_f32 v139, v139, v160\n" \
  "v_sub_f32 v140, v140, v160\n" \
  "v_sub_f32 v141, v141, v160\n" \
  "v_sub_f32 v142, v142, v160\n" \
  "v_sub_f32 v143, v143, v160\n" \
  "v_exp_f32 v128, v128\n" \
  "v_exp_f32 v129, v129\n" \
  "v_exp_f32 v130, v130\n" \
  "v_exp_f32 v131, v131\n" \
  "v_exp_f32 v132, v132\n" \
  "v_exp_f32 v133, v133\n" \
  "v_exp_f32 v134, v134\n" \
  "v_exp_f32 v135, v135\n" \
  "v_exp_f32 v136, v136\n" \
  "v_exp_f32 v137, v137\n" \
  "v_exp_f32 v138, v138\n" \
  "v_exp_f32 v139, v139\n" \
  "v_exp_f32 v140, v140\n" \
  "v_exp_f32 v141, v141\n" \
  "v_exp_f32 v142, v142\n" \
  "v_exp_f32 v143, v143\n" \
  "v_mul_f32 v224, v224, v230\n" \
  "v_add_f32 v224, v224, v128\n" \
  "v_add_f32 v224, v224, v129\n" \
  "v_add_f32 v224, v224, v130\n" \
  "v_add_f32 v224, v224, v131\n" \
  "v_add_f32 v224, v224, v132\n" \
  "v_add_f32 v224, v224, v133\n" \
  "v_add_f32 v224, v224, v134\n" \
  "v_add_f32 v224, v224, v135\n" \
  "v_add_f32 v224, v224, v136\n" \
  "v_add_f32 v224, v224, v137\n" \
  "v_add_f32 v224, v224, v138\n" \
  "v_add_f32 v224, v224, v139\n" \
  "v_add_f32 v224, v224, v140\n" \
  "v_add_f32 v224, v224, v141\n" \
  "v_add_f32 v224, v224, v142\n" \
  "v_add_f32 v224, v224, v143\n" \
  "v_cvt_pk_bf16_f32 v128, v128, v129\n" \
  "v_cvt_pk_bf16_f32 v129, v130, v131\n" \
  "v_cvt_pk_bf16_f32 v130, v132, v133\n" \
  "v_cvt_pk_bf16_f32 v131, v134, v135\n" \
  "v_cvt_pk_bf16_f32 v136, v136, v137\n" \
  "v_cvt_pk_bf16_f32 v137, v138, v139\n" \
  "v_cvt_pk_bf16_f32 v138, v140, v141\n" \
  "v_cvt_pk_bf16_f32 v139, v142, v143\n" \
  "v_mul_f32 v0, v0, v230\n" \
  "v_mul_f32 v1, v1, v230\n" \
  "v_mul_f32 v2, v2, v230\n" \
  "v_mul_f32 v3, v3, v230\n" \
  "v_mul_f32 v4, v4, v230\n" \
  "v_mul_f32 v5, v5, v230\n" \
  "v_mul_f32 v6, v6, v230\n" \
  "v_mul_f32 v7, v7, v230\n" \
  "v_mul_f32 v8, v8, v230\n" \
  "v_mul_f32 v9, v9, v230\n" \
  "v_mul_f32 v10, v10, v230\n" \
  "v_mul_f32 v11, v11, v230\n" \
  "v_mul_f32 v12, v12, v230\n" \
  "v_mul_f32 v13, v13, v230\n" \
  "v_mul_f32 v14, v14, v230\n" \
  "v_mul_f32 v15, v15, v230\n" \
  "v_mul_f32 v32, v32, v230\n" \
  "v_mul_f32 v33, v33, v230\n" \
  "v_mul_f32 v34, v34, v230\n" \
  "v_mul_f32 v35, v35, v230\n" \
  "v_mul_f32 v36, v36, v230\n" \
  "v_mul_f32 v37, v37, v230\n" \
  "v_mul_f32 v38, v38, v230\n" \
  "v_mul_f32 v39, v39, v230\n" \
  "v_mul_f32 v40, v40, v230\n" \
  "v_mul_f32 v41, v41, v230\n" \
  "v_mul_f32 v42, v42, v230\n" \
  "v_mul_f32 v43, v43, v230\n" \
  "v_mul_f32 v44, v44, v230\n" \
  "v_mul_f32 v45, v45, v230\n" \
  "v_mul_f32 v46, v46, v230\n" \
  "v_mul_f32 v47, v47, v230\n" \
  "v_mul_f32 v64, v64, v230\n" \
  "v_mul_f32 v65, v65, v230\n" \
  "v_mul_f32 v66, v66, v230\n" \
  "v_mul_f32 v67, v67, v230\n" \
  "v_mul_f32 v68, v68, v230\n" \
  "v_mul_f32 v69, v69, v230\n" \
  "v_mul_f32 v70, v70, v230\n" \
  "v_mul_f32 v71, v71, v230\n" \
  "v_mul_f32 v72, v72, v230\n" \
  "v_mul_f32 v73, v73, v230\n" \
  "v_mul_f32 v74, v74, v230\n" \
  "v_mul_f32 v75, v75, v230\n" \
  "v_mul_f32 v76, v76, v230\n" \
  "v_mul_f32 v77, v77, v230\n" \
  "v_mul_f32 v78, v78, v230\n" \
  "v_mul_f32 v79, v79, v230\n" \
  "v_mul_f32 v96, v96, v230\n" \
  "v_mul_f32 v97, v97, v230\n" \
  "v_mul_f32 v98, v98, v230\n" \
  "v_mul_f32 v99, v99, v230\n" \
  "v_mul_f32 v100, v100, v230\n" \
  "v_mul_f32 v101, v101, v230\n" \
  "v_mul_f32 v102, v102, v230\n" \
  "v_mul_f32 v103, v103, v230\n" \
  "v_mul_f32 v104, v104, v230\n" \
  "v_mul_f32 v105, v105, v230\n" \
  "v_mul_f32 v106, v106, v230\n" \
  "v_mul_f32 v107, v107, v230\n" \
  "v_mul_f32 v108, v108, v230\n" \
  "v_mul_f32 v109, v109, v230\n" \
  "v_mul_f32 v110, v110, v230\n" \
  "v_mul_f32 v111, v111, v230\n" \
  "s_waitcnt lgkmcnt(14)\n" \
  "v_mfma_f32_32x32x16_bf16 v[0:15], a[64:67], v[128:131], v[0:15]\n" \
  "s_waitcnt lgkmcnt(12)\n" \
  "v_mfma_f32_32x32x16_bf16 v[32:47], a[68:71], v[128:131], v[32:47]\n" \
  "s_waitcnt lgkmcnt(10)\n" \
  "v_mfma_f32_32x32x16_bf16 v[64:79], a[72:75], v[128:131], v[64:79]\n" \
  "s_waitcnt lgkmcnt(8)\n" \
  "v_mfma_f32_32x32x16_bf16 v[96:111], a[76:79], v[128:131], v[96:111]\n" \
  "s_waitcnt lgkmcnt(6)\n" \
  "v_mfma_f32_32x32x16_bf16 v[0:15], a[80:83], v[136:139], v[0:15]\n" \
  "s_waitcnt lgkmcnt(0)\n" \
  "v_mfma_f32_32x32x16_bf16 v[32:47], a[84:87], v[136:139], v[32:47]\n" \
  "v_mfma_f32_32x32x16_bf16 v[64:79], a[88:91], v[136:139], v[64:79]\n" \
  "v_mfma_f32_32x32x16_bf16 v[96:111], a[92:95], v[136:139], v[96:111]\n" \
  "v_max3_f32 v160, v144, v145, v146\n" \
  "v_max3_f32 v160, v160, v147, v148\n" \
  "v_max3_f32 v160, v160, v149, v150\n" \
  "v_max3_f32 v160, v160, v151, v152\n" \
  "v_max3_f32 v160, v160, v153, v154\n" \
  "v_max3_f32 v160, v160, v155, v156\n" \
  "v_max3_f32 v160, v160, v157, v158\n" \
  "v_max_f32 v160, v160, v159\n" \
  "v_mov_b32 v161, v160\n" \
  "s_nop 1\n" \
  "v_permlane32_swap_b32 v160, v161\n" \
  "v_max_f32 v160, v160, v161\n" \
  "v_max_f32 v160, v160, v229\n" \
  "v_sub_f32 v230, v229, v160\n" \
  "v_exp_f32 v230, v230\n" \
  "v_mov_b32 v229, v160\n" \
  "v_sub_f32 v144, v144, v160\n" \
  "v_sub_f32 v145, v145, v160\n" \
  "v_sub_f32 v146, v146, v160\n" \
  "v_sub_f32 v147, v147, v160\n" \
  "v_sub_f32 v148, v148, v160\n" \
  "v_sub_f32 v149, v149, v160\n" \
  "v_sub_f32 v150, v150, v160\n" \
  "v_sub_f32 v151, v151, v160\n" \
  "v_sub_f32 v152, v152, v160\n" \
  "v_sub_f32 v153, v153, v160\n" \
  "v_sub_f32 v154, v154, v160\n" \
  "v_sub_f32 v155, v155, v160\n" \
  "v_sub_f32 v156, v156, v160\n" \
  "v_sub_f32 v157, v157, v160\n" \
  "v_sub_f32 v158, v158, v160\n" \
  "v_sub_f32 v159, v159, v160\n" \
  "v_exp_f32 v144, v144\n" \
  "v_exp_f32 v145, v145\n" \
  "v_exp_f32 v146, v146\n" \
  "v_exp_f32 v147, v147\n" \
  "v_exp_f32 v148, v148\n" \
  "v_exp_f32 v149, v149\n" \
  "v_exp_f32 v150, v150\n" \
  "v_exp_f32 v151, v151\n" \
  "v_exp_f32 v152, v152\n" \
  "v_exp_f32 v153, v153\n" \
  "v_exp_f32 v154, v154\n" \
  "v_exp_f32 v155, v155\n" \
  "v_exp_f32 v156, v156\n" \
  "v_exp_f32 v157, v157\n" \
  "v_exp_f32 v158, v158\n" \
  "v_exp_f32 v159, v159\n" \
  "v_mul_f32 v226, v226, v230\n" \
  "v_add_f32 v226, v226, v144\n" \
  "v_add_f32 v226, v226, v145\n" \
  "v_add_f32 v226, v226, v146\n" \
  "v_add_f32 v226, v226, v147\n" \
  "v_add_f32 v226, v226, v148\n" \
  "v_add_f32 v226, v226, v149\n" \
  "v_add_f32 v226, v226, v150\n" \
  "v_add_f32 v226, v226, v151\n" \
  "v_add_f32 v226, v226, v152\n" \
  "v_add_f32 v226, v226, v153\n" \
  "v_add_f32 v226, v226, v154\n" \
  "v_add_f32 v226, v226, v155\n" \
  "v_add_f32 v226, v226, v156\n" \
  "v_add_f32 v226, v226, v157\n" \
  "v_add_f32 v226, v226, v158\n" \
  "v_add_f32 v226, v226, v159\n" \
  "v_cvt_pk_bf16_f32 v144, v144, v145\n" \
  "v_cvt_pk_bf16_f32 v145, v146, v147\n" \
  "v_cvt_pk_bf16_f32 v146, v148, v149\n" \
  "v_cvt_pk_bf16_f32 v147, v150, v151\n" \
  "v_cvt_pk_bf16_f32 v152, v152, v153\n" \
  "v_cvt_pk_bf16_f32 v153, v154, v155\n" \
  "v_cvt_pk_bf16_f32 v154, v156, v157\n" \
  "v_cvt_pk_bf16_f32 v155, v158, v159\n" \
  "v_mul_f32 v16, v16, v230\n" \
  "v_mul_f32 v17, v17, v230\n" \
  "v_mul_f32 v18, v18, v230\n" \
  "v_mul_f32 v19, v19, v230\n" \
  "v_mul_f32 v20, v20, v230\n" \
  "v_mul_f32 v21, v21, v230\n" \
  "v_mul_f32 v22, v22, v230\n" \
  "v_mul_f32 v23, v23, v230\n" \
  "v_mul_f32 v24, v24, v230\n" \
  "v_mul_f32 v25, v25, v230\n" \
  "v_mul_f32 v26, v26, v230\n" \
  "v_mul_f32 v27, v27, v230\n" \
  "v_mul_f32 v28, v28, v230\n" \
  "v_mul_f32 v29, v29, v230\n" \
  "v_mul_f32 v30, v30, v230\n" \
  "v_mul_f32 v31, v31, v230\n" \
  "v_mul_f32 v48, v48, v230\n" \
  "v_mul_f32 v49, v49, v230\n" \
  "v_mul_f32 v50, v50, v230\n" \
  "v_mul_f32 v51, v51, v230\n" \
  "v_mul_f32 v52, v52, v230\n" \
  "v_mul_f32 v53, v53, v230\n" \
  "v_mul_f32 v54, v54, v230\n" \
  "v_mul_f32 v55, v55, v230\n" \
  "v_mul_f32 v56, v56, v230\n" \
  "v_mul_f32 v57, v57, v230\n" \
  "v_mul_f32 v58, v58, v230\n" \
  "v_mul_f32 v59, v59, v230\n" \
  "v_mul_f32 v60, v60, v230\n" \
  "v_mul_f32 v61, v61, v230\n" \
  "v_mul_f32 v62, v62, v230\n" \
  "v_mul_f32 v63, v63, v230\n" \
  "v_mul_f32 v80, v80, v230\n" \
  "v_mul_f32 v81, v81, v230\n" \
  "v_mul_f32 v82, v82, v230\n" \
  "v_mul_f32 v83, v83, v230\n" \
  "v_mul_f32 v84, v84, v230\n" \
  "v_mul_f32 v85, v85, v230\n" \
  "v_mul_f32 v86, v86, v230\n" \
  "v_mul_f32 v87, v87, v230\n" \
  "v_mul_f32 v88, v88, v230\n" \
  "v_mul_f32 v89, v89, v230\n" \
  "v_mul_f32 v90, v90, v230\n" \
  "v_mul_f32 v91, v91, v230\n" \
  "v_mul_f32 v92, v92, v230\n" \
  "v_mul_f32 v93, v93, v230\n" \
  "v_mul_f32 v94, v94, v230\n" \
  "v_mul_f32 v95, v95, v230\n" \
  "v_mul_f32 v112, v112, v230\n" \
  "v_mul_f32 v113, v113, v230\n" \
  "v_mul_f32 v114, v114, v230\n" \
  "v_mul_f32 v115, v115, v230\n" \
  "v_mul_f32 v116, v116, v230\n" \
  "v_mul_f32 v117, v117, v230\n" \
  "v_mul_f32 v118, v118, v230\n" \
  "v_mul_f32 v119, v119, v230\n" \
  "v_mul_f32 v120, v120, v230\n" \
  "v_mul_f32 v121, v121, v230\n" \
  "v_mul_f32 v122, v122, v230\n" \
  "v_mul_f32 v123, v123, v230\n" \
  "v_mul_f32 v124, v124, v230\n" \
  "v_mul_f32 v125, v125, v230\n" \
  "v_mul_f32 v126, v126, v230\n" \
  "v_mul_f32 v127, v127, v230\n" \
  "v_mfma_f32_32x32x16_bf16 v[16:31], a[64:67], v[144:147], v[16:31]\n" \
  "v_mfma_f32_32x32x16_bf16 v[48:63], a[68:71], v[144:147], v[48:63]\n" \
  "v_mfma_f32_32x32x16_bf16 v[80:95], a[72:75], v[144:147], v[80:95]\n" \
  "v_mfma_f32_32x32x16_bf16 v[112:127], a[76:79], v[144:147], v[112:127]\n" \
  "v_mfma_f32_32x32x16_bf16 v[16:31], a[80:83], v[152:155], v[16:31]\n" \
  "v_mfma_f32_32x32x16_bf16 v[48:63], a[84:87], v[152:155], v[48:63]\n" \
  "v_mfma_f32_32x32x16_bf16 v[80:95], a[88:91], v[152:155], v[80:95]\n" \
  "v_mfma_f32_32x32x16_bf16 v[112:127], a[92:95], v[152:155], v[112:127]\n" \
  "s_nop 15\n" \
  "s_add_u32 s46, s46, 8192\n" \
  "s_cmp_lt_u32 s46, 131072\n" \
  "s_cbranch_scc1 .Lfb_loop_%=\n" \
  "s_nop 15\n" \
  "v_mov_b32 v225, 0\n" \
  "v_add_f32 v228, v224, v225\n" \
  "v_mov_b32 v229, v228\n" \
  "s_nop 1\n" \
  "v_permlane32_swap_b32 v228, v229\n" \
  "v_add_f32 v224, v228, v229\n" \
  "v_rcp_f32 v228, v224\n" \
  "s_nop 0\n" \
  "v_mov_b32 v229, v228\n" \
  "v_mov_b32 v227, 0\n" \
  "v_add_f32 v230, v226, v227\n" \
  "v_mov_b32 v231, v230\n" \
  "s_nop 1\n" \
  "v_permlane32_swap_b32 v230, v231\n" \
  "v_add_f32 v226, v230, v231\n" \
  "v_rcp_f32 v230, v226\n" \
  "s_nop 0\n" \
  "v_mov_b32 v231, v230\n" \
  "v_mul_f32 v0, v0, v228\n" \
  "v_mul_f32 v1, v1, v228\n" \
  "v_mul_f32 v2, v2, v228\n" \
  "v_mul_f32 v3, v3, v228\n" \
  "v_mul_f32 v4, v4, v228\n" \
  "v_mul_f32 v5, v5, v228\n" \
  "v_mul_f32 v6, v6, v228\n" \
  "v_mul_f32 v7, v7, v228\n" \
  "v_mul_f32 v8, v8, v228\n" \
  "v_mul_f32 v9, v9, v228\n" \
  "v_mul_f32 v10, v10, v228\n" \
  "v_mul_f32 v11, v11, v228\n" \
  "v_mul_f32 v12, v12, v228\n" \
  "v_mul_f32 v13, v13, v228\n" \
  "v_mul_f32 v14, v14, v228\n" \
  "v_mul_f32 v15, v15, v228\n" \
  "v_cvt_pk_bf16_f32 v0, v0, v1\n" \
  "v_cvt_pk_bf16_f32 v1, v2, v3\n" \
  "v_cvt_pk_bf16_f32 v2, v4, v5\n" \
  "v_cvt_pk_bf16_f32 v3, v6, v7\n" \
  "s_mov_b32 s43, 0\n" \
  "buffer_store_dwordx4 v[0:3], v232, %[srdO], s43 offen offset:0 sc1\n" \
  "v_cvt_pk_bf16_f32 v8, v8, v9\n" \
  "v_cvt_pk_bf16_f32 v9, v10, v11\n" \
  "v_cvt_pk_bf16_f32 v10, v12, v13\n" \
  "v_cvt_pk_bf16_f32 v11, v14, v15\n" \
  "s_mov_b32 s43, 0\n" \
  "buffer_store_dwordx4 v[8:11], v232, %[srdO], s43 offen offset:1024 sc1\n" \
  "v_mul_f32 v32, v32, v228\n" \
  "v_mul_f32 v33, v33, v228\n" \
  "v_mul_f32 v34, v34, v228\n" \
  "v_mul_f32 v35, v35, v228\n" \
  "v_mul_f32 v36, v36, v228\n" \
  "v_mul_f32 v37, v37, v228\n" \
  "v_mul_f32 v38, v38, v228\n" \
  "v_mul_f32 v39, v39, v228\n" \
  "v_mul_f32 v40, v40, v228\n" \
  "v_mul_f32 v41, v41, v228\n" \
  "v_mul_f32 v42, v42, v228\n" \
  "v_mul_f32 v43, v43, v228\n" \
  "v_mul_f32 v44, v44, v228\n" \
  "v_mul_f32 v45, v45, v228\n" \
  "v_mul_f32 v46, v46, v228\n" \
  "v_mul_f32 v47, v47, v228\n" \
  "v_cvt_pk_bf16_f32 v32, v32, v33\n" \
  "v_cvt_pk_bf16_f32 v33, v34, v35\n" \
  "v_cvt_pk_bf16_f32 v34, v36, v37\n" \
  "v_cvt_pk_bf16_f32 v35, v38, v39\n" \
  "s_mov_b32 s43, 0\n" \
  "buffer_store_dwordx4 v[32:35], v232, %[srdO], s43 offen offset:2048 sc1\n" \
  "v_cvt_pk_bf16_f32 v40, v40, v41\n" \
  "v_cvt_pk_bf16_f32 v41, v42, v43\n" \
  "v_cvt_pk_bf16_f32 v42, v44, v45\n" \
  "v_cvt_pk_bf16_f32 v43, v46, v47\n" \
  "s_mov_b32 s43, 0\n" \
  "buffer_store_dwordx4 v[40:43], v232, %[srdO], s43 offen offset:3072 sc1\n" \
  "v_mul_f32 v64, v64, v228\n" \
  "v_mul_f32 v65, v65, v228\n" \
  "v_mul_f32 v66, v66, v228\n" \
  "v_mul_f32 v67, v67, v228\n" \
  "v_mul_f32 v68, v68, v228\n" \
  "v_mul_f32 v69, v69, v228\n" \
  "v_mul_f32 v70, v70, v228\n" \
  "v_mul_f32 v71, v71, v228\n" \
  "v_mul_f32 v72, v72, v228\n" \
  "v_mul_f32 v73, v73, v228\n" \
  "v_mul_f32 v74, v74, v228\n" \
  "v_mul_f32 v75, v75, v228\n" \
  "v_mul_f32 v76, v76, v228\n" \
  "v_mul_f32 v77, v77, v228\n" \
  "v_mul_f32 v78, v78, v228\n" \
  "v_mul_f32 v79, v79, v228\n" \
  "v_cvt_pk_bf16_f32 v64, v64, v65\n" \
  "v_cvt_pk_bf16_f32 v65, v66, v67\n" \
  "v_cvt_pk_bf16_f32 v66, v68, v69\n" \
  "v_cvt_pk_bf16_f32 v67, v70, v71\n" \
  "s_mov_b32 s43, 4096\n" \
  "buffer_store_dwordx4 v[64:67], v232, %[srdO], s43 offen offset:0 sc1\n" \
  "v_cvt_pk_bf16_f32 v72, v72, v73\n" \
  "v_cvt_pk_bf16_f32 v73, v74, v75\n" \
  "v_cvt_pk_bf16_f32 v74, v76, v77\n" \
  "v_cvt_pk_bf16_f32 v75, v78, v79\n" \
  "s_mov_b32 s43, 4096\n" \
  "buffer_store_dwordx4 v[72:75], v232, %[srdO], s43 offen offset:1024 sc1\n" \
  "v_mul_f32 v96, v96, v228\n" \
  "v_mul_f32 v97, v97, v228\n" \
  "v_mul_f32 v98, v98, v228\n" \
  "v_mul_f32 v99, v99, v228\n" \
  "v_mul_f32 v100, v100, v228\n" \
  "v_mul_f32 v101, v101, v228\n" \
  "v_mul_f32 v102, v102, v228\n" \
  "v_mul_f32 v103, v103, v228\n" \
  "v_mul_f32 v104, v104, v228\n" \
  "v_mul_f32 v105, v105, v228\n" \
  "v_mul_f32 v106, v106, v228\n" \
  "v_mul_f32 v107, v107, v228\n" \
  "v_mul_f32 v108, v108, v228\n" \
  "v_mul_f32 v109, v109, v228\n" \
  "v_mul_f32 v110, v110, v228\n" \
  "v_mul_f32 v111, v111, v228\n" \
  "v_cvt_pk_bf16_f32 v96, v96, v97\n" \
  "v_cvt_pk_bf16_f32 v97, v98, v99\n" \
  "v_cvt_pk_bf16_f32 v98, v100, v101\n" \
  "v_cvt_pk_bf16_f32 v99, v102, v103\n" \
  "s_mov_b32 s43, 4096\n" \
  "buffer_store_dwordx4 v[96:99], v232, %[srdO], s43 offen offset:2048 sc1\n" \
  "v_cvt_pk_bf16_f32 v104, v104, v105\n" \
  "v_cvt_pk_bf16_f32 v105, v106, v107\n" \
  "v_cvt_pk_bf16_f32 v106, v108, v109\n" \
  "v_cvt_pk_bf16_f32 v107, v110, v111\n" \
  "s_mov_b32 s43, 4096\n" \
  "buffer_store_dwordx4 v[104:107], v232, %[srdO], s43 offen offset:3072 sc1\n" \
  "v_mul_f32 v16, v16, v230\n" \
  "v_mul_f32 v17, v17, v230\n" \
  "v_mul_f32 v18, v18, v230\n" \
  "v_mul_f32 v19, v19, v230\n" \
  "v_mul_f32 v20, v20, v230\n" \
  "v_mul_f32 v21, v21, v230\n" \
  "v_mul_f32 v22, v22, v230\n" \
  "v_mul_f32 v23, v23, v230\n" \
  "v_mul_f32 v24, v24, v230\n" \
  "v_mul_f32 v25, v25, v230\n" \
  "v_mul_f32 v26, v26, v230\n" \
  "v_mul_f32 v27, v27, v230\n" \
  "v_mul_f32 v28, v28, v230\n" \
  "v_mul_f32 v29, v29, v230\n" \
  "v_mul_f32 v30, v30, v230\n" \
  "v_mul_f32 v31, v31, v230\n" \
  "v_cvt_pk_bf16_f32 v16, v16, v17\n" \
  "v_cvt_pk_bf16_f32 v17, v18, v19\n" \
  "v_cvt_pk_bf16_f32 v18, v20, v21\n" \
  "v_cvt_pk_bf16_f32 v19, v22, v23\n" \
  "s_mov_b32 s43, 262144\n" \
  "buffer_store_dwordx4 v[16:19], v232, %[srdO], s43 offen offset:0 sc1\n" \
  "v_cvt_pk_bf16_f32 v24, v24, v25\n" \
  "v_cvt_pk_bf16_f32 v25, v26, v27\n" \
  "v_cvt_pk_bf16_f32 v26, v28, v29\n" \
  "v_cvt_pk_bf16_f32 v27, v30, v31\n" \
  "s_mov_b32 s43, 262144\n" \
  "buffer_store_dwordx4 v[24:27], v232, %[srdO], s43 offen offset:1024 sc1\n" \
  "v_mul_f32 v48, v48, v230\n" \
  "v_mul_f32 v49, v49, v230\n" \
  "v_mul_f32 v50, v50, v230\n" \
  "v_mul_f32 v51, v51, v230\n" \
  "v_mul_f32 v52, v52, v230\n" \
  "v_mul_f32 v53, v53, v230\n" \
  "v_mul_f32 v54, v54, v230\n" \
  "v_mul_f32 v55, v55, v230\n" \
  "v_mul_f32 v56, v56, v230\n" \
  "v_mul_f32 v57, v57, v230\n" \
  "v_mul_f32 v58, v58, v230\n" \
  "v_mul_f32 v59, v59, v230\n" \
  "v_mul_f32 v60, v60, v230\n" \
  "v_mul_f32 v61, v61, v230\n" \
  "v_mul_f32 v62, v62, v230\n" \
  "v_mul_f32 v63, v63, v230\n" \
  "v_cvt_pk_bf16_f32 v48, v48, v49\n" \
  "v_cvt_pk_bf16_f32 v49, v50, v51\n" \
  "v_cvt_pk_bf16_f32 v50, v52, v53\n" \
  "v_cvt_pk_bf16_f32 v51, v54, v55\n" \
  "s_mov_b32 s43, 262144\n" \
  "buffer_store_dwordx4 v[48:51], v232, %[srdO], s43 offen offset:2048 sc1\n" \
  "v_cvt_pk_bf16_f32 v56, v56, v57\n" \
  "v_cvt_pk_bf16_f32 v57, v58, v59\n" \
  "v_cvt_pk_bf16_f32 v58, v60, v61\n" \
  "v_cvt_pk_bf16_f32 v59, v62, v63\n" \
  "s_mov_b32 s43, 262144\n" \
  "buffer_store_dwordx4 v[56:59], v232, %[srdO], s43 offen offset:3072 sc1\n" \
  "v_mul_f32 v80, v80, v230\n" \
  "v_mul_f32 v81, v81, v230\n" \
  "v_mul_f32 v82, v82, v230\n" \
  "v_mul_f32 v83, v83, v230\n" \
  "v_mul_f32 v84, v84, v230\n" \
  "v_mul_f32 v85, v85, v230\n" \
  "v_mul_f32 v86, v86, v230\n" \
  "v_mul_f32 v87, v87, v230\n" \
  "v_mul_f32 v88, v88, v230\n" \
  "v_mul_f32 v89, v89, v230\n" \
  "v_mul_f32 v90, v90, v230\n" \
  "v_mul_f32 v91, v91, v230\n" \
  "v_mul_f32 v92, v92, v230\n" \
  "v_mul_f32 v93, v93, v230\n" \
  "v_mul_f32 v94, v94, v230\n" \
  "v_mul_f32 v95, v95, v230\n" \
  "v_cvt_pk_bf16_f32 v80, v80, v81\n" \
  "v_cvt_pk_bf16_f32 v81, v82, v83\n" \
  "v_cvt_pk_bf16_f32 v82, v84, v85\n" \
  "v_cvt_pk_bf16_f32 v83, v86, v87\n" \
  "s_mov_b32 s43, 266240\n" \
  "buffer_store_dwordx4 v[80:83], v232, %[srdO], s43 offen offset:0 sc1\n" \
  "v_cvt_pk_bf16_f32 v88, v88, v89\n" \
  "v_cvt_pk_bf16_f32 v89, v90, v91\n" \
  "v_cvt_pk_bf16_f32 v90, v92, v93\n" \
  "v_cvt_pk_bf16_f32 v91, v94, v95\n" \
  "s_mov_b32 s43, 266240\n" \
  "buffer_store_dwordx4 v[88:91], v232, %[srdO], s43 offen offset:1024 sc1\n" \
  "v_mul_f32 v112, v112, v230\n" \
  "v_mul_f32 v113, v113, v230\n" \
  "v_mul_f32 v114, v114, v230\n" \
  "v_mul_f32 v115, v115, v230\n" \
  "v_mul_f32 v116, v116, v230\n" \
  "v_mul_f32 v117, v117, v230\n" \
  "v_mul_f32 v118, v118, v230\n" \
  "v_mul_f32 v119, v119, v230\n" \
  "v_mul_f32 v120, v120, v230\n" \
  "v_mul_f32 v121, v121, v230\n" \
  "v_mul_f32 v122, v122, v230\n" \
  "v_mul_f32 v123, v123, v230\n" \
  "v_mul_f32 v124, v124, v230\n" \
  "v_mul_f32 v125, v125, v230\n" \
  "v_mul_f32 v126, v126, v230\n" \
  "v_mul_f32 v127, v127, v230\n" \
  "v_cvt_pk_bf16_f32 v112, v112, v113\n" \
  "v_cvt_pk_bf16_f32 v113, v114, v115\n" \
  "v_cvt_pk_bf16_f32 v114, v116, v117\n" \
  "v_cvt_pk_bf16_f32 v115, v118, v119\n" \
  "s_mov_b32 s43, 266240\n" \
  "buffer_store_dwordx4 v[112:115], v232, %[srdO], s43 offen offset:2048 sc1\n" \
  "v_cvt_pk_bf16_f32 v120, v120, v121\n" \
  "v_cvt_pk_bf16_f32 v121, v122, v123\n" \
  "v_cvt_pk_bf16_f32 v122, v124, v125\n" \
  "v_cvt_pk_bf16_f32 v123, v126, v127\n" \
  "s_mov_b32 s43, 266240\n" \
  "buffer_store_dwordx4 v[120:123], v232, %[srdO], s43 offen offset:3072 sc1\n" \
  "s_waitcnt vmcnt(0)\n" \
  "v_mov_b32 v228, %[tag]\n" \
  "v_mov_b32 v229, 0\n" \
  "s_mov_b64 exec, 1\n" \
  "global_store_dword v229, v228, %[fo] sc1\n" \
  "s_mov_b64 exec, -1\n" \
  ".Lfb_skip_%=:\n"

#define T1_ASM \
  "s_nop 4\n" \
  "v_mbcnt_lo_u32_b32 v0, -1, 0\n" \
  "v_mbcnt_hi_u32_b32 v0, -1, v0\n" \
  "v_lshlrev_b32 v140, 4, v0\n" \
  "s_mov_b32 s42, 0\n" \
  "buffer_load_dwordx4 a[0:3], v140, %[srdB], s42 offen offset:0 sc1\n" \
  "buffer_load_dwordx4 a[4:7], v140, %[srdB], s42 offen offset:1024 sc1\n" \
  "buffer_load_dwordx4 a[8:11], v140, %[srdB], s42 offen offset:2048 sc1\n" \
  "buffer_load_dwordx4 a[12:15], v140, %[srdB], s42 offen offset:3072 sc1\n" \
  "s_mov_b32 s42, 4096\n" \
  "buffer_load_dwordx4 a[16:19], v140, %[srdB], s42 offen offset:0 sc1\n" \
  "buffer_load_dwordx4 a[20:23], v140, %[srdB], s42 offen offset:1024 sc1\n" \
  "buffer_load_dwordx4 a[24:27], v140, %[srdB], s42 offen offset:2048 sc1\n" \
  "buffer_load_dwordx4 a[28:31], v140, %[srdB], s42 offen offset:3072 sc1\n" \
  "s_mov_b32 s42, 8192\n" \
  "buffer_load_dwordx4 a[32:35], v140, %[srdB], s42 offen offset:0 sc1\n" \
  "buffer_load_dwordx4 a[36:39], v140, %[srdB], s42 offen offset:1024 sc1\n" \
  "buffer_load_dwordx4 a[40:43], v140, %[srdB], s42 offen offset:2048 sc1\n" \
  "buffer_load_dwordx4 a[44:47], v140, %[srdB], s42 offen offset:3072 sc1\n" \
  "s_mul_i32 s60, %[w], 10240\n" \
  "s_mul_i32 s62, %[w], 10752\n" \
  "s_cmp_ge_u32 %[w], 2\n" \
  "s_cselect_b32 s41, 60416, 0\n" \
  "s_add_u32 s62, s62, s41\n" \
  "s_mov_b32 s56, 0x05040100\n" \
  "s_mov_b32 s57, 0x07060302\n" \
  "v_add_u32 v141, s60, v140\n" \
  "v_mov_b32 v142, v140\n" \
  "v_add_u32 v143, 65536, v140\n" \
  "v_add_u32 v144, 131072, v140\n" \
  "v_and_b32 v1, 31, v0\n" \
  "v_lshrrev_b32 v2, 5, v0\n" \
  "v_mul_u32_u24 v1, 336, v1\n" \
  "v_lshl_add_u32 v145, v2, 3, v1\n" \
  "v_add_u32 v145, s62, v145\n" \
  "v_mul_u32_u24 v3, 13, v0\n" \
  "v_lshrrev_b32 v3, 8, v3\n" \
  "v_mul_u32_u24 v4, 20, v3\n" \
  "v_sub_u32 v4, v0, v4\n" \
  "v_mul_u32_u24 v5, 336, v3\n" \
  "v_lshl_add_u32 v146, v4, 4, v5\n" \
  "v_add_u32 v146, s62, v146\n" \
  "v_mul_u32_u24 v5, 1280, v3\n" \
  "v_lshl_add_u32 v147, v4, 4, v5\n" \
  "v_cmp_gt_u32 vcc, 60, v0\n" \
  "v_mov_b32 v6, 0x7fff0000\n" \
  "s_nop 1\n" \
  "v_cndmask_b32 v147, v6, v147, vcc\n" \
  "v_and_b32 v7, 7, v0\n" \
  "v_bfe_u32 v8, v0, 3, 2\n" \
  "v_lshlrev_b32 v9, 5, v2\n" \
  "v_lshl_add_u32 v10, v8, 3, v9\n" \
  "v_mul_u32_u24 v10, 3456, v10\n" \
  "v_lshl_add_u32 v148, v7, 2, v10\n" \
  "v_lshl_add_u32 v10, v8, 2, v9\n" \
  "v_mul_u32_u24 v10, 1280, v10\n" \
  "v_lshl_add_u32 v149, v7, 2, v10\n" \
  "v_lshlrev_b32 v11, 5, v7\n" \
  "v_lshl_add_u32 v11, v8, 8, v11\n" \
  "v_mul_u32_u24 v12, 7168, v2\n" \
  "v_add_u32 v150, v11, v12\n" \
  "v_mul_u32_u24 v12, 5120, v2\n" \
  "v_add_u32 v151, v11, v12\n" \
  "ds_read_b128 v[80:83], v143 offset:16384\n" \
  "ds_read_b128 v[84:87], v143 offset:17408\n" \
  "ds_read_b128 v[88:91], v143 offset:18432\n" \
  "ds_read_b128 v[92:95], v143 offset:19456\n" \
  "ds_read_b128 v[96:99], v143 offset:20480\n" \
  "ds_read_b128 v[100:103], v143 offset:21504\n" \
  "ds_read_b128 v[104:107], v143 offset:22528\n" \
  "ds_read_b128 v[108:111], v143 offset:23552\n" \
  "ds_read_b128 v[112:115], v143 offset:24576\n" \
  "ds_read_b128 v[116:119], v143 offset:25600\n" \
  "s_waitcnt vmcnt(11) lgkmcnt(9)\n" \
  "v_mfma_f32_32x32x16_bf16 v[0:15], v[80:83], a[0:3], 0\n" \
  "ds_read_b128 v[120:123], v143 offset:26624\n" \
  "s_waitcnt lgkmcnt(9)\n" \
  "v_mfma_f32_32x32x16_bf16 v[16:31], v[84:87], a[0:3], 0\n" \
  "s_add_u32 m0, s60, 40960\n" \
  "ds_read_b128 v[124:127], v143 offset:27648\n" \
  "s_waitcnt lgkmcnt(9)\n" \
  "v_mfma_f32_32x32x16_bf16 v[32:47], v[88:91], a[0:3], 0\n" \
  "ds_read_b128 v[128:131], v143 offset:28672\n" \
  "s_mov_b32 s42, 12288\n" \
  "s_waitcnt lgkmcnt(9)\n" \
  "v_mfma_f32_32x32x16_bf16 v[48:63], v[92:95], a[0:3], 0\n" \
  "s_mov_b32 s40, 81920\n" \
  "ds_read_b128 v[132:135], v143 offset:29696\n" \
  "s_waitcnt lgkmcnt(9)\n" \
  "v_mfma_f32_32x32x16_bf16 v[64:79], v[96:99], a[0:3], 0\n" \
  "ds_read_b128 v[136:139], v143 offset:30720\n" \
  "buffer_load_dwordx4 a[48:51], v140, %[srdB], s42 offen offset:0 sc1\n" \
  "s_waitcnt vmcnt(11) lgkmcnt(5)\n" \
  "v_mfma_f32_32x32x16_bf16 v[0:15], v[100:103], a[4:7], v[0:15]\n" \
  "buffer_load_dwordx4 v141, %[srdA], s40 offen offset:0 sc1 lds\n" \
  "ds_read_b128 v[80:83], v143 offset:31744\n" \
  "v_mfma_f32_32x32x16_bf16 v[16:31], v[104:107], a[4:7], v[16:31]\n" \
  "ds_read_b128 v[84:87], v143 offset:32768\n" \
  "v_mfma_f32_32x32x16_bf16 v[32:47], v[108:111], a[4:7], v[32:47]\n" \
  "buffer_load_dwordx4 v141, %[srdA], s40 offen offset:1024 sc1 lds\n" \
  "ds_read_b128 v[88:91], v143 offset:33792\n" \
  "s_mov_b32 s42, 12288\n" \
  "v_mfma_f32_32x32x16_bf16 v[48:63], v[112:115], a[4:7], v[48:63]\n" \
  "ds_read_b128 v[92:95], v143 offset:34816\n" \
  "v_mfma_f32_32x32x16_bf16 v[64:79], v[116:119], a[4:7], v[64:79]\n" \
  "buffer_load_dwordx4 v141, %[srdA], s40 offen offset:2048 sc1 lds\n" \
  "ds_read_b128 v[96:99], v143 offset:35840\n" \
  "buffer_load_dwordx4 a[52:55], v140, %[srdB], s42 offen offset:1024 sc1\n" \
  "s_waitcnt vmcnt(14) lgkmcnt(5)\n" \
  "v_mfma_f32_32x32x16_bf16 v[0:15], v[120:123], a[8:11], v[0:15]\n" \
  "ds_read_b128 v[100:103], v143 offset:36864\n" \
  "v_mfma_f32_32x32x16_bf16 v[16:31], v[124:127], a[8:11], v[16:31]\n" \
  "buffer_load_dwordx4 v141, %[srdA], s40 offen offset:3072 sc1 lds\n" \
  "ds_read_b128 v[104:107], v143 offset:37888\n" \
  "v_mfma_f32_32x32x16_bf16 v[32:47], v[128:131], a[8:11], v[32:47]\n" \
  "ds_read_b128 v[108:111], v143 offset:38912\n" \
  "s_mov_b32 s42, 12288\n" \
  "v_mfma_f32_32x32x16_bf16 v[48:63], v[132:135], a[8:11], v[48:63]\n" \
  "s_add_u32 m0, s60, 45056\n" \
  "ds_read_b128 v[112:115], v143 offset:39936\n" \
  "v_mfma_f32_32x32x16_bf16 v[64:79], v[136:139], a[8:11], v[64:79]\n" \
  "s_mov_b32 s40, 86016\n" \
  "ds_read_b128 v[116:119], v143 offset:40960\n" \
  "buffer_load_dwordx4 a[56:59], v140, %[srdB], s42 offen offset:2048 sc1\n" \
  "s_waitcnt vmcnt(15) lgkmcnt(5)\n" \
  "v_mfma_f32_32x32x16_bf16 v[0:15], v[80:83], a[12:15], v[0:15]\n" \
  "ds_read_b128 v[120:123], v143 offset:41984\n" \
  "v_mfma_f32_32x32x16_bf16 v[16:31], v[84:87], a[12:15], v[16:31]\n" \
  "buffer_load_dwordx4 v141, %[srdA], s40 offen offset:0 sc1 lds\n" \
  "ds_read_b128 v[124:127], v143 offset:43008\n" \
  "v_mfma_f32_32x32x16_bf16 v[32:47], v[88:91], a[12:15], v[32:47]\n" \
  "ds_read_b128 v[128:131], v143 offset:44032\n" \
  "s_mov_b32 s42, 12288\n" \
  "v_mfma_f32_32x32x16_bf16 v[48:63], v[92:95], a[12:15], v[48:63]\n" \
  "buffer_load_dwordx4 v141, %[srdA], s40 offen offset:1024 sc1 lds\n" \
  "ds_read_b128 v[132:135], v143 offset:45056\n" \
  "v_mfma_f32_32x32x16_bf16 v[64:79], v[96:99], a[12:15], v[64:79]\n" \
  "ds_read_b128 v[136:139], v143 offset:46080\n" \
  "buffer_load_dwordx4 a[60:63], v140, %[srdB], s42 offen offset:3072 sc1\n" \
  "s_waitcnt vmcnt(17) lgkmcnt(5)\n" \
  "v_mfma_f32_32x32x16_bf16 v[0:15], v[100:103], a[16:19], v[0:15]\n" \
  "buffer_load_dwordx4 v141, %[srdA], s40 offen offset:2048 sc1 lds\n" \
  "ds_read_b128 v[80:83], v143 offset:47104\n" \
  "v_mfma_f32_32x32x16_bf16 v[16:31], v[104:107], a[16:19], v[16:31]\n" \
  "ds_read_b128 v[84:87], v143 offset:48128\n" \
  "v_mfma_f32_32x32x16_bf16 v[32:47], v[108:111], a[16:19], v[32:47]\n" \
  "buffer_load_dwordx4 v141, %[srdA], s40 offen offset:3072 sc1 lds\n" \
  "ds_read_b128 v[88:91], v143 offset:49152\n" \
  "s_mov_b32 s42, 16384\n" \
  "v_mfma_f32_32x32x16_bf16 v[48:63], v[112:115], a[16:19], v[48:63]\n" \
  "ds_read_b128 v[92:95], v143 offset:50176\n" \
  "v_mfma_f32_32x32x16_bf16 v[64:79], v[116:119], a[16:19], v[64:79]\n" \
  "s_add_u32 m0, s60, 49152\n" \
  "ds_read_b128 v[96:99], v143 offset:51200\n" \
  "buffer_load_dwordx4 a[64:67], v140, %[srdB], s42 offen offset:0 sc1\n" \
  "s_waitcnt vmcnt(19) lgkmcnt(5)\n" \
  "v_mfma_f32_32x32x16_bf16 v[0:15], v[120:123], a[20:23], v[0:15]\n" \
  "ds_read_b128 v[100:103], v143 offset:52224\n" \
  "v_mfma_f32_32x32x16_bf16 v[16:31], v[124:127], a[20:23], v[16:31]\n" \
  "s_mov_b32 s40, 90112\n" \
  "ds_read_b128 v[104:107], v143 offset:53248\n" \
  "v_mfma_f32_32x32x16_bf16 v[32:47], v[128:131], a[20:23], v[32:47]\n" \
  "ds_read_b128 v[108:111], v143 offset:54272\n" \
  "s_mov_b32 s42, 16384\n" \
  "v_mfma_f32_32x32x16_bf16 v[48:63], v[132:135], a[20:23], v[48:63]\n" \
  "buffer_load_dwordx4 v141, %[srdA], s40 offen offset:0 sc1 lds\n" \
  "ds_read_b128 v[112:115], v143 offset:55296\n" \
  "v_mfma_f32_32x32x16_bf16 v[64:79], v[136:139], a[20:23], v[64:79]\n" \
  "buffer_load_dwordx4 v141, %[srdA], s40 offen offset:1024 sc1 lds\n" \
  "ds_read_b128 v[116:119], v143 offset:56320\n" \
  "buffer_load_dwordx4 a[68:71], v140, %[srdB], s42 offen offset:1024 sc1\n" \
  "s_waitcnt vmcnt(21) lgkmcnt(5)\n" \
  "v_mfma_f32_32x32x16_bf16 v[0:15], v[80:83], a[24:27], v[0:15]\n" \
  "ds_read_b128 v[120:123], v142 offset:0\n" \
  "v_mfma_f32_32x32x16_bf16 v[16:31], v[84:87], a[24:27], v[16:31]\n" \
  "ds_read_b128 v[124:127], v142 offset:1024\n" \
  "v_mfma_f32_32x32x16_bf16 v[32:47], v[88:91], a[24:27], v[32:47]\n" \
  "ds_read_b128 v[128:131], v142 offset:2048\n" \
  "s_mov_b32 s42, 16384\n" \
  "v_mfma_f32_32x32x16_bf16 v[48:63], v[92:95], a[24:27], v[48:63]\n" \
  "ds_read_b128 v[132:135], v142 offset:3072\n" \
  "v_mfma_f32_32x32x16_bf16 v[64:79], v[96:99], a[24:27], v[64:79]\n" \
  "ds_read_b128 v[136:139], v142 offset:4096\n" \
  "buffer_load_dwordx4 a[72:75], v140, %[srdB], s42 offen offset:2048 sc1\n" \
  "s_waitcnt vmcnt(21) lgkmcnt(5)\n" \
  "v_mfma_f32_32x32x16_bf16 v[0:15], v[100:103], a[28:31], v[0:15]\n" \
  "ds_read_b128 v[80:83], v142 offset:5120\n" \
  "v_mfma_f32_32x32x16_bf16 v[16:31], v[104:107], a[28:31], v[16:31]\n" \
  "ds_read_b128 v[84:87], v142 offset:6144\n" \
  "v_mfma_f32_32x32x16_bf16 v[32:47], v[108:111], a[28:31], v[32:47]\n" \
  "ds_read_b128 v[88:91], v142 offset:7168\n" \
  "s_mov_b32 s42, 16384\n" \
  "v_mfma_f32_32x32x16_bf16 v[48:63], v[112:115], a[28:31], v[48:63]\n" \
  "ds_read_b128 v[92:95], v142 offset:8192\n" \
  "v_mfma_f32_32x32x16_bf16 v[64:79], v[116:119], a[28:31], v[64:79]\n" \
  "ds_read_b128 v[96:99], v142 offset:9216\n" \
  "buffer_load_dwordx4 a[76:79], v140, %[srdB], s42 offen offset:3072 sc1\n" \
  "s_waitcnt vmcnt(21) lgkmcnt(5)\n" \
  "v_mfma_f32_32x32x16_bf16 v[0:15], v[120:123], a[32:35], v[0:15]\n" \
  "ds_read_b128 v[100:103], v142 offset:10240\n" \
  "v_mfma_f32_32x32x16_bf16 v[16:31], v[124:127], a[32:35], v[16:31]\n" \
  "ds_read_b128 v[104:107], v142 offset:11264\n" \
  "v_mfma_f32_32x32x16_bf16 v[32:47], v[128:131], a[32:35], v[32:47]\n" \
  "ds_read_b128 v[108:111], v142 offset:12288\n" \
  "s_mov_b32 s42, 20480\n" \
  "v_mfma_f32_32x32x16_bf16 v[48:63], v[132:135], a[32:35], v[48:63]\n" \
  "ds_read_b128 v[112:115], v142 offset:13312\n" \
  "v_mfma_f32_32x32x16_bf16 v[64:79], v[136:139], a[32:35], v[64:79]\n" \
  "ds_read_b128 v[116:119], v142 offset:14336\n" \
  "buffer_load_dwordx4 a[80:83], v140, %[srdB], s42 offen offset:0 sc1\n" \
  "s_waitcnt vmcnt(21) lgkmcnt(5)\n" \
  "v_mfma_f32_32x32x16_bf16 v[0:15], v[80:83], a[36:39], v[0:15]\n" \
  "ds_read_b128 v[120:123], v142 offset:15360\n" \
  "v_mfma_f32_32x32x16_bf16 v[16:31], v[84:87], a[36:39], v[16:31]\n" \
  "ds_read_b128 v[124:127], v142 offset:16384\n" \
  "v_mfma_f32_32x32x16_bf16 v[32:47], v[88:91], a[36:39], v[32:47]\n" \
  "ds_read_b128 v[128:131], v142 offset:17408\n" \
  "s_mov_b32 s42, 20480\n" \
  "v_mfma_f32_32x32x16_bf16 v[48:63], v[92:95], a[36:39], v[48:63]\n" \
  "ds_read_b128 v[132:135], v142 offset:18432\n" \
  "v_mfma_f32_32x32x16_bf16 v[64:79], v[96:99], a[36:39], v[64:79]\n" \
  "ds_read_b128 v[136:139], v142 offset:19456\n" \
  "buffer_load_dwordx4 a[84:87], v140, %[srdB], s42 offen offset:1024 sc1\n" \
  "s_waitcnt vmcnt(21) lgkmcnt(5)\n" \
  "v_mfma_f32_32x32x16_bf16 v[0:15], v[100:103], a[40:43], v[0:15]\n" \
  "ds_read_b128 v[80:83], v142 offset:20480\n" \
  "v_mfma_f32_32x32x16_bf16 v[16:31], v[104:107], a[40:43], v[16:31]\n" \
  "ds_read_b128 v[84:87], v142 offset:21504\n" \
  "v_mfma_f32_32x32x16_bf16 v[32:47], v[108:111], a[40:43], v[32:47]\n" \
  "ds_read_b128 v[88:91], v142 offset:22528\n" \
  "s_mov_b32 s42, 20480\n" \
  "v_mfma_f32_32x32x16_bf16 v[48:63], v[112:115], a[40:43], v[48:63]\n" \
  "ds_read_b128 v[92:95], v142 offset:23552\n" \
  "v_mfma_f32_32x32x16_bf16 v[64:79], v[116:119], a[40:43], v[64:79]\n" \
  "ds_read_b128 v[96:99], v142 offset:24576\n" \
  "buffer_load_dwordx4 a[88:91], v140, %[srdB], s42 offen offset:2048 sc1\n" \
  "s_waitcnt vmcnt(21) lgkmcnt(5)\n" \
  "v_mfma_f32_32x32x16_bf16 v[0:15], v[120:123], a[44:47], v[0:15]\n" \
  "ds_read_b128 v[100:103], v142 offset:25600\n" \
  "v_mfma_f32_32x32x16_bf16 v[16:31], v[124:127], a[44:47], v[16:31]\n" \
  "ds_read_b128 v[104:107], v142 offset:26624\n" \
  "v_mfma_f32_32x32x16_bf16 v[32:47], v[128:131], a[44:47], v[32:47]\n" \
  "ds_read_b128 v[108:111], v142 offset:27648\n" \
  "s_mov_b32 s42, 20480\n" \
  "v_mfma_f32_32x32x16_bf16 v[48:63], v[132:135], a[44:47], v[48:63]\n" \
  "ds_read_b128 v[112:115], v142 offset:28672\n" \
  "v_mfma_f32_32x32x16_bf16 v[64:79], v[136:139], a[44:47], v[64:79]\n" \
  "ds_read_b128 v[116:119], v142 offset:29696\n" \
  "buffer_load_dwordx4 a[92:95], v140, %[srdB], s42 offen offset:3072 sc1\n" \
  "s_waitcnt vmcnt(21) lgkmcnt(5)\n" \
  "v_mfma_f32_32x32x16_bf16 v[0:15], v[80:83], a[48:51], v[0:15]\n" \
  "ds_read_b128 v[120:123], v142 offset:30720\n" \
  "v_mfma_f32_32x32x16_bf16 v[16:31], v[84:87], a[48:51], v[16:31]\n" \
  "ds_read_b128 v[124:127], v142 offset:31744\n" \
  "v_mfma_f32_32x32x16_bf16 v[32:47], v[88:91], a[48:51], v[32:47]\n" \
  "ds_read_b128 v[128:131], v142 offset:32768\n" \
  "s_mov_b32 s42, 24576\n" \
  "v_mfma_f32_32x32x16_bf16 v[48:63], v[92:95], a[48:51], v[48:63]\n" \
  "ds_read_b128 v[132:135], v142 offset:33792\n" \
  "v_mfma_f32_32x32x16_bf16 v[64:79], v[96:99], a[48:51], v[64:79]\n" \
  "ds_read_b128 v[136:139], v142 offset:34816\n" \
  "buffer_load_dwordx4 a[96:99], v140, %[srdB], s42 offen offset:0 sc1\n" \
  "s_waitcnt vmcnt(18) lgkmcnt(5)\n" \
  "v_mfma_f32_32x32x16_bf16 v[0:15], v[100:103], a[52:55], v[0:15]\n" \
  "ds_read_b128 v[80:83], v142 offset:35840\n" \
  "v_mfma_f32_32x32x16_bf16 v[16:31], v[104:107], a[52:55], v[16:31]\n" \
  "ds_read_b128 v[84:87], v142 offset:36864\n" \
  "v_mfma_f32_32x32x16_bf16 v[32:47], v[108:111], a[52:55], v[32:47]\n" \
  "ds_read_b128 v[88:91], v142 offset:37888\n" \
  "s_mov_b32 s42, 24576\n" \
  "v_mfma_f32_32x32x16_bf16 v[48:63], v[112:115], a[52:55], v[48:63]\n" \
  "ds_read_b128 v[92:95], v142 offset:38912\n" \
  "v_mfma_f32_32x32x16_bf16 v[64:79], v[116:119], a[52:55], v[64:79]\n" \
  "ds_read_b128 v[96:99], v142 offset:39936\n" \
  "buffer_load_dwordx4 a[100:103], v140, %[srdB], s42 offen offset:1024 sc1\n" \
  "s_waitcnt vmcnt(9)\n" \
  "s_barrier\n" \
  "s_waitcnt lgkmcnt(5)\n" \
  "v_mfma_f32_32x32x16_bf16 v[0:15], v[120:123], a[56:59], v[0:15]\n" \
  "ds_read_b128 v[100:103], v142 offset:40960\n" \
  "v_mfma_f32_32x32x16_bf16 v[16:31], v[124:127], a[56:59], v[16:31]\n" \
  "s_add_u32 m0, s60, 122880\n" \
  "ds_read_b128 v[104:107], v142 offset:41984\n" \
  "v_mfma_f32_32x32x16_bf16 v[32:47], v[128:131], a[56:59], v[32:47]\n" \
  "s_mov_b32 s40, 122880\n" \
  "ds_read_b128 v[108:111], v142 offset:43008\n" \
  "s_mov_b32 s42, 24576\n" \
  "v_mfma_f32_32x32x16_bf16 v[48:63], v[132:135], a[56:59], v[48:63]\n" \
  "buffer_load_dwordx4 v141, %[srdA], s40 offen offset:0 sc1 lds\n" \
  "ds_read_b128 v[112:115], v142 offset:44032\n" \
  "v_mfma_f32_32x32x16_bf16 v[64:79], v[136:139], a[56:59], v[64:79]\n" \
  "buffer_load_dwordx4 v141, %[srdA], s40 offen offset:1024 sc1 lds\n" \
  "ds_read_b128 v[116:119], v142 offset:45056\n" \
  "buffer_load_dwordx4 a[104:107], v140, %[srdB], s42 offen offset:2048 sc1\n" \
  "s_waitcnt lgkmcnt(5)\n" \
  "v_mfma_f32_32x32x16_bf16 v[0:15], v[80:83], a[60:63], v[0:15]\n" \
  "buffer_load_dwordx4 v141, %[srdA], s40 offen offset:2048 sc1 lds\n" \
  "ds_read_b128 v[120:123], v142 offset:46080\n" \
  "v_mfma_f32_32x32x16_bf16 v[16:31], v[84:87], a[60:63], v[16:31]\n" \
  "ds_read_b128 v[124:127], v142 offset:47104\n" \
  "v_mfma_f32_32x32x16_bf16 v[32:47], v[88:91], a[60:63], v[32:47]\n" \
  "buffer_load_dwordx4 v141, %[srdA], s40 offen offset:3072 sc1 lds\n" \
  "ds_read_b128 v[128:131], v142 offset:48128\n" \
  "s_mov_b32 s42, 24576\n" \
  "v_mfma_f32_32x32x16_bf16 v[48:63], v[92:95], a[60:63], v[48:63]\n" \
  "s_add_u32 m0, s60, 126976\n" \
  "ds_read_b128 v[132:135], v142 offset:49152\n" \
  "v_mfma_f32_32x32x16_bf16 v[64:79], v[96:99], a[60:63], v[64:79]\n" \
  "s_mov_b32 s40, 126976\n" \
  "ds_read_b128 v[136:139], v142 offset:50176\n" \
  "buffer_load_dwordx4 a[108:111], v140, %[srdB], s42 offen offset:3072 sc1\n" \
  "s_waitcnt lgkmcnt(5)\n" \
  "v_mfma_f32_32x32x16_bf16 v[0:15], v[100:103], a[64:67], v[0:15]\n" \
  "buffer_load_dwordx4 v141, %[srdA], s40 offen offset:0 sc1 lds\n" \
  "ds_read_b128 v[80:83], v142 offset:51200\n" \
  "v_mfma_f32_32x32x16_bf16 v[16:31], v[104:107], a[64:67], v[16:31]\n" \
  "buffer_load_dwordx4 v141, %[srdA], s40 offen offset:1024 sc1 lds\n" \
  "ds_read_b128 v[84:87], v142 offset:52224\n" \
  "v_mfma_f32_32x32x16_bf16 v[32:47], v[108:111], a[64:67], v[32:47]\n" \
  "ds_read_b128 v[88:91], v142 offset:53248\n" \
  "s_mov_b32 s42, 28672\n" \
  "v_mfma_f32_32x32x16_bf16 v[48:63], v[112:115], a[64:67], v[48:63]\n" \
  "buffer_load_dwordx4 v141, %[srdA], s40 offen offset:2048 sc1 lds\n" \
  "ds_read_b128 v[92:95], v142 offset:54272\n" \
  "v_mfma_f32_32x32x16_bf16 v[64:79], v[116:119], a[64:67], v[64:79]\n" \
  "buffer_load_dwordx4 v141, %[srdA], s40 offen offset:3072 sc1 lds\n" \
  "ds_read_b128 v[96:99], v142 offset:55296\n" \
  "buffer_load_dwordx4 a[112:115], v140, %[srdB], s42 offen offset:0 sc1\n" \
  "s_waitcnt vmcnt(19) lgkmcnt(5)\n" \
  "v_mfma_f32_32x32x16_bf16 v[0:15], v[120:123], a[68:71], v[0:15]\n" \
  "s_add_u32 m0, s60, 131072\n" \
  "ds_read_b128 v[100:103], v142 offset:56320\n" \
  "v_mfma_f32_32x32x16_bf16 v[16:31], v[124:127], a[68:71], v[16:31]\n" \
  "s_mov_b32 s40, 131072\n" \
  "ds_read_b128 v[104:107], v142 offset:57344\n" \
  "v_mfma_f32_32x32x16_bf16 v[32:47], v[128:131], a[68:71], v[32:47]\n" \
  "buffer_load_dwordx4 v141, %[srdA], s40 offen offset:0 sc1 lds\n" \
  "ds_read_b128 v[108:111], v142 offset:58368\n" \
  "s_mov_b32 s42, 28672\n" \
  "v_mfma_f32_32x32x16_bf16 v[48:63], v[132:135], a[68:71], v[48:63]\n" \
  "buffer_load_dwordx4 v141, %[srdA], s40 offen offset:1024 sc1 lds\n" \
  "ds_read_b128 v[112:115], v142 offset:59392\n" \
  "v_mfma_f32_32x32x16_bf16 v[64:79], v[136:139], a[68:71], v[64:79]\n" \
  "ds_read_b128 v[116:119], v142 offset:60416\n" \
  "buffer_load_dwordx4 a[116:119], v140, %[srdB], s42 offen offset:1024 sc1\n" \
  "s_waitcnt vmcnt(21) lgkmcnt(5)\n" \
  "v_mfma_f32_32x32x16_bf16 v[0:15], v[80:83], a[72:75], v[0:15]\n" \
  "s_add_u32 m0, s60, 81920\n" \
  "ds_read_b128 v[120:123], v142 offset:61440\n" \
  "v_mfma_f32_32x32x16_bf16 v[16:31], v[84:87], a[72:75], v[16:31]\n" \
  "s_mov_b32 s40, 163840\n" \
  "ds_read_b128 v[124:127], v142 offset:62464\n" \
  "v_mfma_f32_32x32x16_bf16 v[32:47], v[88:91], a[72:75], v[32:47]\n" \
  "buffer_load_dwordx4 v141, %[srdA], s40 offen offset:0 sc1 lds\n" \
  "ds_read_b128 v[128:131], v142 offset:63488\n" \
  "s_mov_b32 s42, 28672\n" \
  "v_mfma_f32_32x32x16_bf16 v[48:63], v[92:95], a[72:75], v[48:63]\n" \
  "buffer_load_dwordx4 v141, %[srdA], s40 offen offset:1024 sc1 lds\n" \
  "ds_read_b128 v[132:135], v142 offset:64512\n" \
  "v_mfma_f32_32x32x16_bf16 v[64:79], v[96:99], a[72:75], v[64:79]\n" \
  "buffer_load_dwordx4 v141, %[srdA], s40 offen offset:2048 sc1 lds\n" \
  "ds_read_b128 v[136:139], v143 offset:0\n" \
  "buffer_load_dwordx4 a[120:123], v140, %[srdB], s42 offen offset:2048 sc1\n" \
  "s_waitcnt vmcnt(24) lgkmcnt(5)\n" \
  "v_mfma_f32_32x32x16_bf16 v[0:15], v[100:103], a[76:79], v[0:15]\n" \
  "ds_read_b128 v[80:83], v143 offset:1024\n" \
  "v_mfma_f32_32x32x16_bf16 v[16:31], v[104:107], a[76:79], v[16:31]\n" \
  "buffer_load_dwordx4 v141, %[srdA], s40 offen offset:3072 sc1 lds\n" \
  "ds_read_b128 v[84:87], v143 offset:2048\n" \
  "v_mfma_f32_32x32x16_bf16 v[32:47], v[108:111], a[76:79], v[32:47]\n" \
  "s_add_u32 m0, s60, 86016\n" \
  "ds_read_b128 v[88:91], v143 offset:3072\n" \
  "s_mov_b32 s42, 28672\n" \
  "v_mfma_f32_32x32x16_bf16 v[48:63], v[112:115], a[76:79], v[48:63]\n" \
  "s_mov_b32 s40, 167936\n" \
  "ds_read_b128 v[92:95], v143 offset:4096\n" \
  "v_mfma_f32_32x32x16_bf16 v[64:79], v[116:119], a[76:79], v[64:79]\n" \
  "buffer_load_dwordx4 v141, %[srdA], s40 offen offset:0 sc1 lds\n" \
  "ds_read_b128 v[96:99], v143 offset:5120\n" \
  "buffer_load_dwordx4 a[124:127], v140, %[srdB], s42 offen offset:3072 sc1\n" \
  "s_waitcnt vmcnt(26) lgkmcnt(5)\n" \
  "v_mfma_f32_32x32x16_bf16 v[0:15], v[120:123], a[80:83], v[0:15]\n" \
  "buffer_load_dwordx4 v141, %[srdA], s40 offen offset:1024 sc1 lds\n" \
  "ds_read_b128 v[100:103], v143 offset:6144\n" \
  "s_add_u32 s41, %[gs0], 0\n" \
  "v_mfma_f32_32x32x16_bf16 v[16:31], v[124:127], a[80:83], v[16:31]\n" \
  "ds_read_b128 v[104:107], v143 offset:7168\n" \
  "buffer_load_dword v160, v149, %[srdW2s], s41 offen offset:0\n" \
  "s_add_u32 s41, %[gs0], 0\n" \
  "v_mfma_f32_32x32x16_bf16 v[32:47], v[128:131], a[80:83], v[32:47]\n" \
  "buffer_load_dwordx4 v141, %[srdA], s40 offen offset:2048 sc1 lds\n" \
  "ds_read_b128 v[108:111], v143 offset:8192\n" \
  "s_mov_b32 s42, 32768\n" \
  "buffer_load_dword v161, v149, %[srdW2s], s41 offen offset:1280\n" \
  "v_mfma_f32_32x32x16_bf16 v[48:63], v[132:135], a[80:83], v[48:63]\n" \
  "buffer_load_dwordx4 v141, %[srdA], s40 offen offset:3072 sc1 lds\n" \
  "ds_read_b128 v[112:115], v143 offset:9216\n" \
  "s_add_u32 s41, %[gs0], 0\n" \
  "buffer_load_dword v162, v149, %[srdW2s], s41 offen offset:2560\n" \
  "v_mfma_f32_32x32x16_bf16 v[64:79], v[136:139], a[80:83], v[64:79]\n" \
  "s_add_u32 m0, s60, 90112\n" \
  "ds_read_b128 v[116:119], v143 offset:10240\n" \
  "buffer_load_dwordx4 a[128:131], v140, %[srdB], s42 offen offset:0 sc1\n" \
  "s_add_u32 s41, %[gs0], 0\n" \
  "buffer_load_dword v163, v149, %[srdW2s], s41 offen offset:3840\n" \
  "s_waitcnt vmcnt(33) lgkmcnt(5)\n" \
  "v_mfma_f32_32x32x16_bf16 v[0:15], v[80:83], a[84:87], v[0:15]\n" \
  "s_mov_b32 s40, 172032\n" \
  "s_add_u32 s41, %[gs0], 20480\n" \
  "ds_read_b128 v[120:123], v143 offset:11264\n" \
  "v_mfma_f32_32x32x16_bf16 v[16:31], v[84:87], a[84:87], v[16:31]\n" \
  "buffer_load_dwordx4 v141, %[srdA], s40 offen offset:0 sc1 lds\n" \
  "buffer_load_dword v164, v149, %[srdW2s], s41 offen offset:0\n" \
  "s_add_u32 s41, %[gs0], 20480\n" \
  "ds_read_b128 v[124:127], v143 offset:12288\n" \
  "v_mfma_f32_32x32x16_bf16 v[32:47], v[88:91], a[84:87], v[32:47]\n" \
  "buffer_load_dwordx4 v141, %[srdA], s40 offen offset:1024 sc1 lds\n" \
  "buffer_load_dword v165, v149, %[srdW2s], s41 offen offset:1280\n" \
  "ds_read_b128 v[128:131], v143 offset:13312\n" \
  "s_mov_b32 s42, 32768\n" \
  "v_mfma_f32_32x32x16_bf16 v[48:63], v[92:95], a[84:87], v[48:63]\n" \
  "s_add_u32 s41, %[gs0], 20480\n" \
  "buffer_load_dword v166, v149, %[srdW2s], s41 offen offset:2560\n" \
  "ds_read_b128 v[132:135], v143 offset:14336\n" \
  "v_mfma_f32_32x32x16_bf16 v[64:79], v[96:99], a[84:87], v[64:79]\n" \
  "s_add_u32 s41, %[gs0], 20480\n" \
  "buffer_load_dword v167, v149, %[srdW2s], s41 offen offset:3840\n" \
  "ds_read_b128 v[136:139], v143 offset:15360\n" \
  "buffer_load_dwordx4 a[132:135], v140, %[srdB], s42 offen offset:1024 sc1\n" \
  "s_waitcnt vmcnt(23)\n" \
  "s_barrier\n" \
  "s_waitcnt lgkmcnt(5)\n" \
  "v_mfma_f32_32x32x16_bf16 v[0:15], v[100:103], a[88:91], v[0:15]\n" \
  "ds_read_b128 v[80:83], v143 offset:57344\n" \
  "v_mfma_f32_32x32x16_bf16 v[16:31], v[104:107], a[88:91], v[16:31]\n" \
  "ds_read_b128 v[84:87], v143 offset:58368\n" \
  "v_mfma_f32_32x32x16_bf16 v[32:47], v[108:111], a[88:91], v[32:47]\n" \
  "s_add_u32 m0, s60, 0\n" \
  "ds_read_b128 v[88:91], v143 offset:59392\n" \
  "s_mov_b32 s42, 32768\n" \
  "v_mfma_f32_32x32x16_bf16 v[48:63], v[112:115], a[88:91], v[48:63]\n" \
  "ds_read_b128 v[92:95], v143 offset:60416\n" \
  "v_mfma_f32_32x32x16_bf16 v[64:79], v[116:119], a[88:91], v[64:79]\n" \
  "s_mov_b32 s40, 204800\n" \
  "ds_read_b128 v[96:99], v143 offset:61440\n" \
  "buffer_load_dwordx4 a[136:139], v140, %[srdB], s42 offen offset:2048 sc1\n" \
  "s_waitcnt lgkmcnt(5)\n" \
  "v_mfma_f32_32x32x16_bf16 v[0:15], v[120:123], a[92:95], v[0:15]\n" \
  "ds_read_b128 v[100:103], v143 offset:62464\n" \
  "v_mfma_f32_32x32x16_bf16 v[16:31], v[124:127], a[92:95], v[16:31]\n" \
  "ds_read_b128 v[104:107], v143 offset:63488\n" \
  "v_mfma_f32_32x32x16_bf16 v[32:47], v[128:131], a[92:95], v[32:47]\n" \
  "buffer_load_dwordx4 v141, %[srdA], s40 offen offset:0 sc1 lds\n" \
  "ds_read_b128 v[108:111], v143 offset:64512\n" \
  "s_mov_b32 s42, 32768\n" \
  "v_mfma_f32_32x32x16_bf16 v[48:63], v[132:135], a[92:95], v[48:63]\n" \
  "ds_read_b128 v[112:115], v144 offset:0\n" \
  "v_mfma_f32_32x32x16_bf16 v[64:79], v[136:139], a[92:95], v[64:79]\n" \
  "buffer_load_dwordx4 v141, %[srdA], s40 offen offset:1024 sc1 lds\n" \
  "ds_read_b128 v[116:119], v144 offset:1024\n" \
  "buffer_load_dwordx4 a[140:143], v140, %[srdB], s42 offen offset:3072 sc1\n" \
  "s_waitcnt lgkmcnt(5)\n" \
  "v_mfma_f32_32x32x16_bf16 v[0:15], v[80:83], a[96:99], v[0:15]\n" \
  "ds_read_b128 v[120:123], v144 offset:2048\n" \
  "v_mfma_f32_32x32x16_bf16 v[16:31], v[84:87], a[96:99], v[16:31]\n" \
  "buffer_load_dwordx4 v141, %[srdA], s40 offen offset:2048 sc1 lds\n" \
  "ds_read_b128 v[124:127], v144 offset:3072\n" \
  "v_mfma_f32_32x32x16_bf16 v[32:47], v[88:91], a[96:99], v[32:47]\n" \
  "ds_read_b128 v[128:131], v144 offset:4096\n" \
  "s_mov_b32 s42, 36864\n" \
  "v_mfma_f32_32x32x16_bf16 v[48:63], v[92:95], a[96:99], v[48:63]\n" \
  "ds_read_b128 v[132:135], v144 offset:5120\n" \
  "v_mfma_f32_32x32x16_bf16 v[64:79], v[96:99], a[96:99], v[64:79]\n" \
  "buffer_load_dwordx4 v141, %[srdA], s40 offen offset:3072 sc1 lds\n" \
  "ds_read_b128 v[136:139], v144 offset:6144\n" \
  "buffer_load_dwordx4 a[144:147], v140, %[srdB], s42 offen offset:0 sc1\n" \
  "s_waitcnt lgkmcnt(5)\n" \
  "v_mfma_f32_32x32x16_bf16 v[0:15], v[100:103], a[100:103], v[0:15]\n" \
  "ds_read_b128 v[80:83], v144 offset:7168\n" \
  "v_mfma_f32_32x32x16_bf16 v[16:31], v[104:107], a[100:103], v[16:31]\n" \
  "s_add_u32 m0, s60, 4096\n" \
  "ds_read_b128 v[84:87], v144 offset:8192\n" \
  "v_mfma_f32_32x32x16_bf16 v[32:47], v[108:111], a[100:103], v[32:47]\n" \
  "ds_read_b128 v[88:91], v144 offset:9216\n" \
  "s_mov_b32 s42, 36864\n" \
  "v_mfma_f32_32x32x16_bf16 v[48:63], v[112:115], a[100:103], v[48:63]\n" \
  "s_mov_b32 s40, 208896\n" \
  "ds_read_b128 v[92:95], v144 offset:10240\n" \
  "v_mfma_f32_32x32x16_bf16 v[64:79], v[116:119], a[100:103], v[64:79]\n" \
  "ds_read_b128 v[96:99], v144 offset:11264\n" \
  "buffer_load_dwordx4 a[148:151], v140, %[srdB], s42 offen offset:1024 sc1\n" \
  "s_waitcnt lgkmcnt(5)\n" \
  "v_mfma_f32_32x32x16_bf16 v[0:15], v[120:123], a[104:107], v[0:15]\n" \
  "ds_read_b128 v[100:103], v144 offset:12288\n" \
  "v_mfma_f32_32x32x16_bf16 v[16:31], v[124:127], a[104:107], v[16:31]\n" \
  "buffer_load_dwordx4 v141, %[srdA], s40 offen offset:0 sc1 lds\n" \
  "ds_read_b128 v[104:107], v144 offset:13312\n" \
  "v_mfma_f32_32x32x16_bf16 v[32:47], v[128:131], a[104:107], v[32:47]\n" \
  "ds_read_b128 v[108:111], v144 offset:14336\n" \
  "s_mov_b32 s42, 36864\n" \
  "v_mfma_f32_32x32x16_bf16 v[48:63], v[132:135], a[104:107], v[48:63]\n" \
  "buffer_load_dwordx4 v141, %[srdA], s40 offen offset:1024 sc1 lds\n" \
  "ds_read_b128 v[112:115], v144 offset:15360\n" \
  "v_mfma_f32_32x32x16_bf16 v[64:79], v[136:139], a[104:107], v[64:79]\n" \
  "ds_read_b128 v[116:119], v144 offset:16384\n" \
  "buffer_load_dwordx4 a[152:155], v140, %[srdB], s42 offen offset:2048 sc1\n" \
  "s_waitcnt lgkmcnt(5)\n" \
  "v_mfma_f32_32x32x16_bf16 v[0:15], v[80:83], a[108:111], v[0:15]\n" \
  "ds_read_b128 v[120:123], v144 offset:17408\n" \
  "v_mfma_f32_32x32x16_bf16 v[16:31], v[84:87], a[108:111], v[16:31]\n" \
  "buffer_load_dwordx4 v141, %[srdA], s40 offen offset:2048 sc1 lds\n" \
  "ds_read_b128 v[124:127], v144 offset:18432\n" \
  "v_mfma_f32_32x32x16_bf16 v[32:47], v[88:91], a[108:111], v[32:47]\n" \
  "ds_read_b128 v[128:131], v144 offset:19456\n" \
  "s_mov_b32 s42, 36864\n" \
  "v_mfma_f32_32x32x16_bf16 v[48:63], v[92:95], a[108:111], v[48:63]\n" \
  "buffer_load_dwordx4 v141, %[srdA], s40 offen offset:3072 sc1 lds\n" \
  "ds_read_b128 v[132:135], v144 offset:20480\n" \
  "v_mfma_f32_32x32x16_bf16 v[64:79], v[96:99], a[108:111], v[64:79]\n" \
  "ds_read_b128 v[136:139], v144 offset:21504\n" \
  "buffer_load_dwordx4 a[156:159], v140, %[srdB], s42 offen offset:3072 sc1\n" \
  "s_waitcnt lgkmcnt(5)\n" \
  "v_mfma_f32_32x32x16_bf16 v[0:15], v[100:103], a[112:115], v[0:15]\n" \
  "s_add_u32 m0, s60, 8192\n" \
  "ds_read_b128 v[80:83], v144 offset:22528\n" \
  "s_add_u32 s41, %[gs1], 0\n" \
  "v_mfma_f32_32x32x16_bf16 v[16:31], v[104:107], a[112:115], v[16:31]\n" \
  "ds_read_b128 v[84:87], v144 offset:23552\n" \
  "buffer_load_dword v176, v149, %[srdW2s], s41 offen offset:0\n" \
  "s_add_u32 s41, %[gs1], 0\n" \
  "v_mfma_f32_32x32x16_bf16 v[32:47], v[108:111], a[112:115], v[32:47]\n" \
  "ds_read_b128 v[88:91], v144 offset:24576\n" \
  "s_mov_b32 s42, 40960\n" \
  "buffer_load_dword v177, v149, %[srdW2s], s41 offen offset:1280\n" \
  "v_mfma_f32_32x32x16_bf16 v[48:63], v[112:115], a[112:115], v[48:63]\n" \
  "s_mov_b32 s40, 212992\n" \
  "ds_read_b128 v[92:95], v144 offset:25600\n" \
  "s_add_u32 s41, %[gs1], 0\n" \
  "buffer_load_dword v178, v149, %[srdW2s], s41 offen offset:2560\n" \
  "v_mfma_f32_32x32x16_bf16 v[64:79], v[116:119], a[112:115], v[64:79]\n" \
  "ds_read_b128 v[96:99], v144 offset:26624\n" \
  "buffer_load_dwordx4 a[160:163], v140, %[srdB], s42 offen offset:0 sc1\n" \
  "s_add_u32 s41, %[gs1], 0\n" \
  "buffer_load_dword v179, v149, %[srdW2s], s41 offen offset:3840\n" \
  "s_waitcnt vmcnt(41) lgkmcnt(5)\n" \
  "v_mfma_f32_32x32x16_bf16 v[0:15], v[120:123], a[116:119], v[0:15]\n" \
  "buffer_load_dwordx4 v141, %[srdA], s40 offen offset:0 sc1 lds\n" \
  "s_add_u32 s41, %[gs1], 20480\n" \
  "ds_read_b128 v[100:103], v144 offset:27648\n" \
  "v_mfma_f32_32x32x16_bf16 v[16:31], v[124:127], a[116:119], v[16:31]\n" \
  "buffer_load_dword v180, v149, %[srdW2s], s41 offen offset:0\n" \
  "s_add_u32 s41, %[gs1], 20480\n" \
  "ds_read_b128 v[104:107], v144 offset:28672\n" \
  "v_mfma_f32_32x32x16_bf16 v[32:47], v[128:131], a[116:119], v[32:47]\n" \
  "buffer_load_dwordx4 v141, %[srdA], s40 offen offset:1024 sc1 lds\n" \
  "buffer_load_dword v181, v149, %[srdW2s], s41 offen offset:1280\n" \
  "ds_read_b128 v[108:111], v144 offset:29696\n" \
  "s_mov_b32 s42, 40960\n" \
  "v_mfma_f32_32x32x16_bf16 v[48:63], v[132:135], a[116:119], v[48:63]\n" \
  "s_add_u32 s41, %[gs1], 20480\n" \
  "buffer_load_dword v182, v149, %[srdW2s], s41 offen offset:2560\n" \
  "ds_read_b128 v[112:115], v144 offset:30720\n" \
  "v_mfma_f32_32x32x16_bf16 v[64:79], v[136:139], a[116:119], v[64:79]\n" \
  "s_add_u32 s41, %[gs1], 20480\n" \
  "buffer_load_dword v183, v149, %[srdW2s], s41 offen offset:3840\n" \
  "ds_read_b128 v[116:119], v144 offset:31744\n" \
  "buffer_load_dwordx4 a[164:167], v140, %[srdB], s42 offen offset:1024 sc1\n" \
  "s_waitcnt vmcnt(30)\n" \
  "s_barrier\n" \
  "s_waitcnt lgkmcnt(5)\n" \
  "v_mfma_f32_32x32x16_bf16 v[0:15], v[80:83], a[120:123], v[0:15]\n" \
  "ds_read_b128 v[120:123], v143 offset:16384\n" \
  "v_mfma_f32_32x32x16_bf16 v[16:31], v[84:87], a[120:123], v[16:31]\n" \
  "ds_read_b128 v[124:127], v143 offset:17408\n" \
  "v_mfma_f32_32x32x16_bf16 v[32:47], v[88:91], a[120:123], v[32:47]\n" \
  "s_add_u32 m0, s60, 40960\n" \
  "ds_read_b128 v[128:131], v143 offset:18432\n" \
  "s_mov_b32 s42, 40960\n" \
  "v_mfma_f32_32x32x16_bf16 v[48:63], v[92:95], a[120:123], v[48:63]\n" \
  "ds_read_b128 v[132:135], v143 offset:19456\n" \
  "v_mfma_f32_32x32x16_bf16 v[64:79], v[96:99], a[120:123], v[64:79]\n" \
  "s_mov_b32 s40, 245760\n" \
  "ds_read_b128 v[136:139], v143 offset:20480\n" \
  "buffer_load_dwordx4 a[168:171], v140, %[srdB], s42 offen offset:2048 sc1\n" \
  "s_waitcnt lgkmcnt(5)\n" \
  "v_mfma_f32_32x32x16_bf16 v[0:15], v[100:103], a[124:127], v[0:15]\n" \
  "ds_read_b128 v[80:83], v143 offset:21504\n" \
  "v_mfma_f32_32x32x16_bf16 v[16:31], v[104:107], a[124:127], v[16:31]\n" \
  "ds_read_b128 v[84:87], v143 offset:22528\n" \
  "v_mfma_f32_32x32x16_bf16 v[32:47], v[108:111], a[124:127], v[32:47]\n" \
  "buffer_load_dwordx4 v141, %[srdA], s40 offen offset:0 sc1 lds\n" \
  "ds_read_b128 v[88:91], v143 offset:23552\n" \
  "s_mov_b32 s42, 40960\n" \
  "v_mfma_f32_32x32x16_bf16 v[48:63], v[112:115], a[124:127], v[48:63]\n" \
  "ds_read_b128 v[92:95], v143 offset:24576\n" \
  "v_mfma_f32_32x32x16_bf16 v[64:79], v[116:119], a[124:127], v[64:79]\n" \
  "buffer_load_dwordx4 v141, %[srdA], s40 offen offset:1024 sc1 lds\n" \
  "ds_read_b128 v[96:99], v143 offset:25600\n" \
  "buffer_load_dwordx4 a[172:175], v140, %[srdB], s42 offen offset:3072 sc1\n" \
  "s_waitcnt lgkmcnt(5)\n" \
  "v_mfma_f32_32x32x16_bf16 v[0:15], v[120:123], a[128:131], v[0:15]\n" \
  "ds_read_b128 v[100:103], v143 offset:26624\n" \
  "v_mfma_f32_32x32x16_bf16 v[16:31], v[124:127], a[128:131], v[16:31]\n" \
  "buffer_load_dwordx4 v141, %[srdA], s40 offen offset:2048 sc1 lds\n" \
  "ds_read_b128 v[104:107], v143 offset:27648\n" \
  "v_mfma_f32_32x32x16_bf16 v[32:47], v[128:131], a[128:131], v[32:47]\n" \
  "ds_read_b128 v[108:111], v143 offset:28672\n" \
  "s_mov_b32 s42, 45056\n" \
  "v_mfma_f32_32x32x16_bf16 v[48:63], v[132:135], a[128:131], v[48:63]\n" \
  "ds_read_b128 v[112:115], v143 offset:29696\n" \
  "v_mfma_f32_32x32x16_bf16 v[64:79], v[136:139], a[128:131], v[64:79]\n" \
  "buffer_load_dwordx4 v141, %[srdA], s40 offen offset:3072 sc1 lds\n" \
  "ds_read_b128 v[116:119], v143 offset:30720\n" \
  "buffer_load_dwordx4 a[176:179], v140, %[srdB], s42 offen offset:0 sc1\n" \
  "s_waitcnt vmcnt(33) lgkmcnt(5)\n" \
  "v_mfma_f32_32x32x16_bf16 v[0:15], v[80:83], a[132:135], v[0:15]\n" \
  "ds_read_b128 v[120:123], v143 offset:31744\n" \
  "v_mfma_f32_32x32x16_bf16 v[16:31], v[84:87], a[132:135], v[16:31]\n" \
  "s_add_u32 m0, s60, 45056\n" \
  "ds_read_b128 v[124:127], v143 offset:32768\n" \
  "v_mfma_f32_32x32x16_bf16 v[32:47], v[88:91], a[132:135], v[32:47]\n" \
  "ds_read_b128 v[128:131], v143 offset:33792\n" \
  "s_mov_b32 s42, 45056\n" \
  "v_mfma_f32_32x32x16_bf16 v[48:63], v[92:95], a[132:135], v[48:63]\n" \
  "s_mov_b32 s40, 249856\n" \
  "ds_read_b128 v[132:135], v143 offset:34816\n" \
  "v_mfma_f32_32x32x16_bf16 v[64:79], v[96:99], a[132:135], v[64:79]\n" \
  "ds_read_b128 v[136:139], v143 offset:35840\n" \
  "buffer_load_dwordx4 a[180:183], v140, %[srdB], s42 offen offset:1024 sc1\n" \
  "s_waitcnt vmcnt(33) lgkmcnt(5)\n" \
  "v_mfma_f32_32x32x16_bf16 v[0:15], v[100:103], a[136:139], v[0:15]\n" \
  "ds_read_b128 v[80:83], v143 offset:36864\n" \
  "v_mfma_f32_32x32x16_bf16 v[16:31], v[104:107], a[136:139], v[16:31]\n" \
  "buffer_load_dwordx4 v141, %[srdA], s40 offen offset:0 sc1 lds\n" \
  "ds_read_b128 v[84:87], v143 offset:37888\n" \
  "v_mfma_f32_32x32x16_bf16 v[32:47], v[108:111], a[136:139], v[32:47]\n" \
  "ds_read_b128 v[88:91], v143 offset:38912\n" \
  "s_mov_b32 s42, 45056\n" \
  "v_mfma_f32_32x32x16_bf16 v[48:63], v[112:115], a[136:139], v[48:63]\n" \
  "buffer_load_dwordx4 v141, %[srdA], s40 offen offset:1024 sc1 lds\n" \
  "ds_read_b128 v[92:95], v143 offset:39936\n" \
  "v_mfma_f32_32x32x16_bf16 v[64:79], v[116:119], a[136:139], v[64:79]\n" \
  "ds_read_b128 v[96:99], v143 offset:40960\n" \
  "buffer_load_dwordx4 a[184:187], v140, %[srdB], s42 offen offset:2048 sc1\n" \
  "s_waitcnt vmcnt(33) lgkmcnt(5)\n" \
  "v_mfma_f32_32x32x16_bf16 v[0:15], v[120:123], a[140:143], v[0:15]\n" \
  "ds_read_b128 v[100:103], v143 offset:41984\n" \
  "v_mfma_f32_32x32x16_bf16 v[16:31], v[124:127], a[140:143], v[16:31]\n" \
  "buffer_load_dwordx4 v141, %[srdA], s40 offen offset:2048 sc1 lds\n" \
  "ds_read_b128 v[104:107], v143 offset:43008\n" \
  "v_mfma_f32_32x32x16_bf16 v[32:47], v[128:131], a[140:143], v[32:47]\n" \
  "ds_read_b128 v[108:111], v143 offset:44032\n" \
  "s_mov_b32 s42, 45056\n" \
  "v_mfma_f32_32x32x16_bf16 v[48:63], v[132:135], a[140:143], v[48:63]\n" \
  "buffer_load_dwordx4 v141, %[srdA], s40 offen offset:3072 sc1 lds\n" \
  "ds_read_b128 v[112:115], v143 offset:45056\n" \
  "v_mfma_f32_32x32x16_bf16 v[64:79], v[136:139], a[140:143], v[64:79]\n" \
  "ds_read_b128 v[116:119], v143 offset:46080\n" \
  "buffer_load_dwordx4 a[188:191], v140, %[srdB], s42 offen offset:3072 sc1\n" \
  "s_waitcnt vmcnt(33) lgkmcnt(5)\n" \
  "v_mfma_f32_32x32x16_bf16 v[0:15], v[80:83], a[144:147], v[0:15]\n" \
  "s_add_u32 m0, s60, 49152\n" \
  "ds_read_b128 v[120:123], v143 offset:47104\n" \
  "v_mfma_f32_32x32x16_bf16 v[16:31], v[84:87], a[144:147], v[16:31]\n" \
  "ds_read_b128 v[124:127], v143 offset:48128\n" \
  "v_mfma_f32_32x32x16_bf16 v[32:47], v[88:91], a[144:147], v[32:47]\n" \
  "ds_read_b128 v[128:131], v143 offset:49152\n" \
  "s_mov_b32 s42, 49152\n" \
  "v_mfma_f32_32x32x16_bf16 v[48:63], v[92:95], a[144:147], v[48:63]\n" \
  "s_mov_b32 s40, 253952\n" \
  "ds_read_b128 v[132:135], v143 offset:50176\n" \
  "v_mfma_f32_32x32x16_bf16 v[64:79], v[96:99], a[144:147], v[64:79]\n" \
  "ds_read_b128 v[136:139], v143 offset:51200\n" \
  "buffer_load_dwordx4 a[192:195], v140, %[srdB], s42 offen offset:0 sc1\n" \
  "s_waitcnt vmcnt(33) lgkmcnt(5)\n" \
  "v_mfma_f32_32x32x16_bf16 v[0:15], v[100:103], a[148:151], v[0:15]\n" \
  "buffer_load_dwordx4 v141, %[srdA], s40 offen offset:0 sc1 lds\n" \
  "ds_read_b128 v[80:83], v143 offset:52224\n" \
  "v_mfma_f32_32x32x16_bf16 v[16:31], v[104:107], a[148:151], v[16:31]\n" \
  "ds_read_b128 v[84:87], v143 offset:53248\n" \
  "v_mfma_f32_32x32x16_bf16 v[32:47], v[108:111], a[148:151], v[32:47]\n" \
  "buffer_load_dwordx4 v141, %[srdA], s40 offen offset:1024 sc1 lds\n" \
  "ds_read_b128 v[88:91], v143 offset:54272\n" \
  "s_mov_b32 s42, 49152\n" \
  "v_mfma_f32_32x32x16_bf16 v[48:63], v[112:115], a[148:151], v[48:63]\n" \
  "ds_read_b128 v[92:95], v143 offset:55296\n" \
  "v_mfma_f32_32x32x16_bf16 v[64:79], v[116:119], a[148:151], v[64:79]\n" \
  "ds_read_b128 v[96:99], v143 offset:56320\n" \
  "buffer_load_dwordx4 a[196:199], v140, %[srdB], s42 offen offset:1024 sc1\n" \
  "s_waitcnt vmcnt(22)\n" \
  "s_barrier\n" \
  "s_waitcnt lgkmcnt(5)\n" \
  "v_mfma_f32_32x32x16_bf16 v[0:15], v[120:123], a[152:155], v[0:15]\n" \
  "ds_read_b128 v[100:103], v142 offset:0\n" \
  "v_mfma_f32_32x32x16_bf16 v[16:31], v[124:127], a[152:155], v[16:31]\n" \
  "ds_read_b128 v[104:107], v142 offset:1024\n" \
  "v_mfma_f32_32x32x16_bf16 v[32:47], v[128:131], a[152:155], v[32:47]\n" \
  "s_add_u32 m0, s60, 122880\n" \
  "ds_read_b128 v[108:111], v142 offset:2048\n" \
  "s_mov_b32 s42, 49152\n" \
  "v_mfma_f32_32x32x16_bf16 v[48:63], v[132:135], a[152:155], v[48:63]\n" \
  "ds_read_b128 v[112:115], v142 offset:3072\n" \
  "v_mfma_f32_32x32x16_bf16 v[64:79], v[136:139], a[152:155], v[64:79]\n" \
  "s_mov_b32 s40, 286720\n" \
  "ds_read_b128 v[116:119], v142 offset:4096\n" \
  "buffer_load_dwordx4 a[200:203], v140, %[srdB], s42 offen offset:2048 sc1\n" \
  "s_waitcnt lgkmcnt(5)\n" \
  "v_mfma_f32_32x32x16_bf16 v[0:15], v[80:83], a[156:159], v[0:15]\n" \
  "ds_read_b128 v[120:123], v142 offset:5120\n" \
  "v_mfma_f32_32x32x16_bf16 v[16:31], v[84:87], a[156:159], v[16:31]\n" \
  "ds_read_b128 v[124:127], v142 offset:6144\n" \
  "v_mfma_f32_32x32x16_bf16 v[32:47], v[88:91], a[156:159], v[32:47]\n" \
  "buffer_load_dwordx4 v141, %[srdA], s40 offen offset:0 sc1 lds\n" \
  "ds_read_b128 v[128:131], v142 offset:7168\n" \
  "s_mov_b32 s42, 49152\n" \
  "v_mfma_f32_32x32x16_bf16 v[48:63], v[92:95], a[156:159], v[48:63]\n" \
  "ds_read_b128 v[132:135], v142 offset:8192\n" \
  "v_mfma_f32_32x32x16_bf16 v[64:79], v[96:99], a[156:159], v[64:79]\n" \
  "buffer_load_dwordx4 v141, %[srdA], s40 offen offset:1024 sc1 lds\n" \
  "ds_read_b128 v[136:139], v142 offset:9216\n" \
  "buffer_load_dwordx4 a[204:207], v140, %[srdB], s42 offen offset:3072 sc1\n" \
  "s_waitcnt lgkmcnt(5)\n" \
  "v_mfma_f32_32x32x16_bf16 v[0:15], v[100:103], a[160:163], v[0:15]\n" \
  "ds_read_b128 v[80:83], v142 offset:10240\n" \
  "v_mfma_f32_32x32x16_bf16 v[16:31], v[104:107], a[160:163], v[16:31]\n" \
  "buffer_load_dwordx4 v141, %[srdA], s40 offen offset:2048 sc1 lds\n" \
  "ds_read_b128 v[84:87], v142 offset:11264\n" \
  "v_mfma_f32_32x32x16_bf16 v[32:47], v[108:111], a[160:163], v[32:47]\n" \
  "ds_read_b128 v[88:91], v142 offset:12288\n" \
  "s_mov_b32 s42, 53248\n" \
  "v_mfma_f32_32x32x16_bf16 v[48:63], v[112:115], a[160:163], v[48:63]\n" \
  "ds_read_b128 v[92:95], v142 offset:13312\n" \
  "v_mfma_f32_32x32x16_bf16 v[64:79], v[116:119], a[160:163], v[64:79]\n" \
  "buffer_load_dwordx4 v141, %[srdA], s40 offen offset:3072 sc1 lds\n" \
  "ds_read_b128 v[96:99], v142 offset:14336\n" \
  "buffer_load_dwordx4 a[208:211], v140, %[srdB], s42 offen offset:0 sc1\n" \
  "s_waitcnt vmcnt(25) lgkmcnt(5)\n" \
  "v_mfma_f32_32x32x16_bf16 v[0:15], v[120:123], a[164:167], v[0:15]\n" \
  "ds_read_b128 v[100:103], v142 offset:15360\n" \
  "v_mfma_f32_32x32x16_bf16 v[16:31], v[124:127], a[164:167], v[16:31]\n" \
  "s_add_u32 m0, s60, 126976\n" \
  "ds_read_b128 v[104:107], v142 offset:16384\n" \
  "v_mfma_f32_32x32x16_bf16 v[32:47], v[128:131], a[164:167], v[32:47]\n" \
  "ds_read_b128 v[108:111], v142 offset:17408\n" \
  "s_mov_b32 s42, 53248\n" \
  "v_mfma_f32_32x32x16_bf16 v[48:63], v[132:135], a[164:167], v[48:63]\n" \
  "s_mov_b32 s40, 290816\n" \
  "ds_read_b128 v[112:115], v142 offset:18432\n" \
  "v_mfma_f32_32x32x16_bf16 v[64:79], v[136:139], a[164:167], v[64:79]\n" \
  "ds_read_b128 v[116:119], v142 offset:19456\n" \
  "buffer_load_dwordx4 a[212:215], v140, %[srdB], s42 offen offset:1024 sc1\n" \
  "s_waitcnt vmcnt(25) lgkmcnt(5)\n" \
  "v_mfma_f32_32x32x16_bf16 v[0:15], v[80:83], a[168:171], v[0:15]\n" \
  "ds_read_b128 v[120:123], v142 offset:20480\n" \
  "v_mfma_f32_32x32x16_bf16 v[16:31], v[84:87], a[168:171], v[16:31]\n" \
  "buffer_load_dwordx4 v141, %[srdA], s40 offen offset:0 sc1 lds\n" \
  "ds_read_b128 v[124:127], v142 offset:21504\n" \
  "v_mfma_f32_32x32x16_bf16 v[32:47], v[88:91], a[168:171], v[32:47]\n" \
  "ds_read_b128 v[128:131], v142 offset:22528\n" \
  "s_mov_b32 s42, 53248\n" \
  "v_mfma_f32_32x32x16_bf16 v[48:63], v[92:95], a[168:171], v[48:63]\n" \
  "buffer_load_dwordx4 v141, %[srdA], s40 offen offset:1024 sc1 lds\n" \
  "ds_read_b128 v[132:135], v142 offset:23552\n" \
  "v_mfma_f32_32x32x16_bf16 v[64:79], v[96:99], a[168:171], v[64:79]\n" \
  "ds_read_b128 v[136:139], v142 offset:24576\n" \
  "buffer_load_dwordx4 a[216:219], v140, %[srdB], s42 offen offset:2048 sc1\n" \
  "s_waitcnt vmcnt(25) lgkmcnt(5)\n" \
  "v_mfma_f32_32x32x16_bf16 v[0:15], v[100:103], a[172:175], v[0:15]\n" \
  "ds_read_b128 v[80:83], v142 offset:25600\n" \
  "v_mfma_f32_32x32x16_bf16 v[16:31], v[104:107], a[172:175], v[16:31]\n" \
  "buffer_load_dwordx4 v141, %[srdA], s40 offen offset:2048 sc1 lds\n" \
  "ds_read_b128 v[84:87], v142 offset:26624\n" \
  "v_mfma_f32_32x32x16_bf16 v[32:47], v[108:111], a[172:175], v[32:47]\n" \
  "ds_read_b128 v[88:91], v142 offset:27648\n" \
  "s_mov_b32 s42, 53248\n" \
  "v_mfma_f32_32x32x16_bf16 v[48:63], v[112:115], a[172:175], v[48:63]\n" \
  "buffer_load_dwordx4 v141, %[srdA], s40 offen offset:3072 sc1 lds\n" \
  "ds_read_b128 v[92:95], v142 offset:28672\n" \
  "v_mfma_f32_32x32x16_bf16 v[64:79], v[116:119], a[172:175], v[64:79]\n" \
  "ds_read_b128 v[96:99], v142 offset:29696\n" \
  "buffer_load_dwordx4 a[220:223], v140, %[srdB], s42 offen offset:3072 sc1\n" \
  "s_waitcnt vmcnt(25) lgkmcnt(5)\n" \
  "v_mfma_f32_32x32x16_bf16 v[0:15], v[120:123], a[176:179], v[0:15]\n" \
  "s_add_u32 m0, s60, 131072\n" \
  "ds_read_b128 v[100:103], v142 offset:30720\n" \
  "v_mfma_f32_32x32x16_bf16 v[16:31], v[124:127], a[176:179], v[16:31]\n" \
  "ds_read_b128 v[104:107], v142 offset:31744\n" \
  "v_mfma_f32_32x32x16_bf16 v[32:47], v[128:131], a[176:179], v[32:47]\n" \
  "ds_read_b128 v[108:111], v142 offset:32768\n" \
  "s_mov_b32 s42, 57344\n" \
  "v_mfma_f32_32x32x16_bf16 v[48:63], v[132:135], a[176:179], v[48:63]\n" \
  "s_mov_b32 s40, 294912\n" \
  "ds_read_b128 v[112:115], v142 offset:33792\n" \
  "v_mfma_f32_32x32x16_bf16 v[64:79], v[136:139], a[176:179], v[64:79]\n" \
  "ds_read_b128 v[116:119], v142 offset:34816\n" \
  "buffer_load_dwordx4 a[224:227], v140, %[srdB], s42 offen offset:0 sc1\n" \
  "s_waitcnt vmcnt(25) lgkmcnt(5)\n" \
  "v_mfma_f32_32x32x16_bf16 v[0:15], v[80:83], a[180:183], v[0:15]\n" \
  "buffer_load_dwordx4 v141, %[srdA], s40 offen offset:0 sc1 lds\n" \
  "ds_read_b128 v[120:123], v142 offset:35840\n" \
  "v_mfma_f32_32x32x16_bf16 v[16:31], v[84:87], a[180:183], v[16:31]\n" \
  "ds_read_b128 v[124:127], v142 offset:36864\n" \
  "v_mfma_f32_32x32x16_bf16 v[32:47], v[88:91], a[180:183], v[32:47]\n" \
  "buffer_load_dwordx4 v141, %[srdA], s40 offen offset:1024 sc1 lds\n" \
  "ds_read_b128 v[128:131], v142 offset:37888\n" \
  "s_mov_b32 s42, 57344\n" \
  "v_mfma_f32_32x32x16_bf16 v[48:63], v[92:95], a[180:183], v[48:63]\n" \
  "ds_read_b128 v[132:135], v142 offset:38912\n" \
  "v_mfma_f32_32x32x16_bf16 v[64:79], v[96:99], a[180:183], v[64:79]\n" \
  "ds_read_b128 v[136:139], v142 offset:39936\n" \
  "buffer_load_dwordx4 a[228:231], v140, %[srdB], s42 offen offset:1024 sc1\n" \
  "s_waitcnt vmcnt(19)\n" \
  "s_barrier\n" \
  "s_waitcnt lgkmcnt(5)\n" \
  "v_mfma_f32_32x32x16_bf16 v[0:15], v[100:103], a[184:187], v[0:15]\n" \
  "ds_read_b128 v[80:83], v142 offset:40960\n" \
  "v_mfma_f32_32x32x16_bf16 v[16:31], v[104:107], a[184:187], v[16:31]\n" \
  "ds_read_b128 v[84:87], v142 offset:41984\n" \
  "v_mfma_f32_32x32x16_bf16 v[32:47], v[108:111], a[184:187], v[32:47]\n" \
  "ds_read_b128 v[88:91], v142 offset:43008\n" \
  "s_mov_b32 s42, 57344\n" \
  "v_mfma_f32_32x32x16_bf16 v[48:63], v[112:115], a[184:187], v[48:63]\n" \
  "ds_read_b128 v[92:95], v142 offset:44032\n" \
  "v_mfma_f32_32x32x16_bf16 v[64:79], v[116:119], a[184:187], v[64:79]\n" \
  "ds_read_b128 v[96:99], v142 offset:45056\n" \
  "buffer_load_dwordx4 a[232:235], v140, %[srdB], s42 offen offset:2048 sc1\n" \
  "s_waitcnt lgkmcnt(5)\n" \
  "v_mfma_f32_32x32x16_bf16 v[0:15], v[120:123], a[188:191], v[0:15]\n" \
  "ds_read_b128 v[100:103], v142 offset:46080\n" \
  "v_mfma_f32_32x32x16_bf16 v[16:31], v[124:127], a[188:191], v[16:31]\n" \
  "ds_read_b128 v[104:107], v142 offset:47104\n" \
  "v_mfma_f32_32x32x16_bf16 v[32:47], v[128:131], a[188:191], v[32:47]\n" \
  "ds_read_b128 v[108:111], v142 offset:48128\n" \
  "s_mov_b32 s42, 57344\n" \
  "v_mfma_f32_32x32x16_bf16 v[48:63], v[132:135], a[188:191], v[48:63]\n" \
  "ds_read_b128 v[112:115], v142 offset:49152\n" \
  "v_mfma_f32_32x32x16_bf16 v[64:79], v[136:139], a[188:191], v[64:79]\n" \
  "ds_read_b128 v[116:119], v142 offset:50176\n" \
  "buffer_load_dwordx4 a[236:239], v140, %[srdB], s42 offen offset:3072 sc1\n" \
  "s_waitcnt lgkmcnt(5)\n" \
  "v_mfma_f32_32x32x16_bf16 v[0:15], v[80:83], a[192:195], v[0:15]\n" \
  "ds_read_b128 v[120:123], v142 offset:51200\n" \
  "v_mfma_f32_32x32x16_bf16 v[16:31], v[84:87], a[192:195], v[16:31]\n" \
  "ds_read_b128 v[124:127], v142 offset:52224\n" \
  "v_mfma_f32_32x32x16_bf16 v[32:47], v[88:91], a[192:195], v[32:47]\n" \
  "ds_read_b128 v[128:131], v142 offset:53248\n" \
  "s_mov_b32 s42, 61440\n" \
  "v_mfma_f32_32x32x16_bf16 v[48:63], v[92:95], a[192:195], v[48:63]\n" \
  "ds_read_b128 v[132:135], v142 offset:54272\n" \
  "v_mfma_f32_32x32x16_bf16 v[64:79], v[96:99], a[192:195], v[64:79]\n" \
  "ds_read_b128 v[136:139], v142 offset:55296\n" \
  "buffer_load_dwordx4 a[240:243], v140, %[srdB], s42 offen offset:0 sc1\n" \
  "s_waitcnt vmcnt(21) lgkmcnt(5)\n" \
  "v_mfma_f32_32x32x16_bf16 v[0:15], v[100:103], a[196:199], v[0:15]\n" \
  "ds_read_b128 v[80:83], v142 offset:56320\n" \
  "v_mfma_f32_32x32x16_bf16 v[16:31], v[104:107], a[196:199], v[16:31]\n" \
  "ds_read_b128 v[84:87], v142 offset:57344\n" \
  "v_mfma_f32_32x32x16_bf16 v[32:47], v[108:111], a[196:199], v[32:47]\n" \
  "ds_read_b128 v[88:91], v142 offset:58368\n" \
  "s_mov_b32 s42, 61440\n" \
  "v_mfma_f32_32x32x16_bf16 v[48:63], v[112:115], a[196:199], v[48:63]\n" \
  "ds_read_b128 v[92:95], v142 offset:59392\n" \
  "v_mfma_f32_32x32x16_bf16 v[64:79], v[116:119], a[196:199], v[64:79]\n" \
  "ds_read_b128 v[96:99], v142 offset:60416\n" \
  "buffer_load_dwordx4 a[244:247], v140, %[srdB], s42 offen offset:1024 sc1\n" \
  "s_waitcnt vmcnt(21) lgkmcnt(5)\n" \
  "v_mfma_f32_32x32x16_bf16 v[0:15], v[120:123], a[200:203], v[0:15]\n" \
  "ds_read_b128 v[100:103], v142 offset:61440\n" \
  "v_mfma_f32_32x32x16_bf16 v[16:31], v[124:127], a[200:203], v[16:31]\n" \
  "ds_read_b128 v[104:107], v142 offset:62464\n" \
  "v_mfma_f32_32x32x16_bf16 v[32:47], v[128:131], a[200:203], v[32:47]\n" \
  "ds_read_b128 v[108:111], v142 offset:63488\n" \
  "s_mov_b32 s42, 61440\n" \
  "v_mfma_f32_32x32x16_bf16 v[48:63], v[132:135], a[200:203], v[48:63]\n" \
  "ds_read_b128 v[112:115], v142 offset:64512\n" \
  "v_mfma_f32_32x32x16_bf16 v[64:79], v[136:139], a[200:203], v[64:79]\n" \
  "ds_read_b128 v[116:119], v143 offset:0\n" \
  "buffer_load_dwordx4 a[248:251], v140, %[srdB], s42 offen offset:2048 sc1\n" \
  "s_waitcnt vmcnt(19) lgkmcnt(5)\n" \
  "v_mfma_f32_32x32x16_bf16 v[0:15], v[80:83], a[204:207], v[0:15]\n" \
  "ds_read_b128 v[120:123], v143 offset:1024\n" \
  "v_mfma_f32_32x32x16_bf16 v[16:31], v[84:87], a[204:207], v[16:31]\n" \
  "ds_read_b128 v[124:127], v143 offset:2048\n" \
  "v_mfma_f32_32x32x16_bf16 v[32:47], v[88:91], a[204:207], v[32:47]\n" \
  "ds_read_b128 v[128:131], v143 offset:3072\n" \
  "s_mov_b32 s42, 61440\n" \
  "v_mfma_f32_32x32x16_bf16 v[48:63], v[92:95], a[204:207], v[48:63]\n" \
  "ds_read_b128 v[132:135], v143 offset:4096\n" \
  "v_mfma_f32_32x32x16_bf16 v[64:79], v[96:99], a[204:207], v[64:79]\n" \
  "ds_read_b128 v[136:139], v143 offset:5120\n" \
  "buffer_load_dwordx4 a[252:255], v140, %[srdB], s42 offen offset:3072 sc1\n" \
  "s_waitcnt vmcnt(17) lgkmcnt(5)\n" \
  "v_mfma_f32_32x32x16_bf16 v[0:15], v[100:103], a[208:211], v[0:15]\n" \
  "ds_read_b128 v[80:83], v143 offset:6144\n" \
  "v_perm_b32 v192, v161, v160, s56\n" \
  "v_mfma_f32_32x32x16_bf16 v[16:31], v[104:107], a[208:211], v[16:31]\n" \
  "ds_read_b128 v[84:87], v143 offset:7168\n" \
  "v_perm_b32 v193, v163, v162, s56\n" \
  "v_mfma_f32_32x32x16_bf16 v[32:47], v[108:111], a[208:211], v[32:47]\n" \
  "ds_read_b128 v[88:91], v143 offset:8192\n" \
  "v_perm_b32 v194, v165, v164, s56\n" \
  "v_mfma_f32_32x32x16_bf16 v[48:63], v[112:115], a[208:211], v[48:63]\n" \
  "ds_read_b128 v[92:95], v143 offset:9216\n" \
  "v_perm_b32 v195, v167, v166, s56\n" \
  "v_mfma_f32_32x32x16_bf16 v[64:79], v[116:119], a[208:211], v[64:79]\n" \
  "ds_read_b128 v[96:99], v143 offset:10240\n" \
  "v_perm_b32 v196, v161, v160, s57\n" \
  "s_waitcnt vmcnt(16) lgkmcnt(5)\n" \
  "v_mfma_f32_32x32x16_bf16 v[0:15], v[120:123], a[212:215], v[0:15]\n" \
  "v_perm_b32 v197, v163, v162, s57\n" \
  "ds_read_b128 v[100:103], v143 offset:11264\n" \
  "v_mfma_f32_32x32x16_bf16 v[16:31], v[124:127], a[212:215], v[16:31]\n" \
  "v_perm_b32 v198, v165, v164, s57\n" \
  "ds_read_b128 v[104:107], v143 offset:12288\n" \
  "v_mfma_f32_32x32x16_bf16 v[32:47], v[128:131], a[212:215], v[32:47]\n" \
  "v_perm_b32 v199, v167, v166, s57\n" \
  "ds_read_b128 v[108:111], v143 offset:13312\n" \
  "v_mfma_f32_32x32x16_bf16 v[48:63], v[132:135], a[212:215], v[48:63]\n" \
  "buffer_store_dwordx4 v[192:195], v151, %[srdW2d], %[gd0] offen offset:0 sc1\n" \
  "ds_read_b128 v[112:115], v143 offset:14336\n" \
  "v_mfma_f32_32x32x16_bf16 v[64:79], v[136:139], a[212:215], v[64:79]\n" \
  "buffer_store_dwordx4 v[196:199], v151, %[srdW2d], %[gd0] offen offset:16 sc1\n" \
  "ds_read_b128 v[116:119], v143 offset:15360\n" \
  "s_waitcnt vmcnt(9)\n" \
  "s_barrier\n" \
  "s_waitcnt lgkmcnt(5)\n" \
  "v_mfma_f32_32x32x16_bf16 v[0:15], v[80:83], a[216:219], v[0:15]\n" \
  "ds_read_b128 v[120:123], v143 offset:57344\n" \
  "v_mfma_f32_32x32x16_bf16 v[16:31], v[84:87], a[216:219], v[16:31]\n" \
  "ds_read_b128 v[124:127], v143 offset:58368\n" \
  "v_mfma_f32_32x32x16_bf16 v[32:47], v[88:91], a[216:219], v[32:47]\n" \
  "ds_read_b128 v[128:131], v143 offset:59392\n" \
  "v_mfma_f32_32x32x16_bf16 v[48:63], v[92:95], a[216:219], v[48:63]\n" \
  "ds_read_b128 v[132:135], v143 offset:60416\n" \
  "v_mfma_f32_32x32x16_bf16 v[64:79], v[96:99], a[216:219], v[64:79]\n" \
  "ds_read_b128 v[136:139], v143 offset:61440\n" \
  "s_waitcnt lgkmcnt(5)\n" \
  "v_mfma_f32_32x32x16_bf16 v[0:15], v[100:103], a[220:223], v[0:15]\n" \
  "ds_read_b128 v[80:83], v143 offset:62464\n" \
  "v_mfma_f32_32x32x16_bf16 v[16:31], v[104:107], a[220:223], v[16:31]\n" \
  "ds_read_b128 v[84:87], v143 offset:63488\n" \
  "v_mfma_f32_32x32x16_bf16 v[32:47], v[108:111], a[220:223], v[32:47]\n" \
  "ds_read_b128 v[88:91], v143 offset:64512\n" \
  "v_mfma_f32_32x32x16_bf16 v[48:63], v[112:115], a[220:223], v[48:63]\n" \
  "ds_read_b128 v[92:95], v144 offset:0\n" \
  "v_mfma_f32_32x32x16_bf16 v[64:79], v[116:119], a[220:223], v[64:79]\n" \
  "ds_read_b128 v[96:99], v144 offset:1024\n" \
  "s_waitcnt lgkmcnt(5)\n" \
  "v_mfma_f32_32x32x16_bf16 v[0:15], v[120:123], a[224:227], v[0:15]\n" \
  "ds_read_b128 v[100:103], v144 offset:2048\n" \
  "v_perm_b32 v208, v177, v176, s56\n" \
  "v_mfma_f32_32x32x16_bf16 v[16:31], v[124:127], a[224:227], v[16:31]\n" \
  "ds_read_b128 v[104:107], v144 offset:3072\n" \
  "v_perm_b32 v209, v179, v178, s56\n" \
  "v_mfma_f32_32x32x16_bf16 v[32:47], v[128:131], a[224:227], v[32:47]\n" \
  "ds_read_b128 v[108:111], v144 offset:4096\n" \
  "v_perm_b32 v210, v181, v180, s56\n" \
  "v_mfma_f32_32x32x16_bf16 v[48:63], v[132:135], a[224:227], v[48:63]\n" \
  "ds_read_b128 v[112:115], v144 offset:5120\n" \
  "v_perm_b32 v211, v183, v182, s56\n" \
  "v_mfma_f32_32x32x16_bf16 v[64:79], v[136:139], a[224:227], v[64:79]\n" \
  "ds_read_b128 v[116:119], v144 offset:6144\n" \
  "v_perm_b32 v212, v177, v176, s57\n" \
  "s_waitcnt vmcnt(8) lgkmcnt(5)\n" \
  "v_mfma_f32_32x32x16_bf16 v[0:15], v[80:83], a[228:231], v[0:15]\n" \
  "v_perm_b32 v213, v179, v178, s57\n" \
  "ds_read_b128 v[120:123], v144 offset:7168\n" \
  "v_mfma_f32_32x32x16_bf16 v[16:31], v[84:87], a[228:231], v[16:31]\n" \
  "v_perm_b32 v214, v181, v180, s57\n" \
  "ds_read_b128 v[124:127], v144 offset:8192\n" \
  "v_mfma_f32_32x32x16_bf16 v[32:47], v[88:91], a[228:231], v[32:47]\n" \
  "v_perm_b32 v215, v183, v182, s57\n" \
  "ds_read_b128 v[128:131], v144 offset:9216\n" \
  "v_mfma_f32_32x32x16_bf16 v[48:63], v[92:95], a[228:231], v[48:63]\n" \
  "buffer_store_dwordx4 v[208:211], v151, %[srdW2d], %[gd1] offen offset:0 sc1\n" \
  "ds_read_b128 v[132:135], v144 offset:10240\n" \
  "v_mfma_f32_32x32x16_bf16 v[64:79], v[96:99], a[228:231], v[64:79]\n" \
  "buffer_store_dwordx4 v[212:215], v151, %[srdW2d], %[gd1] offen offset:16 sc1\n" \
  "ds_read_b128 v[136:139], v144 offset:11264\n" \
  "s_waitcnt vmcnt(9) lgkmcnt(5)\n" \
  "v_mfma_f32_32x32x16_bf16 v[0:15], v[100:103], a[232:235], v[0:15]\n" \
  "ds_read_b128 v[80:83], v144 offset:12288\n" \
  "v_mfma_f32_32x32x16_bf16 v[16:31], v[104:107], a[232:235], v[16:31]\n" \
  "ds_read_b128 v[84:87], v144 offset:13312\n" \
  "v_mfma_f32_32x32x16_bf16 v[32:47], v[108:111], a[232:235], v[32:47]\n" \
  "ds_read_b128 v[88:91], v144 offset:14336\n" \
  "v_mfma_f32_32x32x16_bf16 v[48:63], v[112:115], a[232:235], v[48:63]\n" \
  "ds_read_b128 v[92:95], v144 offset:15360\n" \
  "v_mfma_f32_32x32x16_bf16 v[64:79], v[116:119], a[232:235], v[64:79]\n" \
  "ds_read_b128 v[96:99], v144 offset:16384\n" \
  "s_waitcnt vmcnt(8) lgkmcnt(5)\n" \
  "v_mfma_f32_32x32x16_bf16 v[0:15], v[120:123], a[236:239], v[0:15]\n" \
  "ds_read_b128 v[100:103], v144 offset:17408\n" \
  "v_mfma_f32_32x32x16_bf16 v[16:31], v[124:127], a[236:239], v[16:31]\n" \
  "ds_read_b128 v[104:107], v144 offset:18432\n" \
  "v_mfma_f32_32x32x16_bf16 v[32:47], v[128:131], a[236:239], v[32:47]\n" \
  "ds_read_b128 v[108:111], v144 offset:19456\n" \
  "v_mfma_f32_32x32x16_bf16 v[48:63], v[132:135], a[236:239], v[48:63]\n" \
  "ds_read_b128 v[112:115], v144 offset:20480\n" \
  "v_mfma_f32_32x32x16_bf16 v[64:79], v[136:139], a[236:239], v[64:79]\n" \
  "ds_read_b128 v[116:119], v144 offset:21504\n" \
  "s_waitcnt vmcnt(7) lgkmcnt(5)\n" \
  "v_mfma_f32_32x32x16_bf16 v[0:15], v[80:83], a[240:243], v[0:15]\n" \
  "ds_read_b128 v[120:123], v144 offset:22528\n" \
  "v_mfma_f32_32x32x16_bf16 v[16:31], v[84:87], a[240:243], v[16:31]\n" \
  "ds_read_b128 v[124:127], v144 offset:23552\n" \
  "v_mfma_f32_32x32x16_bf16 v[32:47], v[88:91], a[240:243], v[32:47]\n" \
  "ds_read_b128 v[128:131], v144 offset:24576\n" \
  "v_mfma_f32_32x32x16_bf16 v[48:63], v[92:95], a[240:243], v[48:63]\n" \
  "ds_read_b128 v[132:135], v144 offset:25600\n" \
  "v_mfma_f32_32x32x16_bf16 v[64:79], v[96:99], a[240:243], v[64:79]\n" \
  "ds_read_b128 v[136:139], v144 offset:26624\n" \
  "s_waitcnt vmcnt(6) lgkmcnt(5)\n" \
  "v_mfma_f32_32x32x16_bf16 v[0:15], v[100:103], a[244:247], v[0:15]\n" \
  "ds_read_b128 v[80:83], v144 offset:27648\n" \
  "v_mfma_f32_32x32x16_bf16 v[16:31], v[104:107], a[244:247], v[16:31]\n" \
  "ds_read_b128 v[84:87], v144 offset:28672\n" \
  "v_mfma_f32_32x32x16_bf16 v[32:47], v[108:111], a[244:247], v[32:47]\n" \
  "ds_read_b128 v[88:91], v144 offset:29696\n" \
  "v_mfma_f32_32x32x16_bf16 v[48:63], v[112:115], a[244:247], v[48:63]\n" \
  "ds_read_b128 v[92:95], v144 offset:30720\n" \
  "v_mfma_f32_32x32x16_bf16 v[64:79], v[116:119], a[244:247], v[64:79]\n" \
  "ds_read_b128 v[96:99], v144 offset:31744\n" \
  "s_waitcnt vmcnt(5) lgkmcnt(5)\n" \
  "v_mfma_f32_32x32x16_bf16 v[0:15], v[120:123], a[248:251], v[0:15]\n" \
  "v_mfma_f32_32x32x16_bf16 v[16:31], v[124:127], a[248:251], v[16:31]\n" \
  "v_mfma_f32_32x32x16_bf16 v[32:47], v[128:131], a[248:251], v[32:47]\n" \
  "v_mfma_f32_32x32x16_bf16 v[48:63], v[132:135], a[248:251], v[48:63]\n" \
  "v_mfma_f32_32x32x16_bf16 v[64:79], v[136:139], a[248:251], v[64:79]\n" \
  "s_waitcnt vmcnt(4) lgkmcnt(0)\n" \
  "v_mfma_f32_32x32x16_bf16 v[0:15], v[80:83], a[252:255], v[0:15]\n" \
  "v_mfma_f32_32x32x16_bf16 v[16:31], v[84:87], a[252:255], v[16:31]\n" \
  "v_mfma_f32_32x32x16_bf16 v[32:47], v[88:91], a[252:255], v[32:47]\n" \
  "v_mfma_f32_32x32x16_bf16 v[48:63], v[92:95], a[252:255], v[48:63]\n" \
  "v_mfma_f32_32x32x16_bf16 v[64:79], v[96:99], a[252:255], v[64:79]\n" \
  "s_nop 7\n" \
  "v_cvt_pk_bf16_f32 v0, v0, v1\n" \
  "v_cvt_pk_bf16_f32 v1, v2, v3\n" \
  "ds_write_b64 v145, v[0:1] offset:0\n" \
  "v_cvt_pk_bf16_f32 v4, v4, v5\n" \
  "v_cvt_pk_bf16_f32 v5, v6, v7\n" \
  "ds_write_b64 v145, v[4:5] offset:16\n" \
  "v_cvt_pk_bf16_f32 v8, v8, v9\n" \
  "v_cvt_pk_bf16_f32 v9, v10, v11\n" \
  "ds_write_b64 v145, v[8:9] offset:32\n" \
  "v_cvt_pk_bf16_f32 v12, v12, v13\n" \
  "v_cvt_pk_bf16_f32 v13, v14, v15\n" \
  "ds_write_b64 v145, v[12:13] offset:48\n" \
  "v_cvt_pk_bf16_f32 v16, v16, v17\n" \
  "v_cvt_pk_bf16_f32 v17, v18, v19\n" \
  "ds_write_b64 v145, v[16:17] offset:64\n" \
  "v_cvt_pk_bf16_f32 v20, v20, v21\n" \
  "v_cvt_pk_bf16_f32 v21, v22, v23\n" \
  "ds_write_b64 v145, v[20:21] offset:80\n" \
  "v_cvt_pk_bf16_f32 v24, v24, v25\n" \
  "v_cvt_pk_bf16_f32 v25, v26, v27\n" \
  "ds_write_b64 v145, v[24:25] offset:96\n" \
  "v_cvt_pk_bf16_f32 v28, v28, v29\n" \
  "v_cvt_pk_bf16_f32 v29, v30, v31\n" \
  "ds_write_b64 v145, v[28:29] offset:112\n" \
  "v_cvt_pk_bf16_f32 v32, v32, v33\n" \
  "v_cvt_pk_bf16_f32 v33, v34, v35\n" \
  "ds_write_b64 v145, v[32:33] offset:128\n" \
  "v_cvt_pk_bf16_f32 v36, v36, v37\n" \
  "v_cvt_pk_bf16_f32 v37, v38, v39\n" \
  "ds_write_b64 v145, v[36:37] offset:144\n" \
  "v_cvt_pk_bf16_f32 v40, v40, v41\n" \
  "v_cvt_pk_bf16_f32 v41, v42, v43\n" \
  "ds_write_b64 v145, v[40:41] offset:160\n" \
  "v_cvt_pk_bf16_f32 v44, v44, v45\n" \
  "v_cvt_pk_bf16_f32 v45, v46, v47\n" \
  "ds_write_b64 v145, v[44:45] offset:176\n" \
  "v_cvt_pk_bf16_f32 v48, v48, v49\n" \
  "v_cvt_pk_bf16_f32 v49, v50, v51\n" \
  "ds_write_b64 v145, v[48:49] offset:192\n" \
  "v_cvt_pk_bf16_f32 v52, v52, v53\n" \
  "v_cvt_pk_bf16_f32 v53, v54, v55\n" \
  "ds_write_b64 v145, v[52:53] offset:208\n" \
  "v_cvt_pk_bf16_f32 v56, v56, v57\n" \
  "v_cvt_pk_bf16_f32 v57, v58, v59\n" \
  "ds_write_b64 v145, v[56:57] offset:224\n" \
  "v_cvt_pk_bf16_f32 v60, v60, v61\n" \
  "v_cvt_pk_bf16_f32 v61, v62, v63\n" \
  "ds_write_b64 v145, v[60:61] offset:240\n" \
  "v_cvt_pk_bf16_f32 v64, v64, v65\n" \
  "v_cvt_pk_bf16_f32 v65, v66, v67\n" \
  "ds_write_b64 v145, v[64:65] offset:256\n" \
  "v_cvt_pk_bf16_f32 v68, v68, v69\n" \
  "v_cvt_pk_bf16_f32 v69, v70, v71\n" \
  "ds_write_b64 v145, v[68:69] offset:272\n" \
  "v_cvt_pk_bf16_f32 v72, v72, v73\n" \
  "v_cvt_pk_bf16_f32 v73, v74, v75\n" \
  "ds_write_b64 v145, v[72:73] offset:288\n" \
  "v_cvt_pk_bf16_f32 v76, v76, v77\n" \
  "v_cvt_pk_bf16_f32 v77, v78, v79\n" \
  "ds_write_b64 v145, v[76:77] offset:304\n" \
  "s_waitcnt lgkmcnt(0)\n" \
  "ds_read_b128 v[80:83], v146 offset:0\n" \
  "ds_read_b128 v[84:87], v146 offset:1008\n" \
  "ds_read_b128 v[88:91], v146 offset:2016\n" \
  "ds_read_b128 v[92:95], v146 offset:3024\n" \
  "ds_read_b128 v[96:99], v146 offset:4032\n" \
  "ds_read_b128 v[100:103], v146 offset:5040\n" \
  "ds_read_b128 v[104:107], v146 offset:6048\n" \
  "ds_read_b128 v[108:111], v146 offset:7056\n" \
  "ds_read_b128 v[112:115], v146 offset:8064\n" \
  "ds_read_b128 v[116:119], v146 offset:9072\n" \
  "ds_read_b128 v[120:123], v146 offset:10080\n" \
  "s_mov_b32 s40, 0\n" \
  "s_waitcnt lgkmcnt(10)\n" \
  "buffer_store_dwordx4 v[80:83], v147, %[srdP], s40 offen offset:0 sc1\n" \
  "s_mov_b32 s40, 3840\n" \
  "s_waitcnt lgkmcnt(9)\n" \
  "buffer_store_dwordx4 v[84:87], v147, %[srdP], s40 offen offset:0 sc1\n" \
  "s_mov_b32 s40, 7680\n" \
  "s_waitcnt lgkmcnt(8)\n" \
  "buffer_store_dwordx4 v[88:91], v147, %[srdP], s40 offen offset:0 sc1\n" \
  "s_mov_b32 s40, 11520\n" \
  "s_waitcnt lgkmcnt(7)\n" \
  "buffer_store_dwordx4 v[92:95], v147, %[srdP], s40 offen offset:0 sc1\n" \
  "s_mov_b32 s40, 15360\n" \
  "s_waitcnt lgkmcnt(6)\n" \
  "buffer_store_dwordx4 v[96:99], v147, %[srdP], s40 offen offset:0 sc1\n" \
  "s_mov_b32 s40, 19200\n" \
  "s_waitcnt lgkmcnt(5)\n" \
  "buffer_store_dwordx4 v[100:103], v147, %[srdP], s40 offen offset:0 sc1\n" \
  "s_mov_b32 s40, 23040\n" \
  "s_waitcnt lgkmcnt(4)\n" \
  "buffer_store_dwordx4 v[104:107], v147, %[srdP], s40 offen offset:0 sc1\n" \
  "s_mov_b32 s40, 26880\n" \
  "s_waitcnt lgkmcnt(3)\n" \
  "buffer_store_dwordx4 v[108:111], v147, %[srdP], s40 offen offset:0 sc1\n" \
  "s_mov_b32 s40, 30720\n" \
  "s_waitcnt lgkmcnt(2)\n" \
  "buffer_store_dwordx4 v[112:115], v147, %[srdP], s40 offen offset:0 sc1\n" \
  "s_mov_b32 s40, 34560\n" \
  "s_waitcnt lgkmcnt(1)\n" \
  "buffer_store_dwordx4 v[116:119], v147, %[srdP], s40 offen offset:0 sc1\n" \
  "s_mov_b32 s40, 38400\n" \
  "s_waitcnt lgkmcnt(0)\n" \
  "buffer_store_dwordx4 v[120:123], v147, %[srdP], s40 offen offset:0 sc1\n" \
  "s_waitcnt vmcnt(0)\n" \
  "v_mov_b32 v0, %[tag]\n" \
  "v_mov_b32 v1, 0\n" \
  "s_mov_b64 exec, 1\n" \
  "global_store_dword v1, v0, %[fpw] sc1\n" \
  "s_mov_b64 exec, -1\n"

#define T34_ASM \
  "s_nop 4\n" \
  "v_mbcnt_lo_u32_b32 v0, -1, 0\n" \
  "v_mbcnt_hi_u32_b32 v0, -1, v0\n" \
  "s_mul_i32 s60, %[w], 20480\n" \
  "s_mul_i32 s61, %[w], 40960\n" \
  "v_lshlrev_b32 v240, 4, v0\n" \
  "v_mov_b32 v241, v240\n" \
  "v_add_u32 v242, 65536, v240\n" \
  "v_add_u32 v243, 131072, v240\n" \
  "v_lshrrev_b32 v1, 4, v0\n" \
  "v_lshlrev_b32 v1, 3, v1\n" \
  "v_add_u32 v244, %[cbias], v1\n" \
  "v_add_u32 v245, 8, v240\n" \
  "v_add_u32 v246, s60, v240\n" \
  "v_add_u32 v247, s61, v240\n" \
  "s_mov_b32 s42, 0\n" \
  "buffer_load_dwordx4 a[0:3], v240, %[srdH2], s42 offen offset:0 sc1\n" \
  "s_mov_b32 s42, 20480\n" \
  "buffer_load_dwordx4 a[80:83], v240, %[srdH2], s42 offen offset:0 sc1\n" \
  "s_mov_b32 s42, 0\n" \
  "buffer_load_dwordx4 a[4:7], v240, %[srdH2], s42 offen offset:1024 sc1\n" \
  "s_mov_b32 s42, 20480\n" \
  "buffer_load_dwordx4 a[84:87], v240, %[srdH2], s42 offen offset:1024 sc1\n" \
  "buffer_load_dwordx2 v[148:149], v244, %[srdB1], 0 offen offset:0\n" \
  "buffer_load_dwordx2 v[150:151], v244, %[srdB1], 0 offen offset:32\n" \
  "buffer_load_dwordx2 v[152:153], v244, %[srdB1], 0 offen offset:64\n" \
  "buffer_load_dwordx2 v[154:155], v244, %[srdB1], 0 offen offset:96\n" \
  "buffer_load_dwordx2 v[156:157], v244, %[srdB1], 0 offen offset:128\n" \
  "buffer_load_dwordx2 v[158:159], v244, %[srdB1], 0 offen offset:160\n" \
  "buffer_load_dwordx2 v[160:161], v244, %[srdB1], 0 offen offset:192\n" \
  "s_mov_b32 s42, 0\n" \
  "buffer_load_dwordx4 a[8:11], v240, %[srdH2], s42 offen offset:2048 sc1\n" \
  "s_mov_b32 s42, 20480\n" \
  "buffer_load_dwordx4 a[88:91], v240, %[srdH2], s42 offen offset:2048 sc1\n" \
  "s_mov_b32 s42, 0\n" \
  "buffer_load_dwordx4 a[12:15], v240, %[srdH2], s42 offen offset:3072 sc1\n" \
  "s_mov_b32 s42, 20480\n" \
  "buffer_load_dwordx4 a[92:95], v240, %[srdH2], s42 offen offset:3072 sc1\n" \
  "global_load_dwordx4 a[200:203], v240, %[fx1] sc1\n" \
  "ds_read_b128 v[64:67], v241 offset:0\n" \
  "ds_read_b128 v[68:71], v241 offset:1024\n" \
  "ds_read_b128 v[72:75], v241 offset:2048\n" \
  "ds_read_b128 v[76:79], v241 offset:3072\n" \
  "ds_read_b128 v[80:83], v241 offset:4096\n" \
  "ds_read_b128 v[84:87], v241 offset:5120\n" \
  "ds_read_b128 v[88:91], v241 offset:6144\n" \
  "ds_read_b128 v[92:95], v241 offset:7168\n" \
  "ds_read_b128 v[96:99], v241 offset:8192\n" \
  "ds_read_b128 v[100:103], v241 offset:9216\n" \
  "ds_read_b128 v[104:107], v241 offset:10240\n" \
  "ds_read_b128 v[108:111], v241 offset:11264\n" \
  "ds_read_b128 v[112:115], v241 offset:12288\n" \
  "ds_read_b128 v[116:119], v241 offset:13312\n" \
  "s_waitcnt vmcnt(15) lgkmcnt(13)\n" \
  "v_mfma_f32_16x16x32_bf16 v[0:3], v[64:67], a[0:3], 0\n" \
  "s_waitcnt vmcnt(14)\n" \
  "v_mfma_f32_16x16x32_bf16 v[28:31], v[64:67], a[80:83], 0\n" \
  "ds_read_b128 v[120:123], v241 offset:14336\n" \
  "s_mov_b32 s42, 4096\n" \
  "s_waitcnt lgkmcnt(13)\n" \
  "v_mfma_f32_16x16x32_bf16 v[4:7], v[68:71], a[0:3], 0\n" \
  "v_mfma_f32_16x16x32_bf16 v[32:35], v[68:71], a[80:83], 0\n" \
  "ds_read_b128 v[124:127], v241 offset:15360\n" \
  "buffer_load_dwordx4 a[16:19], v240, %[srdH2], s42 offen offset:0 sc1\n" \
  "s_waitcnt lgkmcnt(13)\n" \
  "v_mfma_f32_16x16x32_bf16 v[8:11], v[72:75], a[0:3], 0\n" \
  "v_mfma_f32_16x16x32_bf16 v[36:39], v[72:75], a[80:83], 0\n" \
  "ds_read_b128 v[128:131], v241 offset:16384\n" \
  "s_mov_b32 s42, 24576\n" \
  "s_waitcnt lgkmcnt(3)\n" \
  "v_mfma_f32_16x16x32_bf16 v[12:15], v[76:79], a[0:3], 0\n" \
  "buffer_load_dwordx4 a[96:99], v240, %[srdH2], s42 offen offset:0 sc1\n" \
  "v_mfma_f32_16x16x32_bf16 v[40:43], v[76:79], a[80:83], 0\n" \
  "ds_read_b128 v[132:135], v241 offset:17408\n" \
  "v_mfma_f32_16x16x32_bf16 v[16:19], v[80:83], a[0:3], 0\n" \
  "s_mov_b32 s42, 4096\n" \
  "v_mfma_f32_16x16x32_bf16 v[44:47], v[80:83], a[80:83], 0\n" \
  "ds_read_b128 v[136:139], v241 offset:18432\n" \
  "v_mfma_f32_16x16x32_bf16 v[20:23], v[84:87], a[0:3], 0\n" \
  "buffer_load_dwordx4 a[20:23], v240, %[srdH2], s42 offen offset:1024 sc1\n" \
  "v_mfma_f32_16x16x32_bf16 v[48:51], v[84:87], a[80:83], 0\n" \
  "ds_read_b128 v[140:143], v241 offset:19456\n" \
  "v_mfma_f32_16x16x32_bf16 v[24:27], v[88:91], a[0:3], 0\n" \
  "s_mov_b32 s42, 24576\n" \
  "v_mfma_f32_16x16x32_bf16 v[52:55], v[88:91], a[80:83], 0\n" \
  "ds_read_b128 v[144:147], v241 offset:20480\n" \
  "buffer_load_dwordx4 a[100:103], v240, %[srdH2], s42 offen offset:1024 sc1\n" \
  "s_waitcnt vmcnt(17)\n" \
  "v_mfma_f32_16x16x32_bf16 v[0:3], v[92:95], a[4:7], v[0:3]\n" \
  "s_waitcnt vmcnt(16)\n" \
  "v_mfma_f32_16x16x32_bf16 v[28:31], v[92:95], a[84:87], v[28:31]\n" \
  "ds_read_b128 v[64:67], v241 offset:21504\n" \
  "s_mov_b32 s42, 4096\n" \
  "v_mfma_f32_16x16x32_bf16 v[4:7], v[96:99], a[4:7], v[4:7]\n" \
  "v_mfma_f32_16x16x32_bf16 v[32:35], v[96:99], a[84:87], v[32:35]\n" \
  "ds_read_b128 v[68:71], v241 offset:22528\n" \
  "buffer_load_dwordx4 a[24:27], v240, %[srdH2], s42 offen offset:2048 sc1\n" \
  "v_mfma_f32_16x16x32_bf16 v[8:11], v[100:103], a[4:7], v[8:11]\n" \
  "v_mfma_f32_16x16x32_bf16 v[36:39], v[100:103], a[84:87], v[36:39]\n" \
  "ds_read_b128 v[72:75], v241 offset:23552\n" \
  "s_mov_b32 s42, 24576\n" \
  "v_mfma_f32_16x16x32_bf16 v[12:15], v[104:107], a[4:7], v[12:15]\n" \
  "buffer_load_dwordx4 a[104:107], v240, %[srdH2], s42 offen offset:2048 sc1\n" \
  "v_mfma_f32_16x16x32_bf16 v[40:43], v[104:107], a[84:87], v[40:43]\n" \
  "ds_read_b128 v[76:79], v241 offset:24576\n" \
  "v_mfma_f32_16x16x32_bf16 v[16:19], v[108:111], a[4:7], v[16:19]\n" \
  "s_mov_b32 s42, 4096\n" \
  "v_mfma_f32_16x16x32_bf16 v[44:47], v[108:111], a[84:87], v[44:47]\n" \
  "ds_read_b128 v[80:83], v241 offset:25600\n" \
  "v_mfma_f32_16x16x32_bf16 v[20:23], v[112:115], a[4:7], v[20:23]\n" \
  "buffer_load_dwordx4 a[28:31], v240, %[srdH2], s42 offen offset:3072 sc1\n" \
  "v_mfma_f32_16x16x32_bf16 v[48:51], v[112:115], a[84:87], v[48:51]\n" \
  "ds_read_b128 v[84:87], v241 offset:26624\n" \
  "v_mfma_f32_16x16x32_bf16 v[24:27], v[116:119], a[4:7], v[24:27]\n" \
  "s_mov_b32 s42, 24576\n" \
  "v_mfma_f32_16x16x32_bf16 v[52:55], v[116:119], a[84:87], v[52:55]\n" \
  "ds_read_b128 v[88:91], v241 offset:27648\n" \
  "buffer_load_dwordx4 a[108:111], v240, %[srdH2], s42 offen offset:3072 sc1\n" \
  "s_waitcnt vmcnt(12) lgkmcnt(3)\n" \
  "v_mfma_f32_16x16x32_bf16 v[0:3], v[120:123], a[8:11], v[0:3]\n" \
  "s_waitcnt vmcnt(11)\n" \
  "v_mfma_f32_16x16x32_bf16 v[28:31], v[120:123], a[88:91], v[28:31]\n" \
  "ds_read_b128 v[92:95], v241 offset:28672\n" \
  "s_mov_b32 s42, 8192\n" \
  "v_mfma_f32_16x16x32_bf16 v[4:7], v[124:127], a[8:11], v[4:7]\n" \
  "v_mfma_f32_16x16x32_bf16 v[32:35], v[124:127], a[88:91], v[32:35]\n" \
  "ds_read_b128 v[96:99], v241 offset:29696\n" \
  "buffer_load_dwordx4 a[32:35], v240, %[srdH2], s42 offen offset:0 sc1\n" \
  "v_mfma_f32_16x16x32_bf16 v[8:11], v[128:131], a[8:11], v[8:11]\n" \
  "v_mfma_f32_16x16x32_bf16 v[36:39], v[128:131], a[88:91], v[36:39]\n" \
  "ds_read_b128 v[100:103], v241 offset:30720\n" \
  "s_mov_b32 s42, 28672\n" \
  "v_mfma_f32_16x16x32_bf16 v[12:15], v[132:135], a[8:11], v[12:15]\n" \
  "buffer_load_dwordx4 a[112:115], v240, %[srdH2], s42 offen offset:0 sc1\n" \
  "v_mfma_f32_16x16x32_bf16 v[40:43], v[132:135], a[88:91], v[40:43]\n" \
  "ds_read_b128 v[104:107], v241 offset:31744\n" \
  "v_mfma_f32_16x16x32_bf16 v[16:19], v[136:139], a[8:11], v[16:19]\n" \
  "s_mov_b32 s42, 8192\n" \
  "v_mfma_f32_16x16x32_bf16 v[44:47], v[136:139], a[88:91], v[44:47]\n" \
  "ds_read_b128 v[108:111], v241 offset:32768\n" \
  "v_mfma_f32_16x16x32_bf16 v[20:23], v[140:143], a[8:11], v[20:23]\n" \
  "buffer_load_dwordx4 a[36:39], v240, %[srdH2], s42 offen offset:1024 sc1\n" \
  "v_mfma_f32_16x16x32_bf16 v[48:51], v[140:143], a[88:91], v[48:51]\n" \
  "ds_read_b128 v[112:115], v241 offset:33792\n" \
  "v_mfma_f32_16x16x32_bf16 v[24:27], v[144:147], a[8:11], v[24:27]\n" \
  "s_mov_b32 s42, 28672\n" \
  "v_mfma_f32_16x16x32_bf16 v[52:55], v[144:147], a[88:91], v[52:55]\n" \
  "ds_read_b128 v[116:119], v241 offset:34816\n" \
  "buffer_load_dwordx4 a[116:119], v240, %[srdH2], s42 offen offset:1024 sc1\n" \
  "s_waitcnt vmcnt(14)\n" \
  "v_mfma_f32_16x16x32_bf16 v[0:3], v[64:67], a[12:15], v[0:3]\n" \
  "s_waitcnt vmcnt(13)\n" \
  "v_mfma_f32_16x16x32_bf16 v[28:31], v[64:67], a[92:95], v[28:31]\n" \
  "ds_read_b128 v[120:123], v241 offset:35840\n" \
  "s_mov_b32 s42, 8192\n" \
  "v_mfma_f32_16x16x32_bf16 v[4:7], v[68:71], a[12:15], v[4:7]\n" \
  "v_mfma_f32_16x16x32_bf16 v[32:35], v[68:71], a[92:95], v[32:35]\n" \
  "ds_read_b128 v[124:127], v241 offset:36864\n" \
  "buffer_load_dwordx4 a[40:43], v240, %[srdH2], s42 offen offset:2048 sc1\n" \
  "v_mfma_f32_16x16x32_bf16 v[8:11], v[72:75], a[12:15], v[8:11]\n" \
  "v_mfma_f32_16x16x32_bf16 v[36:39], v[72:75], a[92:95], v[36:39]\n" \
  "ds_read_b128 v[128:131], v241 offset:37888\n" \
  "s_mov_b32 s42, 28672\n" \
  "v_mfma_f32_16x16x32_bf16 v[12:15], v[76:79], a[12:15], v[12:15]\n" \
  "buffer_load_dwordx4 a[120:123], v240, %[srdH2], s42 offen offset:2048 sc1\n" \
  "v_mfma_f32_16x16x32_bf16 v[40:43], v[76:79], a[92:95], v[40:43]\n" \
  "ds_read_b128 v[132:135], v241 offset:38912\n" \
  "s_waitcnt lgkmcnt(3)\n" \
  "v_mfma_f32_16x16x32_bf16 v[16:19], v[80:83], a[12:15], v[16:19]\n" \
  "s_mov_b32 s42, 8192\n" \
  "v_mfma_f32_16x16x32_bf16 v[44:47], v[80:83], a[92:95], v[44:47]\n" \
  "ds_read_b128 v[136:139], v241 offset:39936\n" \
  "v_mfma_f32_16x16x32_bf16 v[20:23], v[84:87], a[12:15], v[20:23]\n" \
  "buffer_load_dwordx4 a[44:47], v240, %[srdH2], s42 offen offset:3072 sc1\n" \
  "v_mfma_f32_16x16x32_bf16 v[48:51], v[84:87], a[92:95], v[48:51]\n" \
  "ds_read_b128 v[140:143], v241 offset:40960\n" \
  "v_mfma_f32_16x16x32_bf16 v[24:27], v[88:91], a[12:15], v[24:27]\n" \
  "s_mov_b32 s42, 28672\n" \
  "v_mfma_f32_16x16x32_bf16 v[52:55], v[88:91], a[92:95], v[52:55]\n" \
  "ds_read_b128 v[144:147], v241 offset:41984\n" \
  "buffer_load_dwordx4 a[124:127], v240, %[srdH2], s42 offen offset:3072 sc1\n" \
  "s_waitcnt vmcnt(15)\n" \
  "v_mfma_f32_16x16x32_bf16 v[0:3], v[92:95], a[16:19], v[0:3]\n" \
  "s_waitcnt vmcnt(14)\n" \
  "v_mfma_f32_16x16x32_bf16 v[28:31], v[92:95], a[96:99], v[28:31]\n" \
  "ds_read_b128 v[64:67], v241 offset:43008\n" \
  "s_mov_b32 s42, 12288\n" \
  "v_mfma_f32_16x16x32_bf16 v[4:7], v[96:99], a[16:19], v[4:7]\n" \
  "v_mfma_f32_16x16x32_bf16 v[32:35], v[96:99], a[96:99], v[32:35]\n" \
  "ds_read_b128 v[68:71], v241 offset:44032\n" \
  "buffer_load_dwordx4 a[48:51], v240, %[srdH2], s42 offen offset:0 sc1\n" \
  "v_mfma_f32_16x16x32_bf16 v[8:11], v[100:103], a[16:19], v[8:11]\n" \
  "v_mfma_f32_16x16x32_bf16 v[36:39], v[100:103], a[96:99], v[36:39]\n" \
  "ds_read_b128 v[72:75], v241 offset:45056\n" \
  "s_mov_b32 s42, 32768\n" \
  "v_mfma_f32_16x16x32_bf16 v[12:15], v[104:107], a[16:19], v[12:15]\n" \
  "buffer_load_dwordx4 a[128:131], v240, %[srdH2], s42 offen offset:0 sc1\n" \
  "v_mfma_f32_16x16x32_bf16 v[40:43], v[104:107], a[96:99], v[40:43]\n" \
  "ds_read_b128 v[76:79], v241 offset:46080\n" \
  "v_mfma_f32_16x16x32_bf16 v[16:19], v[108:111], a[16:19], v[16:19]\n" \
  "s_mov_b32 s42, 12288\n" \
  "v_mfma_f32_16x16x32_bf16 v[44:47], v[108:111], a[96:99], v[44:47]\n" \
  "ds_read_b128 v[80:83], v241 offset:47104\n" \
  "v_mfma_f32_16x16x32_bf16 v[20:23], v[112:115], a[16:19], v[20:23]\n" \
  "buffer_load_dwordx4 a[52:55], v240, %[srdH2], s42 offen offset:1024 sc1\n" \
  "v_mfma_f32_16x16x32_bf16 v[48:51], v[112:115], a[96:99], v[48:51]\n" \
  "ds_read_b128 v[84:87], v241 offset:48128\n" \
  "v_mfma_f32_16x16x32_bf16 v[24:27], v[116:119], a[16:19], v[24:27]\n" \
  "s_mov_b32 s42, 32768\n" \
  "v_mfma_f32_16x16x32_bf16 v[52:55], v[116:119], a[96:99], v[52:55]\n" \
  "ds_read_b128 v[88:91], v241 offset:49152\n" \
  "buffer_load_dwordx4 a[132:135], v240, %[srdH2], s42 offen offset:1024 sc1\n" \
  "s_waitcnt vmcnt(17)\n" \
  "v_mfma_f32_16x16x32_bf16 v[0:3], v[120:123], a[20:23], v[0:3]\n" \
  "s_waitcnt vmcnt(16)\n" \
  "v_mfma_f32_16x16x32_bf16 v[28:31], v[120:123], a[100:103], v[28:31]\n" \
  "ds_read_b128 v[92:95], v241 offset:50176\n" \
  "s_mov_b32 s42, 12288\n" \
  "s_waitcnt lgkmcnt(3)\n" \
  "v_mfma_f32_16x16x32_bf16 v[4:7], v[124:127], a[20:23], v[4:7]\n" \
  "v_mfma_f32_16x16x32_bf16 v[32:35], v[124:127], a[100:103], v[32:35]\n" \
  "ds_read_b128 v[96:99], v241 offset:51200\n" \
  "buffer_load_dwordx4 a[56:59], v240, %[srdH2], s42 offen offset:2048 sc1\n" \
  "v_mfma_f32_16x16x32_bf16 v[8:11], v[128:131], a[20:23], v[8:11]\n" \
  "v_mfma_f32_16x16x32_bf16 v[36:39], v[128:131], a[100:103], v[36:39]\n" \
  "ds_read_b128 v[100:103], v241 offset:52224\n" \
  "s_mov_b32 s42, 32768\n" \
  "v_mfma_f32_16x16x32_bf16 v[12:15], v[132:135], a[20:23], v[12:15]\n" \
  "buffer_load_dwordx4 a[136:139], v240, %[srdH2], s42 offen offset:2048 sc1\n" \
  "v_mfma_f32_16x16x32_bf16 v[40:43], v[132:135], a[100:103], v[40:43]\n" \
  "ds_read_b128 v[104:107], v241 offset:53248\n" \
  "v_mfma_f32_16x16x32_bf16 v[16:19], v[136:139], a[20:23], v[16:19]\n" \
  "s_mov_b32 s42, 12288\n" \
  "v_mfma_f32_16x16x32_bf16 v[44:47], v[136:139], a[100:103], v[44:47]\n" \
  "ds_read_b128 v[108:111], v241 offset:54272\n" \
  "v_mfma_f32_16x16x32_bf16 v[20:23], v[140:143], a[20:23], v[20:23]\n" \
  "buffer_load_dwordx4 a[60:63], v240, %[srdH2], s42 offen offset:3072 sc1\n" \
  "v_mfma_f32_16x16x32_bf16 v[48:51], v[140:143], a[100:103], v[48:51]\n" \
  "ds_read_b128 v[112:115], v241 offset:55296\n" \
  "v_mfma_f32_16x16x32_bf16 v[24:27], v[144:147], a[20:23], v[24:27]\n" \
  "s_mov_b32 s42, 32768\n" \
  "v_mfma_f32_16x16x32_bf16 v[52:55], v[144:147], a[100:103], v[52:55]\n" \
  "ds_read_b128 v[116:119], v241 offset:56320\n" \
  "buffer_load_dwordx4 a[140:143], v240, %[srdH2], s42 offen offset:3072 sc1\n" \
  "s_waitcnt vmcnt(19)\n" \
  "v_mfma_f32_16x16x32_bf16 v[0:3], v[64:67], a[24:27], v[0:3]\n" \
  "s_waitcnt vmcnt(18)\n" \
  "v_mfma_f32_16x16x32_bf16 v[28:31], v[64:67], a[104:107], v[28:31]\n" \
  "ds_read_b128 v[120:123], v241 offset:57344\n" \
  "s_mov_b32 s42, 16384\n" \
  "v_mfma_f32_16x16x32_bf16 v[4:7], v[68:71], a[24:27], v[4:7]\n" \
  "v_mfma_f32_16x16x32_bf16 v[32:35], v[68:71], a[104:107], v[32:35]\n" \
  "ds_read_b128 v[124:127], v241 offset:58368\n" \
  "buffer_load_dwordx4 a[64:67], v240, %[srdH2], s42 offen offset:0 sc1\n" \
  "v_mfma_f32_16x16x32_bf16 v[8:11], v[72:75], a[24:27], v[8:11]\n" \
  "v_mfma_f32_16x16x32_bf16 v[36:39], v[72:75], a[104:107], v[36:39]\n" \
  "ds_read_b128 v[128:131], v241 offset:59392\n" \
  "s_mov_b32 s42, 36864\n" \
  "v_mfma_f32_16x16x32_bf16 v[12:15], v[76:79], a[24:27], v[12:15]\n" \
  "buffer_load_dwordx4 a[144:147], v240, %[srdH2], s42 offen offset:0 sc1\n" \
  "v_mfma_f32_16x16x32_bf16 v[40:43], v[76:79], a[104:107], v[40:43]\n" \
  "ds_read_b128 v[132:135], v241 offset:60416\n" \
  "v_mfma_f32_16x16x32_bf16 v[16:19], v[80:83], a[24:27], v[16:19]\n" \
  "s_mov_b32 s42, 16384\n" \
  "v_mfma_f32_16x16x32_bf16 v[44:47], v[80:83], a[104:107], v[44:47]\n" \
  "ds_read_b128 v[136:139], v241 offset:61440\n" \
  "s_waitcnt lgkmcnt(3)\n" \
  "v_mfma_f32_16x16x32_bf16 v[20:23], v[84:87], a[24:27], v[20:23]\n" \
  "buffer_load_dwordx4 a[68:71], v240, %[srdH2], s42 offen offset:1024 sc1\n" \
  "v_mfma_f32_16x16x32_bf16 v[48:51], v[84:87], a[104:107], v[48:51]\n" \
  "ds_read_b128 v[140:143], v241 offset:62464\n" \
  "v_mfma_f32_16x16x32_bf16 v[24:27], v[88:91], a[24:27], v[24:27]\n" \
  "s_mov_b32 s42, 36864\n" \
  "v_mfma_f32_16x16x32_bf16 v[52:55], v[88:91], a[104:107], v[52:55]\n" \
  "ds_read_b128 v[144:147], v241 offset:63488\n" \
  "buffer_load_dwordx4 a[148:151], v240, %[srdH2], s42 offen offset:1024 sc1\n" \
  "s_waitcnt vmcnt(21)\n" \
  "v_mfma_f32_16x16x32_bf16 v[0:3], v[92:95], a[28:31], v[0:3]\n" \
  "s_waitcnt vmcnt(20)\n" \
  "v_mfma_f32_16x16x32_bf16 v[28:31], v[92:95], a[108:111], v[28:31]\n" \
  "ds_read_b128 v[64:67], v241 offset:64512\n" \
  "s_mov_b32 s42, 16384\n" \
  "v_mfma_f32_16x16x32_bf16 v[4:7], v[96:99], a[28:31], v[4:7]\n" \
  "v_mfma_f32_16x16x32_bf16 v[32:35], v[96:99], a[108:111], v[32:35]\n" \
  "ds_read_b128 v[68:71], v242 offset:0\n" \
  "buffer_load_dwordx4 a[72:75], v240, %[srdH2], s42 offen offset:2048 sc1\n" \
  "v_mfma_f32_16x16x32_bf16 v[8:11], v[100:103], a[28:31], v[8:11]\n" \
  "v_mfma_f32_16x16x32_bf16 v[36:39], v[100:103], a[108:111], v[36:39]\n" \
  "ds_read_b128 v[72:75], v242 offset:1024\n" \
  "s_mov_b32 s42, 36864\n" \
  "v_mfma_f32_16x16x32_bf16 v[12:15], v[104:107], a[28:31], v[12:15]\n" \
  "buffer_load_dwordx4 a[152:155], v240, %[srdH2], s42 offen offset:2048 sc1\n" \
  "v_mfma_f32_16x16x32_bf16 v[40:43], v[104:107], a[108:111], v[40:43]\n" \
  "ds_read_b128 v[76:79], v242 offset:2048\n" \
  "v_mfma_f32_16x16x32_bf16 v[16:19], v[108:111], a[28:31], v[16:19]\n" \
  "s_mov_b32 s42, 16384\n" \
  "v_mfma_f32_16x16x32_bf16 v[44:47], v[108:111], a[108:111], v[44:47]\n" \
  "ds_read_b128 v[80:83], v242 offset:3072\n" \
  "v_mfma_f32_16x16x32_bf16 v[20:23], v[112:115], a[28:31], v[20:23]\n" \
  "buffer_load_dwordx4 a[76:79], v240, %[srdH2], s42 offen offset:3072 sc1\n" \
  "v_mfma_f32_16x16x32_bf16 v[48:51], v[112:115], a[108:111], v[48:51]\n" \
  "ds_read_b128 v[84:87], v242 offset:4096\n" \
  "v_mfma_f32_16x16x32_bf16 v[24:27], v[116:119], a[28:31], v[24:27]\n" \
  "s_mov_b32 s42, 36864\n" \
  "v_mfma_f32_16x16x32_bf16 v[52:55], v[116:119], a[108:111], v[52:55]\n" \
  "ds_read_b128 v[88:91], v242 offset:5120\n" \
  "buffer_load_dwordx4 a[156:159], v240, %[srdH2], s42 offen offset:3072 sc1\n" \
  "s_waitcnt vmcnt(23)\n" \
  "v_mfma_f32_16x16x32_bf16 v[0:3], v[120:123], a[32:35], v[0:3]\n" \
  "s_waitcnt vmcnt(22)\n" \
  "v_mfma_f32_16x16x32_bf16 v[28:31], v[120:123], a[112:115], v[28:31]\n" \
  "ds_read_b128 v[92:95], v242 offset:6144\n" \
  "v_mfma_f32_16x16x32_bf16 v[4:7], v[124:127], a[32:35], v[4:7]\n" \
  "v_mfma_f32_16x16x32_bf16 v[32:35], v[124:127], a[112:115], v[32:35]\n" \
  "ds_read_b128 v[96:99], v242 offset:7168\n" \
  "s_waitcnt lgkmcnt(3)\n" \
  "v_mfma_f32_16x16x32_bf16 v[8:11], v[128:131], a[32:35], v[8:11]\n" \
  "v_mfma_f32_16x16x32_bf16 v[36:39], v[128:131], a[112:115], v[36:39]\n" \
  "ds_read_b128 v[100:103], v242 offset:8192\n" \
  "v_mfma_f32_16x16x32_bf16 v[12:15], v[132:135], a[32:35], v[12:15]\n" \
  "v_mfma_f32_16x16x32_bf16 v[40:43], v[132:135], a[112:115], v[40:43]\n" \
  "ds_read_b128 v[104:107], v242 offset:9216\n" \
  "v_mfma_f32_16x16x32_bf16 v[16:19], v[136:139], a[32:35], v[16:19]\n" \
  "v_mfma_f32_16x16x32_bf16 v[44:47], v[136:139], a[112:115], v[44:47]\n" \
  "ds_read_b128 v[108:111], v242 offset:10240\n" \
  "v_mfma_f32_16x16x32_bf16 v[20:23], v[140:143], a[32:35], v[20:23]\n" \
  "v_mfma_f32_16x16x32_bf16 v[48:51], v[140:143], a[112:115], v[48:51]\n" \
  "ds_read_b128 v[112:115], v242 offset:11264\n" \
  "v_mfma_f32_16x16x32_bf16 v[24:27], v[144:147], a[32:35], v[24:27]\n" \
  "v_mfma_f32_16x16x32_bf16 v[52:55], v[144:147], a[112:115], v[52:55]\n" \
  "ds_read_b128 v[116:119], v242 offset:12288\n" \
  "s_waitcnt vmcnt(21)\n" \
  "v_mfma_f32_16x16x32_bf16 v[0:3], v[64:67], a[36:39], v[0:3]\n" \
  "s_waitcnt vmcnt(20)\n" \
  "v_mfma_f32_16x16x32_bf16 v[28:31], v[64:67], a[116:119], v[28:31]\n" \
  "ds_read_b128 v[120:123], v242 offset:13312\n" \
  "v_mfma_f32_16x16x32_bf16 v[4:7], v[68:71], a[36:39], v[4:7]\n" \
  "v_mfma_f32_16x16x32_bf16 v[32:35], v[68:71], a[116:119], v[32:35]\n" \
  "ds_read_b128 v[124:127], v242 offset:14336\n" \
  "v_mfma_f32_16x16x32_bf16 v[8:11], v[72:75], a[36:39], v[8:11]\n" \
  "v_mfma_f32_16x16x32_bf16 v[36:39], v[72:75], a[116:119], v[36:39]\n" \
  "ds_read_b128 v[128:131], v242 offset:15360\n" \
  "v_mfma_f32_16x16x32_bf16 v[12:15], v[76:79], a[36:39], v[12:15]\n" \
  "v_mfma_f32_16x16x32_bf16 v[40:43], v[76:79], a[116:119], v[40:43]\n" \
  "ds_read_b128 v[132:135], v242 offset:16384\n" \
  "v_mfma_f32_16x16x32_bf16 v[16:19], v[80:83], a[36:39], v[16:19]\n" \
  "v_mfma_f32_16x16x32_bf16 v[44:47], v[80:83], a[116:119], v[44:47]\n" \
  "ds_read_b128 v[136:139], v242 offset:17408\n" \
  "v_mfma_f32_16x16x32_bf16 v[20:23], v[84:87], a[36:39], v[20:23]\n" \
  "v_mfma_f32_16x16x32_bf16 v[48:51], v[84:87], a[116:119], v[48:51]\n" \
  "ds_read_b128 v[140:143], v242 offset:18432\n" \
  "s_waitcnt lgkmcnt(3)\n" \
  "v_mfma_f32_16x16x32_bf16 v[24:27], v[88:91], a[36:39], v[24:27]\n" \
  "v_mfma_f32_16x16x32_bf16 v[52:55], v[88:91], a[116:119], v[52:55]\n" \
  "ds_read_b128 v[144:147], v242 offset:19456\n" \
  "s_waitcnt vmcnt(19)\n" \
  "v_mfma_f32_16x16x32_bf16 v[0:3], v[92:95], a[40:43], v[0:3]\n" \
  "s_waitcnt vmcnt(18)\n" \
  "v_mfma_f32_16x16x32_bf16 v[28:31], v[92:95], a[120:123], v[28:31]\n" \
  "ds_read_b128 v[64:67], v242 offset:20480\n" \
  "v_mfma_f32_16x16x32_bf16 v[4:7], v[96:99], a[40:43], v[4:7]\n" \
  "v_mfma_f32_16x16x32_bf16 v[32:35], v[96:99], a[120:123], v[32:35]\n" \
  "ds_read_b128 v[68:71], v242 offset:21504\n" \
  "v_mfma_f32_16x16x32_bf16 v[8:11], v[100:103], a[40:43], v[8:11]\n" \
  "v_mfma_f32_16x16x32_bf16 v[36:39], v[100:103], a[120:123], v[36:39]\n" \
  "ds_read_b128 v[72:75], v242 offset:22528\n" \
  "v_mfma_f32_16x16x32_bf16 v[12:15], v[104:107], a[40:43], v[12:15]\n" \
  "v_mfma_f32_16x16x32_bf16 v[40:43], v[104:107], a[120:123], v[40:43]\n" \
  "ds_read_b128 v[76:79], v242 offset:23552\n" \
  "v_mfma_f32_16x16x32_bf16 v[16:19], v[108:111], a[40:43], v[16:19]\n" \
  "v_mfma_f32_16x16x32_bf16 v[44:47], v[108:111], a[120:123], v[44:47]\n" \
  "ds_read_b128 v[80:83], v242 offset:24576\n" \
  "v_mfma_f32_16x16x32_bf16 v[20:23], v[112:115], a[40:43], v[20:23]\n" \
  "v_mfma_f32_16x16x32_bf16 v[48:51], v[112:115], a[120:123], v[48:51]\n" \
  "ds_read_b128 v[84:87], v242 offset:25600\n" \
  "v_mfma_f32_16x16x32_bf16 v[24:27], v[116:119], a[40:43], v[24:27]\n" \
  "v_mfma_f32_16x16x32_bf16 v[52:55], v[116:119], a[120:123], v[52:55]\n" \
  "ds_read_b128 v[88:91], v242 offset:26624\n" \
  "s_waitcnt vmcnt(17)\n" \
  "v_mfma_f32_16x16x32_bf16 v[0:3], v[120:123], a[44:47], v[0:3]\n" \
  "s_waitcnt vmcnt(16)\n" \
  "v_mfma_f32_16x16x32_bf16 v[28:31], v[120:123], a[124:127], v[28:31]\n" \
  "ds_read_b128 v[92:95], v242 offset:27648\n" \
  "v_mfma_f32_16x16x32_bf16 v[4:7], v[124:127], a[44:47], v[4:7]\n" \
  "v_mfma_f32_16x16x32_bf16 v[32:35], v[124:127], a[124:127], v[32:35]\n" \
  "ds_read_b128 v[96:99], v242 offset:28672\n" \
  "v_mfma_f32_16x16x32_bf16 v[8:11], v[128:131], a[44:47], v[8:11]\n" \
  "v_mfma_f32_16x16x32_bf16 v[36:39], v[128:131], a[124:127], v[36:39]\n" \
  "ds_read_b128 v[100:103], v242 offset:29696\n" \
  "s_waitcnt lgkmcnt(3)\n" \
  "v_mfma_f32_16x16x32_bf16 v[12:15], v[132:135], a[44:47], v[12:15]\n" \
  "v_mfma_f32_16x16x32_bf16 v[40:43], v[132:135], a[124:127], v[40:43]\n" \
  "ds_read_b128 v[104:107], v242 offset:30720\n" \
  "v_mfma_f32_16x16x32_bf16 v[16:19], v[136:139], a[44:47], v[16:19]\n" \
  "v_mfma_f32_16x16x32_bf16 v[44:47], v[136:139], a[124:127], v[44:47]\n" \
  "ds_read_b128 v[108:111], v242 offset:31744\n" \
  "v_mfma_f32_16x16x32_bf16 v[20:23], v[140:143], a[44:47], v[20:23]\n" \
  "v_mfma_f32_16x16x32_bf16 v[48:51], v[140:143], a[124:127], v[48:51]\n" \
  "ds_read_b128 v[112:115], v242 offset:32768\n" \
  "v_mfma_f32_16x16x32_bf16 v[24:27], v[144:147], a[44:47], v[24:27]\n" \
  "v_mfma_f32_16x16x32_bf16 v[52:55], v[144:147], a[124:127], v[52:55]\n" \
  "ds_read_b128 v[116:119], v242 offset:33792\n" \
  "s_waitcnt vmcnt(15)\n" \
  "v_mfma_f32_16x16x32_bf16 v[0:3], v[64:67], a[48:51], v[0:3]\n" \
  "s_waitcnt vmcnt(14)\n" \
  "v_mfma_f32_16x16x32_bf16 v[28:31], v[64:67], a[128:131], v[28:31]\n" \
  "ds_read_b128 v[120:123], v242 offset:34816\n" \
  "v_mfma_f32_16x16x32_bf16 v[4:7], v[68:71], a[48:51], v[4:7]\n" \
  "v_mfma_f32_16x16x32_bf16 v[32:35], v[68:71], a[128:131], v[32:35]\n" \
  "ds_read_b128 v[124:127], v242 offset:35840\n" \
  "v_mfma_f32_16x16x32_bf16 v[8:11], v[72:75], a[48:51], v[8:11]\n" \
  "v_mfma_f32_16x16x32_bf16 v[36:39], v[72:75], a[128:131], v[36:39]\n" \
  "ds_read_b128 v[128:131], v242 offset:36864\n" \
  "v_mfma_f32_16x16x32_bf16 v[12:15], v[76:79], a[48:51], v[12:15]\n" \
  "v_mfma_f32_16x16x32_bf16 v[40:43], v[76:79], a[128:131], v[40:43]\n" \
  "ds_read_b128 v[132:135], v242 offset:37888\n" \
  "v_mfma_f32_16x16x32_bf16 v[16:19], v[80:83], a[48:51], v[16:19]\n" \
  "v_mfma_f32_16x16x32_bf16 v[44:47], v[80:83], a[128:131], v[44:47]\n" \
  "ds_read_b128 v[136:139], v242 offset:38912\n" \
  "v_mfma_f32_16x16x32_bf16 v[20:23], v[84:87], a[48:51], v[20:23]\n" \
  "v_mfma_f32_16x16x32_bf16 v[48:51], v[84:87], a[128:131], v[48:51]\n" \
  "ds_read_b128 v[140:143], v242 offset:39936\n" \
  "v_mfma_f32_16x16x32_bf16 v[24:27], v[88:91], a[48:51], v[24:27]\n" \
  "v_mfma_f32_16x16x32_bf16 v[52:55], v[88:91], a[128:131], v[52:55]\n" \
  "ds_read_b128 v[144:147], v242 offset:40960\n" \
  "s_waitcnt vmcnt(13) lgkmcnt(3)\n" \
  "v_mfma_f32_16x16x32_bf16 v[0:3], v[92:95], a[52:55], v[0:3]\n" \
  "s_waitcnt vmcnt(12)\n" \
  "v_mfma_f32_16x16x32_bf16 v[28:31], v[92:95], a[132:135], v[28:31]\n" \
  "ds_read_b128 v[64:67], v242 offset:41984\n" \
  "v_mfma_f32_16x16x32_bf16 v[4:7], v[96:99], a[52:55], v[4:7]\n" \
  "v_mfma_f32_16x16x32_bf16 v[32:35], v[96:99], a[132:135], v[32:35]\n" \
  "ds_read_b128 v[68:71], v242 offset:43008\n" \
  "v_mfma_f32_16x16x32_bf16 v[8:11], v[100:103], a[52:55], v[8:11]\n" \
  "v_mfma_f32_16x16x32_bf16 v[36:39], v[100:103], a[132:135], v[36:39]\n" \
  "ds_read_b128 v[72:75], v242 offset:44032\n" \
  "v_mfma_f32_16x16x32_bf16 v[12:15], v[104:107], a[52:55], v[12:15]\n" \
  "v_mfma_f32_16x16x32_bf16 v[40:43], v[104:107], a[132:135], v[40:43]\n" \
  "ds_read_b128 v[76:79], v242 offset:45056\n" \
  "v_mfma_f32_16x16x32_bf16 v[16:19], v[108:111], a[52:55], v[16:19]\n" \
  "v_mfma_f32_16x16x32_bf16 v[44:47], v[108:111], a[132:135], v[44:47]\n" \
  "ds_read_b128 v[80:83], v242 offset:46080\n" \
  "v_mfma_f32_16x16x32_bf16 v[20:23], v[112:115], a[52:55], v[20:23]\n" \
  "v_mfma_f32_16x16x32_bf16 v[48:51], v[112:115], a[132:135], v[48:51]\n" \
  "ds_read_b128 v[84:87], v242 offset:47104\n" \
  "v_mfma_f32_16x16x32_bf16 v[24:27], v[116:119], a[52:55], v[24:27]\n" \
  "v_mfma_f32_16x16x32_bf16 v[52:55], v[116:119], a[132:135], v[52:55]\n" \
  "ds_read_b128 v[88:91], v242 offset:48128\n" \
  "s_waitcnt vmcnt(11)\n" \
  "v_mfma_f32_16x16x32_bf16 v[0:3], v[120:123], a[56:59], v[0:3]\n" \
  "s_waitcnt vmcnt(10)\n" \
  "v_mfma_f32_16x16x32_bf16 v[28:31], v[120:123], a[136:139], v[28:31]\n" \
  "ds_read_b128 v[92:95], v242 offset:49152\n" \
  "v_mfma_f32_16x16x32_bf16 v[4:7], v[124:127], a[56:59], v[4:7]\n" \
  "v_mfma_f32_16x16x32_bf16 v[32:35], v[124:127], a[136:139], v[32:35]\n" \
  "ds_read_b128 v[96:99], v242 offset:50176\n" \
  "v_mfma_f32_16x16x32_bf16 v[8:11], v[128:131], a[56:59], v[8:11]\n" \
  "v_mfma_f32_16x16x32_bf16 v[36:39], v[128:131], a[136:139], v[36:39]\n" \
  "ds_read_b128 v[100:103], v242 offset:51200\n" \
  "v_mfma_f32_16x16x32_bf16 v[12:15], v[132:135], a[56:59], v[12:15]\n" \
  "v_mfma_f32_16x16x32_bf16 v[40:43], v[132:135], a[136:139], v[40:43]\n" \
  "ds_read_b128 v[104:107], v242 offset:52224\n" \
  "s_waitcnt lgkmcnt(3)\n" \
  "v_mfma_f32_16x16x32_bf16 v[16:19], v[136:139], a[56:59], v[16:19]\n" \
  "v_mfma_f32_16x16x32_bf16 v[44:47], v[136:139], a[136:139], v[44:47]\n" \
  "ds_read_b128 v[108:111], v242 offset:53248\n" \
  "v_mfma_f32_16x16x32_bf16 v[20:23], v[140:143], a[56:59], v[20:23]\n" \
  "v_mfma_f32_16x16x32_bf16 v[48:51], v[140:143], a[136:139], v[48:51]\n" \
  "ds_read_b128 v[112:115], v242 offset:54272\n" \
  "v_mfma_f32_16x16x32_bf16 v[24:27], v[144:147], a[56:59], v[24:27]\n" \
  "v_mfma_f32_16x16x32_bf16 v[52:55], v[144:147], a[136:139], v[52:55]\n" \
  "ds_read_b128 v[116:119], v242 offset:55296\n" \
  "s_waitcnt vmcnt(9)\n" \
  "v_mfma_f32_16x16x32_bf16 v[0:3], v[64:67], a[60:63], v[0:3]\n" \
  "s_waitcnt vmcnt(8)\n" \
  "v_mfma_f32_16x16x32_bf16 v[28:31], v[64:67], a[140:143], v[28:31]\n" \
  "ds_read_b128 v[120:123], v242 offset:56320\n" \
  "v_mfma_f32_16x16x32_bf16 v[4:7], v[68:71], a[60:63], v[4:7]\n" \
  "v_mfma_f32_16x16x32_bf16 v[32:35], v[68:71], a[140:143], v[32:35]\n" \
  "ds_read_b128 v[124:127], v242 offset:57344\n" \
  "v_mfma_f32_16x16x32_bf16 v[8:11], v[72:75], a[60:63], v[8:11]\n" \
  "v_mfma_f32_16x16x32_bf16 v[36:39], v[72:75], a[140:143], v[36:39]\n" \
  "ds_read_b128 v[128:131], v242 offset:58368\n" \
  "v_mfma_f32_16x16x32_bf16 v[12:15], v[76:79], a[60:63], v[12:15]\n" \
  "v_mfma_f32_16x16x32_bf16 v[40:43], v[76:79], a[140:143], v[40:43]\n" \
  "ds_read_b128 v[132:135], v242 offset:59392\n" \
  "v_mfma_f32_16x16x32_bf16 v[16:19], v[80:83], a[60:63], v[16:19]\n" \
  "v_mfma_f32_16x16x32_bf16 v[44:47], v[80:83], a[140:143], v[44:47]\n" \
  "ds_read_b128 v[136:139], v242 offset:60416\n" \
  "v_mfma_f32_16x16x32_bf16 v[20:23], v[84:87], a[60:63], v[20:23]\n" \
  "v_mfma_f32_16x16x32_bf16 v[48:51], v[84:87], a[140:143], v[48:51]\n" \
  "ds_read_b128 v[140:143], v242 offset:61440\n" \
  "v_mfma_f32_16x16x32_bf16 v[24:27], v[88:91], a[60:63], v[24:27]\n" \
  "v_mfma_f32_16x16x32_bf16 v[52:55], v[88:91], a[140:143], v[52:55]\n" \
  "ds_read_b128 v[144:147], v242 offset:62464\n" \
  "s_waitcnt vmcnt(7)\n" \
  "v_mfma_f32_16x16x32_bf16 v[0:3], v[92:95], a[64:67], v[0:3]\n" \
  "s_waitcnt vmcnt(6)\n" \
  "v_mfma_f32_16x16x32_bf16 v[28:31], v[92:95], a[144:147], v[28:31]\n" \
  "ds_read_b128 v[64:67], v242 offset:63488\n" \
  "s_waitcnt lgkmcnt(3)\n" \
  "v_mfma_f32_16x16x32_bf16 v[4:7], v[96:99], a[64:67], v[4:7]\n" \
  "v_mfma_f32_16x16x32_bf16 v[32:35], v[96:99], a[144:147], v[32:35]\n" \
  "ds_read_b128 v[68:71], v242 offset:64512\n" \
  "v_mfma_f32_16x16x32_bf16 v[8:11], v[100:103], a[64:67], v[8:11]\n" \
  "v_mfma_f32_16x16x32_bf16 v[36:39], v[100:103], a[144:147], v[36:39]\n" \
  "ds_read_b128 v[72:75], v243 offset:0\n" \
  "v_mfma_f32_16x16x32_bf16 v[12:15], v[104:107], a[64:67], v[12:15]\n" \
  "v_mfma_f32_16x16x32_bf16 v[40:43], v[104:107], a[144:147], v[40:43]\n" \
  "ds_read_b128 v[76:79], v243 offset:1024\n" \
  "v_mfma_f32_16x16x32_bf16 v[16:19], v[108:111], a[64:67], v[16:19]\n" \
  "v_mfma_f32_16x16x32_bf16 v[44:47], v[108:111], a[144:147], v[44:47]\n" \
  "ds_read_b128 v[80:83], v243 offset:2048\n" \
  "v_mfma_f32_16x16x32_bf16 v[20:23], v[112:115], a[64:67], v[20:23]\n" \
  "v_mfma_f32_16x16x32_bf16 v[48:51], v[112:115], a[144:147], v[48:51]\n" \
  "ds_read_b128 v[84:87], v243 offset:3072\n" \
  "v_mfma_f32_16x16x32_bf16 v[24:27], v[116:119], a[64:67], v[24:27]\n" \
  "v_mfma_f32_16x16x32_bf16 v[52:55], v[116:119], a[144:147], v[52:55]\n" \
  "ds_read_b128 v[88:91], v243 offset:4096\n" \
  "s_waitcnt vmcnt(5)\n" \
  "v_mfma_f32_16x16x32_bf16 v[0:3], v[120:123], a[68:71], v[0:3]\n" \
  "s_waitcnt vmcnt(4)\n" \
  "v_mfma_f32_16x16x32_bf16 v[28:31], v[120:123], a[148:151], v[28:31]\n" \
  "ds_read_b128 v[92:95], v243 offset:5120\n" \
  "v_mfma_f32_16x16x32_bf16 v[4:7], v[124:127], a[68:71], v[4:7]\n" \
  "v_mfma_f32_16x16x32_bf16 v[32:35], v[124:127], a[148:151], v[32:35]\n" \
  "ds_read_b128 v[96:99], v243 offset:6144\n" \
  "v_mfma_f32_16x16x32_bf16 v[8:11], v[128:131], a[68:71], v[8:11]\n" \
  "v_mfma_f32_16x16x32_bf16 v[36:39], v[128:131], a[148:151], v[36:39]\n" \
  "ds_read_b128 v[100:103], v243 offset:7168\n" \
  "v_mfma_f32_16x16x32_bf16 v[12:15], v[132:135], a[68:71], v[12:15]\n" \
  "v_mfma_f32_16x16x32_bf16 v[40:43], v[132:135], a[148:151], v[40:43]\n" \
  "ds_read_b128 v[104:107], v243 offset:8192\n" \
  "v_mfma_f32_16x16x32_bf16 v[16:19], v[136:139], a[68:71], v[16:19]\n" \
  "v_mfma_f32_16x16x32_bf16 v[44:47], v[136:139], a[148:151], v[44:47]\n" \
  "ds_read_b128 v[108:111], v243 offset:9216\n" \
  "s_waitcnt lgkmcnt(3)\n" \
  "v_mfma_f32_16x16x32_bf16 v[20:23], v[140:143], a[68:71], v[20:23]\n" \
  "v_mfma_f32_16x16x32_bf16 v[48:51], v[140:143], a[148:151], v[48:51]\n" \
  "ds_read_b128 v[112:115], v243 offset:10240\n" \
  "v_mfma_f32_16x16x32_bf16 v[24:27], v[144:147], a[68:71], v[24:27]\n" \
  "v_mfma_f32_16x16x32_bf16 v[52:55], v[144:147], a[148:151], v[52:55]\n" \
  "ds_read_b128 v[116:119], v243 offset:11264\n" \
  "s_waitcnt vmcnt(3)\n" \
  "v_mfma_f32_16x16x32_bf16 v[0:3], v[64:67], a[72:75], v[0:3]\n" \
  "s_waitcnt vmcnt(2)\n" \
  "v_mfma_f32_16x16x32_bf16 v[28:31], v[64:67], a[152:155], v[28:31]\n" \
  "v_mfma_f32_16x16x32_bf16 v[4:7], v[68:71], a[72:75], v[4:7]\n" \
  "v_mfma_f32_16x16x32_bf16 v[32:35], v[68:71], a[152:155], v[32:35]\n" \
  "v_mfma_f32_16x16x32_bf16 v[8:11], v[72:75], a[72:75], v[8:11]\n" \
  "v_mfma_f32_16x16x32_bf16 v[36:39], v[72:75], a[152:155], v[36:39]\n" \
  "v_mfma_f32_16x16x32_bf16 v[12:15], v[76:79], a[72:75], v[12:15]\n" \
  "v_mfma_f32_16x16x32_bf16 v[40:43], v[76:79], a[152:155], v[40:43]\n" \
  "v_mfma_f32_16x16x32_bf16 v[16:19], v[80:83], a[72:75], v[16:19]\n" \
  "v_mfma_f32_16x16x32_bf16 v[44:47], v[80:83], a[152:155], v[44:47]\n" \
  "v_mfma_f32_16x16x32_bf16 v[20:23], v[84:87], a[72:75], v[20:23]\n" \
  "v_mfma_f32_16x16x32_bf16 v[48:51], v[84:87], a[152:155], v[48:51]\n" \
  "v_mfma_f32_16x16x32_bf16 v[24:27], v[88:91], a[72:75], v[24:27]\n" \
  "v_mfma_f32_16x16x32_bf16 v[52:55], v[88:91], a[152:155], v[52:55]\n" \
  "s_waitcnt vmcnt(1)\n" \
  "v_mfma_f32_16x16x32_bf16 v[0:3], v[92:95], a[76:79], v[0:3]\n" \
  "s_waitcnt vmcnt(0)\n" \
  "v_mfma_f32_16x16x32_bf16 v[28:31], v[92:95], a[156:159], v[28:31]\n" \
  "v_mfma_f32_16x16x32_bf16 v[4:7], v[96:99], a[76:79], v[4:7]\n" \
  "v_mfma_f32_16x16x32_bf16 v[32:35], v[96:99], a[156:159], v[32:35]\n" \
  "s_waitcnt lgkmcnt(0)\n" \
  "v_mfma_f32_16x16x32_bf16 v[8:11], v[100:103], a[76:79], v[8:11]\n" \
  "v_mfma_f32_16x16x32_bf16 v[36:39], v[100:103], a[156:159], v[36:39]\n" \
  "v_mfma_f32_16x16x32_bf16 v[12:15], v[104:107], a[76:79], v[12:15]\n" \
  "v_mfma_f32_16x16x32_bf16 v[40:43], v[104:107], a[156:159], v[40:43]\n" \
  "v_mfma_f32_16x16x32_bf16 v[16:19], v[108:111], a[76:79], v[16:19]\n" \
  "v_mfma_f32_16x16x32_bf16 v[44:47], v[108:111], a[156:159], v[44:47]\n" \
  "v_mfma_f32_16x16x32_bf16 v[20:23], v[112:115], a[76:79], v[20:23]\n" \
  "v_mfma_f32_16x16x32_bf16 v[48:51], v[112:115], a[156:159], v[48:51]\n" \
  "v_mfma_f32_16x16x32_bf16 v[24:27], v[116:119], a[76:79], v[24:27]\n" \
  "v_mfma_f32_16x16x32_bf16 v[52:55], v[116:119], a[156:159], v[52:55]\n" \
  "v_lshlrev_b32 v64, 16, v148\n" \
  "v_and_b32 v65, 0xffff0000, v148\n" \
  "v_lshlrev_b32 v66, 16, v149\n" \
  "v_and_b32 v67, 0xffff0000, v149\n" \
  "v_lshlrev_b32 v68, 16, v150\n" \
  "v_and_b32 v69, 0xffff0000, v150\n" \
  "v_lshlrev_b32 v70, 16, v151\n" \
  "v_and_b32 v71, 0xffff0000, v151\n" \
  "v_lshlrev_b32 v72, 16, v152\n" \
  "v_and_b32 v73, 0xffff0000, v152\n" \
  "v_lshlrev_b32 v74, 16, v153\n" \
  "v_and_b32 v75, 0xffff0000, v153\n" \
  "v_lshlrev_b32 v76, 16, v154\n" \
  "v_and_b32 v77, 0xffff0000, v154\n" \
  "v_lshlrev_b32 v78, 16, v155\n" \
  "v_and_b32 v79, 0xffff0000, v155\n" \
  "v_lshlrev_b32 v80, 16, v156\n" \
  "v_and_b32 v81, 0xffff0000, v156\n" \
  "v_lshlrev_b32 v82, 16, v157\n" \
  "v_and_b32 v83, 0xffff0000, v157\n" \
  "v_lshlrev_b32 v84, 16, v158\n" \
  "v_and_b32 v85, 0xffff0000, v158\n" \
  "v_lshlrev_b32 v86, 16, v159\n" \
  "v_and_b32 v87, 0xffff0000, v159\n" \
  "v_lshlrev_b32 v88, 16, v160\n" \
  "v_and_b32 v89, 0xffff0000, v160\n" \
  "v_lshlrev_b32 v90, 16, v161\n" \
  "v_and_b32 v91, 0xffff0000, v161\n" \
  "s_cmp_eq_u32 %[par], 0\n" \
  "s_cbranch_scc0 .Lt3_odd_%=\n" \
  "v_pk_add_f32 v[0:1], v[0:1], v[64:65]\n" \
  "v_pk_add_f32 v[2:3], v[2:3], v[66:67]\n" \
  "v_max_f32 v0, 0, v0\n" \
  "v_max_f32 v1, 0, v1\n" \
  "v_max_f32 v2, 0, v2\n" \
  "v_max_f32 v3, 0, v3\n" \
  "v_pk_mul_f32 v[0:1], v[0:1], v[0:1]\n" \
  "v_pk_mul_f32 v[2:3], v[2:3], v[2:3]\n" \
  "v_cvt_pk_bf16_f32 v96, v0, v1\n" \
  "v_cvt_pk_bf16_f32 v97, v2, v3\n" \
  "v_pk_add_f32 v[4:5], v[4:5], v[68:69]\n" \
  "v_pk_add_f32 v[6:7], v[6:7], v[70:71]\n" \
  "v_max_f32 v4, 0, v4\n" \
  "v_max_f32 v5, 0, v5\n" \
  "v_max_f32 v6, 0, v6\n" \
  "v_max_f32 v7, 0, v7\n" \
  "v_pk_mul_f32 v[4:5], v[4:5], v[4:5]\n" \
  "v_pk_mul_f32 v[6:7], v[6:7], v[6:7]\n" \
  "v_cvt_pk_bf16_f32 v98, v4, v5\n" \
  "v_cvt_pk_bf16_f32 v99, v6, v7\n" \
  "v_pk_add_f32 v[8:9], v[8:9], v[72:73]\n" \
  "v_pk_add_f32 v[10:11], v[10:11], v[74:75]\n" \
  "v_max_f32 v8, 0, v8\n" \
  "v_max_f32 v9, 0, v9\n" \
  "v_max_f32 v10, 0, v10\n" \
  "v_max_f32 v11, 0, v11\n" \
  "v_pk_mul_f32 v[8:9], v[8:9], v[8:9]\n" \
  "v_pk_mul_f32 v[10:11], v[10:11], v[10:11]\n" \
  "v_cvt_pk_bf16_f32 v100, v8, v9\n" \
  "v_cvt_pk_bf16_f32 v101, v10, v11\n" \
  "v_pk_add_f32 v[12:13], v[12:13], v[76:77]\n" \
  "v_pk_add_f32 v[14:15], v[14:15], v[78:79]\n" \
  "v_max_f32 v12, 0, v12\n" \
  "v_max_f32 v13, 0, v13\n" \
  "v_max_f32 v14, 0, v14\n" \
  "v_max_f32 v15, 0, v15\n" \
  "v_pk_mul_f32 v[12:13], v[12:13], v[12:13]\n" \
  "v_pk_mul_f32 v[14:15], v[14:15], v[14:15]\n" \
  "v_cvt_pk_bf16_f32 v102, v12, v13\n" \
  "v_cvt_pk_bf16_f32 v103, v14, v15\n" \
  "v_pk_add_f32 v[16:17], v[16:17], v[80:81]\n" \
  "v_pk_add_f32 v[18:19], v[18:19], v[82:83]\n" \
  "v_max_f32 v16, 0, v16\n" \
  "v_max_f32 v17, 0, v17\n" \
  "v_max_f32 v18, 0, v18\n" \
  "v_max_f32 v19, 0, v19\n" \
  "v_pk_mul_f32 v[16:17], v[16:17], v[16:17]\n" \
  "v_pk_mul_f32 v[18:19], v[18:19], v[18:19]\n" \
  "v_cvt_pk_bf16_f32 v104, v16, v17\n" \
  "v_cvt_pk_bf16_f32 v105, v18, v19\n" \
  "v_pk_add_f32 v[20:21], v[20:21], v[84:85]\n" \
  "v_pk_add_f32 v[22:23], v[22:23], v[86:87]\n" \
  "v_max_f32 v20, 0, v20\n" \
  "v_max_f32 v21, 0, v21\n" \
  "v_max_f32 v22, 0, v22\n" \
  "v_max_f32 v23, 0, v23\n" \
  "v_pk_mul_f32 v[20:21], v[20:21], v[20:21]\n" \
  "v_pk_mul_f32 v[22:23], v[22:23], v[22:23]\n" \
  "v_cvt_pk_bf16_f32 v106, v20, v21\n" \
  "v_cvt_pk_bf16_f32 v107, v22, v23\n" \
  "v_pk_add_f32 v[24:25], v[24:25], v[88:89]\n" \
  "v_pk_add_f32 v[26:27], v[26:27], v[90:91]\n" \
  "v_max_f32 v24, 0, v24\n" \
  "v_max_f32 v25, 0, v25\n" \
  "v_max_f32 v26, 0, v26\n" \
  "v_max_f32 v27, 0, v27\n" \
  "v_pk_mul_f32 v[24:25], v[24:25], v[24:25]\n" \
  "v_pk_mul_f32 v[26:27], v[26:27], v[26:27]\n" \
  "v_cvt_pk_bf16_f32 v108, v24, v25\n" \
  "v_cvt_pk_bf16_f32 v109, v26, v27\n" \
  "s_mov_b32 s40, 0\n" \
  "buffer_store_dwordx4 v[96:99], v240, %[srdA1o], s40 offen offset:0 sc1\n" \
  "buffer_store_dwordx4 v[100:103], v240, %[srdA1o], s40 offen offset:1024 sc1\n" \
  "buffer_store_dwordx4 v[104:107], v240, %[srdA1o], s40 offen offset:2048 sc1\n" \
  "buffer_store_dwordx2 v[108:109], v240, %[srdA1o], s40 offen offset:3072 sc1\n" \
  "v_pk_add_f32 v[28:29], v[28:29], v[64:65]\n" \
  "v_pk_add_f32 v[30:31], v[30:31], v[66:67]\n" \
  "v_max_f32 v28, 0, v28\n" \
  "v_max_f32 v29, 0, v29\n" \
  "v_max_f32 v30, 0, v30\n" \
  "v_max_f32 v31, 0, v31\n" \
  "v_pk_mul_f32 v[28:29], v[28:29], v[28:29]\n" \
  "v_pk_mul_f32 v[30:31], v[30:31], v[30:31]\n" \
  "v_cvt_pk_bf16_f32 v116, v28, v29\n" \
  "v_cvt_pk_bf16_f32 v117, v30, v31\n" \
  "v_pk_add_f32 v[32:33], v[32:33], v[68:69]\n" \
  "v_pk_add_f32 v[34:35], v[34:35], v[70:71]\n" \
  "v_max_f32 v32, 0, v32\n" \
  "v_max_f32 v33, 0, v33\n" \
  "v_max_f32 v34, 0, v34\n" \
  "v_max_f32 v35, 0, v35\n" \
  "v_pk_mul_f32 v[32:33], v[32:33], v[32:33]\n" \
  "v_pk_mul_f32 v[34:35], v[34:35], v[34:35]\n" \
  "v_cvt_pk_bf16_f32 v118, v32, v33\n" \
  "v_cvt_pk_bf16_f32 v119, v34, v35\n" \
  "v_pk_add_f32 v[36:37], v[36:37], v[72:73]\n" \
  "v_pk_add_f32 v[38:39], v[38:39], v[74:75]\n" \
  "v_max_f32 v36, 0, v36\n" \
  "v_max_f32 v37, 0, v37\n" \
  "v_max_f32 v38, 0, v38\n" \
  "v_max_f32 v39, 0, v39\n" \
  "v_pk_mul_f32 v[36:37], v[36:37], v[36:37]\n" \
  "v_pk_mul_f32 v[38:39], v[38:39], v[38:39]\n" \
  "v_cvt_pk_bf16_f32 v120, v36, v37\n" \
  "v_cvt_pk_bf16_f32 v121, v38, v39\n" \
  "v_pk_add_f32 v[40:41], v[40:41], v[76:77]\n" \
  "v_pk_add_f32 v[42:43], v[42:43], v[78:79]\n" \
  "v_max_f32 v40, 0, v40\n" \
  "v_max_f32 v41, 0, v41\n" \
  "v_max_f32 v42, 0, v42\n" \
  "v_max_f32 v43, 0, v43\n" \
  "v_pk_mul_f32 v[40:41], v[40:41], v[40:41]\n" \
  "v_pk_mul_f32 v[42:43], v[42:43], v[42:43]\n" \
  "v_cvt_pk_bf16_f32 v122, v40, v41\n" \
  "v_cvt_pk_bf16_f32 v123, v42, v43\n" \
  "v_pk_add_f32 v[44:45], v[44:45], v[80:81]\n" \
  "v_pk_add_f32 v[46:47], v[46:47], v[82:83]\n" \
  "v_max_f32 v44, 0, v44\n" \
  "v_max_f32 v45, 0, v45\n" \
  "v_max_f32 v46, 0, v46\n" \
  "v_max_f32 v47, 0, v47\n" \
  "v_pk_mul_f32 v[44:45], v[44:45], v[44:45]\n" \
  "v_pk_mul_f32 v[46:47], v[46:47], v[46:47]\n" \
  "v_cvt_pk_bf16_f32 v124, v44, v45\n" \
  "v_cvt_pk_bf16_f32 v125, v46, v47\n" \
  "v_pk_add_f32 v[48:49], v[48:49], v[84:85]\n" \
  "v_pk_add_f32 v[50:51], v[50:51], v[86:87]\n" \
  "v_max_f32 v48, 0, v48\n" \
  "v_max_f32 v49, 0, v49\n" \
  "v_max_f32 v50, 0, v50\n" \
  "v_max_f32 v51, 0, v51\n" \
  "v_pk_mul_f32 v[48:49], v[48:49], v[48:49]\n" \
  "v_pk_mul_f32 v[50:51], v[50:51], v[50:51]\n" \
  "v_cvt_pk_bf16_f32 v126, v48, v49\n" \
  "v_cvt_pk_bf16_f32 v127, v50, v51\n" \
  "v_pk_add_f32 v[52:53], v[52:53], v[88:89]\n" \
  "v_pk_add_f32 v[54:55], v[54:55], v[90:91]\n" \
  "v_max_f32 v52, 0, v52\n" \
  "v_max_f32 v53, 0, v53\n" \
  "v_max_f32 v54, 0, v54\n" \
  "v_max_f32 v55, 0, v55\n" \
  "v_pk_mul_f32 v[52:53], v[52:53], v[52:53]\n" \
  "v_pk_mul_f32 v[54:55], v[54:55], v[54:55]\n" \
  "v_cvt_pk_bf16_f32 v128, v52, v53\n" \
  "v_cvt_pk_bf16_f32 v129, v54, v55\n" \
  "s_mov_b32 s40, 57344\n" \
  "buffer_store_dwordx4 v[116:119], v240, %[srdA1o], s40 offen offset:0 sc1\n" \
  "buffer_store_dwordx4 v[120:123], v240, %[srdA1o], s40 offen offset:1024 sc1\n" \
  "buffer_store_dwordx4 v[124:127], v240, %[srdA1o], s40 offen offset:2048 sc1\n" \
  "buffer_store_dwordx2 v[128:129], v240, %[srdA1o], s40 offen offset:3072 sc1\n" \
  "s_branch .Lt3_end_%=\n" \
  ".Lt3_odd_%=:\n" \
  "v_pk_add_f32 v[0:1], v[0:1], v[64:65]\n" \
  "v_pk_add_f32 v[2:3], v[2:3], v[66:67]\n" \
  "v_max_f32 v0, 0, v0\n" \
  "v_max_f32 v1, 0, v1\n" \
  "v_max_f32 v2, 0, v2\n" \
  "v_max_f32 v3, 0, v3\n" \
  "v_pk_mul_f32 v[0:1], v[0:1], v[0:1]\n" \
  "v_pk_mul_f32 v[2:3], v[2:3], v[2:3]\n" \
  "v_cvt_pk_bf16_f32 v98, v0, v1\n" \
  "v_cvt_pk_bf16_f32 v99, v2, v3\n" \
  "v_pk_add_f32 v[4:5], v[4:5], v[68:69]\n" \
  "v_pk_add_f32 v[6:7], v[6:7], v[70:71]\n" \
  "v_max_f32 v4, 0, v4\n" \
  "v_max_f32 v5, 0, v5\n" \
  "v_max_f32 v6, 0, v6\n" \
  "v_max_f32 v7, 0, v7\n" \
  "v_pk_mul_f32 v[4:5], v[4:5], v[4:5]\n" \
  "v_pk_mul_f32 v[6:7], v[6:7], v[6:7]\n" \
  "v_cvt_pk_bf16_f32 v100, v4, v5\n" \
  "v_cvt_pk_bf16_f32 v101, v6, v7\n" \
  "v_pk_add_f32 v[8:9], v[8:9], v[72:73]\n" \
  "v_pk_add_f32 v[10:11], v[10:11], v[74:75]\n" \
  "v_max_f32 v8, 0, v8\n" \
  "v_max_f32 v9, 0, v9\n" \
  "v_max_f32 v10, 0, v10\n" \
  "v_max_f32 v11, 0, v11\n" \
  "v_pk_mul_f32 v[8:9], v[8:9], v[8:9]\n" \
  "v_pk_mul_f32 v[10:11], v[10:11], v[10:11]\n" \
  "v_cvt_pk_bf16_f32 v102, v8, v9\n" \
  "v_cvt_pk_bf16_f32 v103, v10, v11\n" \
  "v_pk_add_f32 v[12:13], v[12:13], v[76:77]\n" \
  "v_pk_add_f32 v[14:15], v[14:15], v[78:79]\n" \
  "v_max_f32 v12, 0, v12\n" \
  "v_max_f32 v13, 0, v13\n" \
  "v_max_f32 v14, 0, v14\n" \
  "v_max_f32 v15, 0, v15\n" \
  "v_pk_mul_f32 v[12:13], v[12:13], v[12:13]\n" \
  "v_pk_mul_f32 v[14:15], v[14:15], v[14:15]\n" \
  "v_cvt_pk_bf16_f32 v104, v12, v13\n" \
  "v_cvt_pk_bf16_f32 v105, v14, v15\n" \
  "v_pk_add_f32 v[16:17], v[16:17], v[80:81]\n" \
  "v_pk_add_f32 v[18:19], v[18:19], v[82:83]\n" \
  "v_max_f32 v16, 0, v16\n" \
  "v_max_f32 v17, 0, v17\n" \
  "v_max_f32 v18, 0, v18\n" \
  "v_max_f32 v19, 0, v19\n" \
  "v_pk_mul_f32 v[16:17], v[16:17], v[16:17]\n" \
  "v_pk_mul_f32 v[18:19], v[18:19], v[18:19]\n" \
  "v_cvt_pk_bf16_f32 v106, v16, v17\n" \
  "v_cvt_pk_bf16_f32 v107, v18, v19\n" \
  "v_pk_add_f32 v[20:21], v[20:21], v[84:85]\n" \
  "v_pk_add_f32 v[22:23], v[22:23], v[86:87]\n" \
  "v_max_f32 v20, 0, v20\n" \
  "v_max_f32 v21, 0, v21\n" \
  "v_max_f32 v22, 0, v22\n" \
  "v_max_f32 v23, 0, v23\n" \
  "v_pk_mul_f32 v[20:21], v[20:21], v[20:21]\n" \
  "v_pk_mul_f32 v[22:23], v[22:23], v[22:23]\n" \
  "v_cvt_pk_bf16_f32 v108, v20, v21\n" \
  "v_cvt_pk_bf16_f32 v109, v22, v23\n" \
  "v_pk_add_f32 v[24:25], v[24:25], v[88:89]\n" \
  "v_pk_add_f32 v[26:27], v[26:27], v[90:91]\n" \
  "v_max_f32 v24, 0, v24\n" \
  "v_max_f32 v25, 0, v25\n" \
  "v_max_f32 v26, 0, v26\n" \
  "v_max_f32 v27, 0, v27\n" \
  "v_pk_mul_f32 v[24:25], v[24:25], v[24:25]\n" \
  "v_pk_mul_f32 v[26:27], v[26:27], v[26:27]\n" \
  "v_cvt_pk_bf16_f32 v110, v24, v25\n" \
  "v_cvt_pk_bf16_f32 v111, v26, v27\n" \
  "s_mov_b32 s40, 0\n" \
  "buffer_store_dwordx2 v[98:99], v245, %[srdA1o], s40 offen offset:0 sc1\n" \
  "buffer_store_dwordx4 v[100:103], v240, %[srdA1o], s40 offen offset:1024 sc1\n" \
  "buffer_store_dwordx4 v[104:107], v240, %[srdA1o], s40 offen offset:2048 sc1\n" \
  "buffer_store_dwordx4 v[108:111], v240, %[srdA1o], s40 offen offset:3072 sc1\n" \
  "v_pk_add_f32 v[28:29], v[28:29], v[64:65]\n" \
  "v_pk_add_f32 v[30:31], v[30:31], v[66:67]\n" \
  "v_max_f32 v28, 0, v28\n" \
  "v_max_f32 v29, 0, v29\n" \
  "v_max_f32 v30, 0, v30\n" \
  "v_max_f32 v31, 0, v31\n" \
  "v_pk_mul_f32 v[28:29], v[28:29], v[28:29]\n" \
  "v_pk_mul_f32 v[30:31], v[30:31], v[30:31]\n" \
  "v_cvt_pk_bf16_f32 v118, v28, v29\n" \
  "v_cvt_pk_bf16_f32 v119, v30, v31\n" \
  "v_pk_add_f32 v[32:33], v[32:33], v[68:69]\n" \
  "v_pk_add_f32 v[34:35], v[34:35], v[70:71]\n" \
  "v_max_f32 v32, 0, v32\n" \
  "v_max_f32 v33, 0, v33\n" \
  "v_max_f32 v34, 0, v34\n" \
  "v_max_f32 v35, 0, v35\n" \
  "v_pk_mul_f32 v[32:33], v[32:33], v[32:33]\n" \
  "v_pk_mul_f32 v[34:35], v[34:35], v[34:35]\n" \
  "v_cvt_pk_bf16_f32 v120, v32, v33\n" \
  "v_cvt_pk_bf16_f32 v121, v34, v35\n" \
  "v_pk_add_f32 v[36:37], v[36:37], v[72:73]\n" \
  "v_pk_add_f32 v[38:39], v[38:39], v[74:75]\n" \
  "v_max_f32 v36, 0, v36\n" \
  "v_max_f32 v37, 0, v37\n" \
  "v_max_f32 v38, 0, v38\n" \
  "v_max_f32 v39, 0, v39\n" \
  "v_pk_mul_f32 v[36:37], v[36:37], v[36:37]\n" \
  "v_pk_mul_f32 v[38:39], v[38:39], v[38:39]\n" \
  "v_cvt_pk_bf16_f32 v122, v36, v37\n" \
  "v_cvt_pk_bf16_f32 v123, v38, v39\n" \
  "v_pk_add_f32 v[40:41], v[40:41], v[76:77]\n" \
  "v_pk_add_f32 v[42:43], v[42:43], v[78:79]\n" \
  "v_max_f32 v40, 0, v40\n" \
  "v_max_f32 v41, 0, v41\n" \
  "v_max_f32 v42, 0, v42\n" \
  "v_max_f32 v43, 0, v43\n" \
  "v_pk_mul_f32 v[40:41], v[40:41], v[40:41]\n" \
  "v_pk_mul_f32 v[42:43], v[42:43], v[42:43]\n" \
  "v_cvt_pk_bf16_f32 v124, v40, v41\n" \
  "v_cvt_pk_bf16_f32 v125, v42, v43\n" \
  "v_pk_add_f32 v[44:45], v[44:45], v[80:81]\n" \
  "v_pk_add_f32 v[46:47], v[46:47], v[82:83]\n" \
  "v_max_f32 v44, 0, v44\n" \
  "v_max_f32 v45, 0, v45\n" \
  "v_max_f32 v46, 0, v46\n" \
  "v_max_f32 v47, 0, v47\n" \
  "v_pk_mul_f32 v[44:45], v[44:45], v[44:45]\n" \
  "v_pk_mul_f32 v[46:47], v[46:47], v[46:47]\n" \
  "v_cvt_pk_bf16_f32 v126, v44, v45\n" \
  "v_cvt_pk_bf16_f32 v127, v46, v47\n" \
  "v_pk_add_f32 v[48:49], v[48:49], v[84:85]\n" \
  "v_pk_add_f32 v[50:51], v[50:51], v[86:87]\n" \
  "v_max_f32 v48, 0, v48\n" \
  "v_max_f32 v49, 0, v49\n" \
  "v_max_f32 v50, 0, v50\n" \
  "v_max_f32 v51, 0, v51\n" \
  "v_pk_mul_f32 v[48:49], v[48:49], v[48:49]\n" \
  "v_pk_mul_f32 v[50:51], v[50:51], v[50:51]\n" \
  "v_cvt_pk_bf16_f32 v128, v48, v49\n" \
  "v_cvt_pk_bf16_f32 v129, v50, v51\n" \
  "v_pk_add_f32 v[52:53], v[52:53], v[88:89]\n" \
  "v_pk_add_f32 v[54:55], v[54:55], v[90:91]\n" \
  "v_max_f32 v52, 0, v52\n" \
  "v_max_f32 v53, 0, v53\n" \
  "v_max_f32 v54, 0, v54\n" \
  "v_max_f32 v55, 0, v55\n" \
  "v_pk_mul_f32 v[52:53], v[52:53], v[52:53]\n" \
  "v_pk_mul_f32 v[54:55], v[54:55], v[54:55]\n" \
  "v_cvt_pk_bf16_f32 v130, v52, v53\n" \
  "v_cvt_pk_bf16_f32 v131, v54, v55\n" \
  "s_mov_b32 s40, 57344\n" \
  "buffer_store_dwordx2 v[118:119], v245, %[srdA1o], s40 offen offset:0 sc1\n" \
  "buffer_store_dwordx4 v[120:123], v240, %[srdA1o], s40 offen offset:1024 sc1\n" \
  "buffer_store_dwordx4 v[124:127], v240, %[srdA1o], s40 offen offset:2048 sc1\n" \
  "buffer_store_dwordx4 v[128:131], v240, %[srdA1o], s40 offen offset:3072 sc1\n" \
  ".Lt3_end_%=:\n" \
  "s_waitcnt vmcnt(0)\n" \
  "v_mov_b32 v56, %[tag]\n" \
  "v_mov_b32 v57, 0\n" \
  "s_mov_b64 exec, 1\n" \
  "global_store_dword v57, v56, %[fout] sc1\n" \
  "s_mov_b64 exec, -1\n" \
  "v_accvgpr_read_b32 v92, a200\n" \
  "v_accvgpr_read_b32 v93, a201\n" \
  "v_accvgpr_read_b32 v94, a202\n" \
  "v_accvgpr_read_b32 v95, a203\n" \
  "s_mov_b32 s57, 0\n" \
  ".Lx1_chk_%=:\n" \
  "s_nop 1\n" \
  "v_cmp_ne_u32 vcc, %[tag], v92\n" \
  "s_nop 4\n" \
  "s_mov_b64 s[58:59], vcc\n" \
  "s_nop 1\n" \
  "v_cmp_ne_u32 vcc, %[tag], v93\n" \
  "s_nop 4\n" \
  "s_or_b64 s[58:59], s[58:59], vcc\n" \
  "s_nop 1\n" \
  "v_cmp_ne_u32 vcc, %[tag], v94\n" \
  "s_nop 4\n" \
  "s_or_b64 s[58:59], s[58:59], vcc\n" \
  "s_nop 1\n" \
  "v_cmp_ne_u32 vcc, %[tag], v95\n" \
  "s_nop 4\n" \
  "s_or_b64 s[58:59], s[58:59], vcc\n" \
  "s_cmp_eq_u64 s[58:59], 0\n" \
  "s_cbranch_scc1 .Lx1_done_%=\n" \
  "s_add_u32 s57, s57, 1\n" \
  "s_cmp_gt_u32 s57, 0x10000\n" \
  "s_cbranch_scc1 .Lx1_done_%=\n" \
  "s_sleep 1\n" \
  "global_load_dwordx4 v[92:95], v240, %[fx1] sc1\n" \
  "s_waitcnt vmcnt(0)\n" \
  "s_branch .Lx1_chk_%=\n" \
  ".Lx1_done_%=:\n" \
  "s_barrier\n" \
  "s_add_u32 m0, s61, 0\n" \
  "s_mov_b32 s42, 0\n" \
  "buffer_load_dwordx4 v240, %[srdW2], s42 offen offset:0 sc1 lds\n" \
  "buffer_load_dwordx4 v240, %[srdW2], s42 offen offset:1024 sc1 lds\n" \
  "buffer_load_dwordx4 v240, %[srdW2], s42 offen offset:2048 sc1 lds\n" \
  "buffer_load_dwordx4 v240, %[srdW2], s42 offen offset:3072 sc1 lds\n" \
  "s_add_u32 m0, s61, 4096\n" \
  "s_mov_b32 s42, 4096\n" \
  "buffer_load_dwordx4 v240, %[srdW2], s42 offen offset:0 sc1 lds\n" \
  "buffer_load_dwordx4 v240, %[srdW2], s42 offen offset:1024 sc1 lds\n" \
  "buffer_load_dwordx4 v240, %[srdW2], s42 offen offset:2048 sc1 lds\n" \
  "buffer_load_dwordx4 v240, %[srdW2], s42 offen offset:3072 sc1 lds\n" \
  "s_add_u32 m0, s61, 8192\n" \
  "s_mov_b32 s42, 8192\n" \
  "buffer_load_dwordx4 v240, %[srdW2], s42 offen offset:0 sc1 lds\n" \
  "buffer_load_dwordx4 v240, %[srdW2], s42 offen offset:1024 sc1 lds\n" \
  "buffer_load_dwordx4 v240, %[srdW2], s42 offen offset:2048 sc1 lds\n" \
  "buffer_load_dwordx4 v240, %[srdW2], s42 offen offset:3072 sc1 lds\n" \
  "s_add_u32 m0, s61, 12288\n" \
  "s_mov_b32 s42, 12288\n" \
  "buffer_load_dwordx4 v240, %[srdW2], s42 offen offset:0 sc1 lds\n" \
  "buffer_load_dwordx4 v240, %[srdW2], s42 offen offset:1024 sc1 lds\n" \
  "buffer_load_dwordx4 v240, %[srdW2], s42 offen offset:2048 sc1 lds\n" \
  "buffer_load_dwordx4 v240, %[srdW2], s42 offen offset:3072 sc1 lds\n" \
  "s_add_u32 m0, s61, 16384\n" \
  "s_mov_b32 s42, 16384\n" \
  "buffer_load_dwordx4 v240, %[srdW2], s42 offen offset:0 sc1 lds\n" \
  "buffer_load_dwordx4 v240, %[srdW2], s42 offen offset:1024 sc1 lds\n" \
  "buffer_load_dwordx4 v240, %[srdW2], s42 offen offset:2048 sc1 lds\n" \
  "buffer_load_dwordx4 v240, %[srdW2], s42 offen offset:3072 sc1 lds\n" \
  "s_add_u32 m0, s61, 20480\n" \
  "s_mov_b32 s42, 20480\n" \
  "buffer_load_dwordx4 v240, %[srdW2], s42 offen offset:0 sc1 lds\n" \
  "buffer_load_dwordx4 v240, %[srdW2], s42 offen offset:1024 sc1 lds\n" \
  "buffer_load_dwordx4 v240, %[srdW2], s42 offen offset:2048 sc1 lds\n" \
  "buffer_load_dwordx4 v240, %[srdW2], s42 offen offset:3072 sc1 lds\n" \
  "s_add_u32 m0, s61, 24576\n" \
  "s_mov_b32 s42, 24576\n" \
  "buffer_load_dwordx4 v240, %[srdW2], s42 offen offset:0 sc1 lds\n" \
  "buffer_load_dwordx4 v240, %[srdW2], s42 offen offset:1024 sc1 lds\n" \
  "buffer_load_dwordx4 v240, %[srdW2], s42 offen offset:2048 sc1 lds\n" \
  "buffer_load_dwordx4 v240, %[srdW2], s42 offen offset:3072 sc1 lds\n" \
  "s_add_u32 m0, s61, 28672\n" \
  "s_mov_b32 s42, 28672\n" \
  "buffer_load_dwordx4 v240, %[srdW2], s42 offen offset:0 sc1 lds\n" \
  "buffer_load_dwordx4 v240, %[srdW2], s42 offen offset:1024 sc1 lds\n" \
  "buffer_load_dwordx4 v240, %[srdW2], s42 offen offset:2048 sc1 lds\n" \
  "buffer_load_dwordx4 v240, %[srdW2], s42 offen offset:3072 sc1 lds\n" \
  "s_add_u32 m0, s61, 32768\n" \
  "s_mov_b32 s42, 32768\n" \
  "buffer_load_dwordx4 v240, %[srdW2], s42 offen offset:0 sc1 lds\n" \
  "buffer_load_dwordx4 v240, %[srdW2], s42 offen offset:1024 sc1 lds\n" \
  "buffer_load_dwordx4 v240, %[srdW2], s42 offen offset:2048 sc1 lds\n" \
  "buffer_load_dwordx4 v240, %[srdW2], s42 offen offset:3072 sc1 lds\n" \
  "s_add_u32 m0, s61, 36864\n" \
  "s_mov_b32 s42, 36864\n" \
  "buffer_load_dwordx4 v240, %[srdW2], s42 offen offset:0 sc1 lds\n" \
  "buffer_load_dwordx4 v240, %[srdW2], s42 offen offset:1024 sc1 lds\n" \
  "buffer_load_dwordx4 v240, %[srdW2], s42 offen offset:2048 sc1 lds\n" \
  "buffer_load_dwordx4 v240, %[srdW2], s42 offen offset:3072 sc1 lds\n" \
  "v_lshrrev_b32 v57, 4, v240\n" \
  "v_and_b32 v58, 3, v57\n" \
  "v_bfe_u32 v57, v57, 2, 1\n" \
  "v_lshlrev_b32 v57, 6, v57\n" \
  "v_lshl_add_u32 v57, v58, 2, v57\n" \
  "s_lshl_b32 s58, %[w], 4\n" \
  "v_add_u32 v57, s58, v57\n" \
  "s_mov_b64 exec, 0xff\n" \
  "s_mov_b32 s43, 0\n" \
  ".Lsm_poll_%=:\n" \
  "global_load_dword v58, v57, %[fin] sc1\n" \
  "s_waitcnt vmcnt(0)\n" \
  "v_cmp_ne_u32 vcc, v58, v56\n" \
  "s_nop 4\n" \
  "s_cmp_eq_u64 vcc, 0\n" \
  "s_cbranch_scc1 .Lsm_pdone_%=\n" \
  "s_add_u32 s43, s43, 1\n" \
  "s_cmp_gt_u32 s43, 0x10000\n" \
  "s_cbranch_scc1 .Lsm_pdone_%=\n" \
  "s_sleep 1\n" \
  "s_branch .Lsm_poll_%=\n" \
  ".Lsm_pdone_%=:\n" \
  "s_mov_b64 exec, -1\n" \
  "s_nop 4\n" \
  "s_mov_b32 s41, 0\n" \
  "buffer_load_dwordx4 a[0:3], v240, %[srdA0], s41 offen offset:0 sc1\n" \
  "buffer_load_dwordx4 a[4:7], v240, %[srdA1], s41 offen offset:0 sc1\n" \
  "buffer_load_dwordx4 a[8:11], v240, %[srdA2], s41 offen offset:0 sc1\n" \
  "buffer_load_dwordx4 a[12:15], v240, %[srdA3], s41 offen offset:0 sc1\n" \
  "s_mov_b32 s41, 0\n" \
  "buffer_load_dwordx4 a[16:19], v240, %[srdA0], s41 offen offset:1024 sc1\n" \
  "buffer_load_dwordx4 a[20:23], v240, %[srdA1], s41 offen offset:1024 sc1\n" \
  "buffer_load_dwordx4 a[24:27], v240, %[srdA2], s41 offen offset:1024 sc1\n" \
  "buffer_load_dwordx4 a[28:31], v240, %[srdA3], s41 offen offset:1024 sc1\n" \
  "s_mov_b32 s41, 0\n" \
  "buffer_load_dwordx4 a[32:35], v240, %[srdA0], s41 offen offset:2048 sc1\n" \
  "buffer_load_dwordx4 a[36:39], v240, %[srdA1], s41 offen offset:2048 sc1\n" \
  "buffer_load_dwordx4 a[40:43], v240, %[srdA2], s41 offen offset:2048 sc1\n" \
  "buffer_load_dwordx4 a[44:47], v240, %[srdA3], s41 offen offset:2048 sc1\n" \
  "s_mov_b32 s41, 0\n" \
  "buffer_load_dwordx4 a[48:51], v240, %[srdA0], s41 offen offset:3072 sc1\n" \
  "buffer_load_dwordx4 a[52:55], v240, %[srdA1], s41 offen offset:3072 sc1\n" \
  "buffer_load_dwordx4 a[56:59], v240, %[srdA2], s41 offen offset:3072 sc1\n" \
  "buffer_load_dwordx4 a[60:63], v240, %[srdA3], s41 offen offset:3072 sc1\n" \
  "ds_read_b128 v[80:83], v247 offset:0\n" \
  "ds_read_b128 v[84:87], v247 offset:1024\n" \
  "ds_read_b128 v[88:91], v247 offset:2048\n" \
  "ds_read_b128 v[92:95], v247 offset:3072\n" \
  "ds_read_b128 v[96:99], v247 offset:4096\n" \
  "s_waitcnt vmcnt(15) lgkmcnt(4)\n" \
  "v_mfma_f32_16x16x32_bf16 v[0:3], v[80:83], a[0:3], 0\n" \
  "s_mov_b32 s41, 4096\n" \
  "s_waitcnt vmcnt(14)\n" \
  "v_mfma_f32_16x16x32_bf16 v[20:23], v[80:83], a[4:7], 0\n" \
  "buffer_load_dwordx4 a[64:67], v240, %[srdA0], s41 offen offset:0 sc1\n" \
  "s_waitcnt vmcnt(14)\n" \
  "v_mfma_f32_16x16x32_bf16 v[40:43], v[80:83], a[8:11], 0\n" \
  "buffer_load_dwordx4 a[68:71], v240, %[srdA1], s41 offen offset:0 sc1\n" \
  "s_waitcnt vmcnt(14)\n" \
  "v_mfma_f32_16x16x32_bf16 v[60:63], v[80:83], a[12:15], 0\n" \
  "ds_read_b128 v[100:103], v247 offset:5120\n" \
  "buffer_load_dwordx4 a[72:75], v240, %[srdA2], s41 offen offset:0 sc1\n" \
  "s_waitcnt lgkmcnt(4)\n" \
  "v_mfma_f32_16x16x32_bf16 v[4:7], v[84:87], a[0:3], 0\n" \
  "ds_read_b128 v[104:107], v247 offset:6144\n" \
  "buffer_load_dwordx4 a[76:79], v240, %[srdA3], s41 offen offset:0 sc1\n" \
  "v_mfma_f32_16x16x32_bf16 v[24:27], v[84:87], a[4:7], 0\n" \
  "s_mov_b32 s41, 4096\n" \
  "v_mfma_f32_16x16x32_bf16 v[44:47], v[84:87], a[8:11], 0\n" \
  "ds_read_b128 v[108:111], v247 offset:7168\n" \
  "buffer_load_dwordx4 a[80:83], v240, %[srdA0], s41 offen offset:1024 sc1\n" \
  "v_mfma_f32_16x16x32_bf16 v[64:67], v[84:87], a[12:15], 0\n" \
  "buffer_load_dwordx4 a[84:87], v240, %[srdA1], s41 offen offset:1024 sc1\n" \
  "s_waitcnt lgkmcnt(3)\n" \
  "v_mfma_f32_16x16x32_bf16 v[8:11], v[88:91], a[0:3], 0\n" \
  "ds_read_b128 v[112:115], v247 offset:8192\n" \
  "buffer_load_dwordx4 a[88:91], v240, %[srdA2], s41 offen offset:1024 sc1\n" \
  "v_mfma_f32_16x16x32_bf16 v[28:31], v[88:91], a[4:7], 0\n" \
  "ds_read_b128 v[116:119], v247 offset:9216\n" \
  "buffer_load_dwordx4 a[92:95], v240, %[srdA3], s41 offen offset:1024 sc1\n" \
  "v_mfma_f32_16x16x32_bf16 v[48:51], v[88:91], a[8:11], 0\n" \
  "s_mov_b32 s40, 40960\n" \
  "v_mfma_f32_16x16x32_bf16 v[68:71], v[88:91], a[12:15], 0\n" \
  "buffer_load_dwordx4 v[120:123], v240, %[srdW2], s40 offen offset:0 sc1\n" \
  "v_mfma_f32_16x16x32_bf16 v[12:15], v[92:95], a[0:3], 0\n" \
  "buffer_load_dwordx4 v[124:127], v240, %[srdW2], s40 offen offset:1024 sc1\n" \
  "v_mfma_f32_16x16x32_bf16 v[32:35], v[92:95], a[4:7], 0\n" \
  "buffer_load_dwordx4 v[128:131], v240, %[srdW2], s40 offen offset:2048 sc1\n" \
  "v_mfma_f32_16x16x32_bf16 v[52:55], v[92:95], a[8:11], 0\n" \
  "buffer_load_dwordx4 v[132:135], v240, %[srdW2], s40 offen offset:3072 sc1\n" \
  "v_mfma_f32_16x16x32_bf16 v[72:75], v[92:95], a[12:15], 0\n" \
  "s_mov_b32 s40, 45056\n" \
  "buffer_load_dwordx4 v[136:139], v240, %[srdW2], s40 offen offset:0 sc1\n" \
  "v_mfma_f32_16x16x32_bf16 v[16:19], v[96:99], a[0:3], 0\n" \
  "v_mfma_f32_16x16x32_bf16 v[36:39], v[96:99], a[4:7], 0\n" \
  "v_mfma_f32_16x16x32_bf16 v[56:59], v[96:99], a[8:11], 0\n" \
  "v_mfma_f32_16x16x32_bf16 v[76:79], v[96:99], a[12:15], 0\n" \
  "s_waitcnt vmcnt(24) lgkmcnt(0)\n" \
  "v_mfma_f32_16x16x32_bf16 v[0:3], v[100:103], a[16:19], v[0:3]\n" \
  "s_mov_b32 s41, 4096\n" \
  "s_waitcnt vmcnt(23)\n" \
  "v_mfma_f32_16x16x32_bf16 v[20:23], v[100:103], a[20:23], v[20:23]\n" \
  "buffer_load_dwordx4 a[96:99], v240, %[srdA0], s41 offen offset:2048 sc1\n" \
  "s_waitcnt vmcnt(23)\n" \
  "v_mfma_f32_16x16x32_bf16 v[40:43], v[100:103], a[24:27], v[40:43]\n" \
  "buffer_load_dwordx4 a[100:103], v240, %[srdA1], s41 offen offset:2048 sc1\n" \
  "s_waitcnt vmcnt(23)\n" \
  "v_mfma_f32_16x16x32_bf16 v[60:63], v[100:103], a[28:31], v[60:63]\n" \
  "ds_read_b128 v[80:83], v247 offset:10240\n" \
  "buffer_load_dwordx4 a[104:107], v240, %[srdA2], s41 offen offset:2048 sc1\n" \
  "v_mfma_f32_16x16x32_bf16 v[4:7], v[104:107], a[16:19], v[4:7]\n" \
  "ds_read_b128 v[84:87], v247 offset:11264\n" \
  "buffer_load_dwordx4 a[108:111], v240, %[srdA3], s41 offen offset:2048 sc1\n" \
  "v_mfma_f32_16x16x32_bf16 v[24:27], v[104:107], a[20:23], v[24:27]\n" \
  "s_mov_b32 s41, 4096\n" \
  "v_mfma_f32_16x16x32_bf16 v[44:47], v[104:107], a[24:27], v[44:47]\n" \
  "ds_read_b128 v[88:91], v247 offset:12288\n" \
  "buffer_load_dwordx4 a[112:115], v240, %[srdA0], s41 offen offset:3072 sc1\n" \
  "v_mfma_f32_16x16x32_bf16 v[64:67], v[104:107], a[28:31], v[64:67]\n" \
  "buffer_load_dwordx4 a[116:119], v240, %[srdA1], s41 offen offset:3072 sc1\n" \
  "v_mfma_f32_16x16x32_bf16 v[8:11], v[108:111], a[16:19], v[8:11]\n" \
  "ds_read_b128 v[92:95], v247 offset:13312\n" \
  "buffer_load_dwordx4 a[120:123], v240, %[srdA2], s41 offen offset:3072 sc1\n" \
  "v_mfma_f32_16x16x32_bf16 v[28:31], v[108:111], a[20:23], v[28:31]\n" \
  "ds_read_b128 v[96:99], v247 offset:14336\n" \
  "buffer_load_dwordx4 a[124:127], v240, %[srdA3], s41 offen offset:3072 sc1\n" \
  "v_mfma_f32_16x16x32_bf16 v[48:51], v[108:111], a[24:27], v[48:51]\n" \
  "s_mov_b32 s40, 45056\n" \
  "v_mfma_f32_16x16x32_bf16 v[68:71], v[108:111], a[28:31], v[68:71]\n" \
  "buffer_load_dwordx4 v[140:143], v240, %[srdW2], s40 offen offset:1024 sc1\n" \
  "v_mfma_f32_16x16x32_bf16 v[12:15], v[112:115], a[16:19], v[12:15]\n" \
  "buffer_load_dwordx4 v[144:147], v240, %[srdW2], s40 offen offset:2048 sc1\n" \
  "v_mfma_f32_16x16x32_bf16 v[32:35], v[112:115], a[20:23], v[32:35]\n" \
  "buffer_load_dwordx4 v[148:151], v240, %[srdW2], s40 offen offset:3072 sc1\n" \
  "v_mfma_f32_16x16x32_bf16 v[52:55], v[112:115], a[24:27], v[52:55]\n" \
  "s_mov_b32 s40, 49152\n" \
  "v_mfma_f32_16x16x32_bf16 v[72:75], v[112:115], a[28:31], v[72:75]\n" \
  "buffer_load_dwordx4 v[152:155], v240, %[srdW2], s40 offen offset:0 sc1\n" \
  "buffer_load_dwordx4 v[156:159], v240, %[srdW2], s40 offen offset:1024 sc1\n" \
  "v_mfma_f32_16x16x32_bf16 v[16:19], v[116:119], a[16:19], v[16:19]\n" \
  "v_mfma_f32_16x16x32_bf16 v[36:39], v[116:119], a[20:23], v[36:39]\n" \
  "v_mfma_f32_16x16x32_bf16 v[56:59], v[116:119], a[24:27], v[56:59]\n" \
  "v_mfma_f32_16x16x32_bf16 v[76:79], v[116:119], a[28:31], v[76:79]\n" \
  "s_waitcnt vmcnt(33) lgkmcnt(0)\n" \
  "v_mfma_f32_16x16x32_bf16 v[0:3], v[80:83], a[32:35], v[0:3]\n" \
  "s_mov_b32 s41, 8192\n" \
  "s_waitcnt vmcnt(32)\n" \
  "v_mfma_f32_16x16x32_bf16 v[20:23], v[80:83], a[36:39], v[20:23]\n" \
  "buffer_load_dwordx4 a[128:131], v240, %[srdA0], s41 offen offset:0 sc1\n" \
  "s_waitcnt vmcnt(32)\n" \
  "v_mfma_f32_16x16x32_bf16 v[40:43], v[80:83], a[40:43], v[40:43]\n" \
  "buffer_load_dwordx4 a[132:135], v240, %[srdA1], s41 offen offset:0 sc1\n" \
  "s_waitcnt vmcnt(32)\n" \
  "v_mfma_f32_16x16x32_bf16 v[60:63], v[80:83], a[44:47], v[60:63]\n" \
  "ds_read_b128 v[100:103], v247 offset:15360\n" \
  "buffer_load_dwordx4 a[136:139], v240, %[srdA2], s41 offen offset:0 sc1\n" \
  "v_mfma_f32_16x16x32_bf16 v[4:7], v[84:87], a[32:35], v[4:7]\n" \
  "ds_read_b128 v[104:107], v247 offset:16384\n" \
  "buffer_load_dwordx4 a[140:143], v240, %[srdA3], s41 offen offset:0 sc1\n" \
  "v_mfma_f32_16x16x32_bf16 v[24:27], v[84:87], a[36:39], v[24:27]\n" \
  "s_mov_b32 s41, 8192\n" \
  "v_mfma_f32_16x16x32_bf16 v[44:47], v[84:87], a[40:43], v[44:47]\n" \
  "ds_read_b128 v[108:111], v247 offset:17408\n" \
  "buffer_load_dwordx4 a[144:147], v240, %[srdA0], s41 offen offset:1024 sc1\n" \
  "v_mfma_f32_16x16x32_bf16 v[64:67], v[84:87], a[44:47], v[64:67]\n" \
  "buffer_load_dwordx4 a[148:151], v240, %[srdA1], s41 offen offset:1024 sc1\n" \
  "v_mfma_f32_16x16x32_bf16 v[8:11], v[88:91], a[32:35], v[8:11]\n" \
  "ds_read_b128 v[112:115], v247 offset:18432\n" \
  "buffer_load_dwordx4 a[152:155], v240, %[srdA2], s41 offen offset:1024 sc1\n" \
  "v_mfma_f32_16x16x32_bf16 v[28:31], v[88:91], a[36:39], v[28:31]\n" \
  "ds_read_b128 v[116:119], v247 offset:19456\n" \
  "buffer_load_dwordx4 a[156:159], v240, %[srdA3], s41 offen offset:1024 sc1\n" \
  "v_mfma_f32_16x16x32_bf16 v[48:51], v[88:91], a[40:43], v[48:51]\n" \
  "s_mov_b32 s40, 49152\n" \
  "v_mfma_f32_16x16x32_bf16 v[68:71], v[88:91], a[44:47], v[68:71]\n" \
  "buffer_load_dwordx4 v[160:163], v240, %[srdW2], s40 offen offset:2048 sc1\n" \
  "v_mfma_f32_16x16x32_bf16 v[12:15], v[92:95], a[32:35], v[12:15]\n" \
  "buffer_load_dwordx4 v[164:167], v240, %[srdW2], s40 offen offset:3072 sc1\n" \
  "v_mfma_f32_16x16x32_bf16 v[32:35], v[92:95], a[36:39], v[32:35]\n" \
  "s_mov_b32 s40, 53248\n" \
  "v_mfma_f32_16x16x32_bf16 v[52:55], v[92:95], a[40:43], v[52:55]\n" \
  "buffer_load_dwordx4 v[168:171], v240, %[srdW2], s40 offen offset:0 sc1\n" \
  "v_mfma_f32_16x16x32_bf16 v[72:75], v[92:95], a[44:47], v[72:75]\n" \
  "buffer_load_dwordx4 v[172:175], v240, %[srdW2], s40 offen offset:1024 sc1\n" \
  "buffer_load_dwordx4 v[176:179], v240, %[srdW2], s40 offen offset:2048 sc1\n" \
  "v_mfma_f32_16x16x32_bf16 v[16:19], v[96:99], a[32:35], v[16:19]\n" \
  "v_mfma_f32_16x16x32_bf16 v[36:39], v[96:99], a[36:39], v[36:39]\n" \
  "v_mfma_f32_16x16x32_bf16 v[56:59], v[96:99], a[40:43], v[56:59]\n" \
  "v_mfma_f32_16x16x32_bf16 v[76:79], v[96:99], a[44:47], v[76:79]\n" \
  "s_waitcnt vmcnt(42) lgkmcnt(0)\n" \
  "v_mfma_f32_16x16x32_bf16 v[0:3], v[100:103], a[48:51], v[0:3]\n" \
  "s_waitcnt vmcnt(41)\n" \
  "v_mfma_f32_16x16x32_bf16 v[20:23], v[100:103], a[52:55], v[20:23]\n" \
  "s_mov_b32 s41, 8192\n" \
  "s_waitcnt vmcnt(40)\n" \
  "v_mfma_f32_16x16x32_bf16 v[40:43], v[100:103], a[56:59], v[40:43]\n" \
  "buffer_load_dwordx4 a[160:163], v240, %[srdA0], s41 offen offset:2048 sc1\n" \
  "s_waitcnt vmcnt(40)\n" \
  "v_mfma_f32_16x16x32_bf16 v[60:63], v[100:103], a[60:63], v[60:63]\n" \
  "ds_read_b128 v[80:83], v247 offset:20480\n" \
  "buffer_load_dwordx4 a[164:167], v240, %[srdA1], s41 offen offset:2048 sc1\n" \
  "v_mfma_f32_16x16x32_bf16 v[4:7], v[104:107], a[48:51], v[4:7]\n" \
  "ds_read_b128 v[84:87], v247 offset:21504\n" \
  "v_mfma_f32_16x16x32_bf16 v[24:27], v[104:107], a[52:55], v[24:27]\n" \
  "buffer_load_dwordx4 a[168:171], v240, %[srdA2], s41 offen offset:2048 sc1\n" \
  "v_mfma_f32_16x16x32_bf16 v[44:47], v[104:107], a[56:59], v[44:47]\n" \
  "ds_read_b128 v[88:91], v247 offset:22528\n" \
  "buffer_load_dwordx4 a[172:175], v240, %[srdA3], s41 offen offset:2048 sc1\n" \
  "v_mfma_f32_16x16x32_bf16 v[64:67], v[104:107], a[60:63], v[64:67]\n" \
  "s_mov_b32 s40, 53248\n" \
  "v_mfma_f32_16x16x32_bf16 v[8:11], v[108:111], a[48:51], v[8:11]\n" \
  "ds_read_b128 v[92:95], v247 offset:23552\n" \
  "v_mfma_f32_16x16x32_bf16 v[28:31], v[108:111], a[52:55], v[28:31]\n" \
  "ds_read_b128 v[96:99], v247 offset:24576\n" \
  "buffer_load_dwordx4 v[180:183], v240, %[srdW2], s40 offen offset:3072 sc1\n" \
  "v_mfma_f32_16x16x32_bf16 v[48:51], v[108:111], a[56:59], v[48:51]\n" \
  "s_mov_b32 s40, 57344\n" \
  "v_mfma_f32_16x16x32_bf16 v[68:71], v[108:111], a[60:63], v[68:71]\n" \
  "buffer_load_dwordx4 v[184:187], v240, %[srdW2], s40 offen offset:0 sc1\n" \
  "v_mfma_f32_16x16x32_bf16 v[12:15], v[112:115], a[48:51], v[12:15]\n" \
  "v_mfma_f32_16x16x32_bf16 v[32:35], v[112:115], a[52:55], v[32:35]\n" \
  "buffer_load_dwordx4 v[188:191], v240, %[srdW2], s40 offen offset:1024 sc1\n" \
  "v_mfma_f32_16x16x32_bf16 v[52:55], v[112:115], a[56:59], v[52:55]\n" \
  "buffer_load_dwordx4 v[192:195], v240, %[srdW2], s40 offen offset:2048 sc1\n" \
  "v_mfma_f32_16x16x32_bf16 v[72:75], v[112:115], a[60:63], v[72:75]\n" \
  "buffer_load_dwordx4 v[196:199], v240, %[srdW2], s40 offen offset:3072 sc1\n" \
  "v_mfma_f32_16x16x32_bf16 v[16:19], v[116:119], a[48:51], v[16:19]\n" \
  "v_mfma_f32_16x16x32_bf16 v[36:39], v[116:119], a[52:55], v[36:39]\n" \
  "v_mfma_f32_16x16x32_bf16 v[56:59], v[116:119], a[56:59], v[56:59]\n" \
  "v_mfma_f32_16x16x32_bf16 v[76:79], v[116:119], a[60:63], v[76:79]\n" \
  "s_waitcnt vmcnt(47) lgkmcnt(0)\n" \
  "v_mfma_f32_16x16x32_bf16 v[0:3], v[80:83], a[64:67], v[0:3]\n" \
  "s_waitcnt vmcnt(46)\n" \
  "v_mfma_f32_16x16x32_bf16 v[20:23], v[80:83], a[68:71], v[20:23]\n" \
  "s_mov_b32 s41, 8192\n" \
  "s_waitcnt vmcnt(45)\n" \
  "v_mfma_f32_16x16x32_bf16 v[40:43], v[80:83], a[72:75], v[40:43]\n" \
  "buffer_load_dwordx4 a[176:179], v240, %[srdA0], s41 offen offset:3072 sc1\n" \
  "s_waitcnt vmcnt(45)\n" \
  "v_mfma_f32_16x16x32_bf16 v[60:63], v[80:83], a[76:79], v[60:63]\n" \
  "ds_read_b128 v[100:103], v247 offset:25600\n" \
  "buffer_load_dwordx4 a[180:183], v240, %[srdA1], s41 offen offset:3072 sc1\n" \
  "v_mfma_f32_16x16x32_bf16 v[4:7], v[84:87], a[64:67], v[4:7]\n" \
  "ds_read_b128 v[104:107], v247 offset:26624\n" \
  "v_mfma_f32_16x16x32_bf16 v[24:27], v[84:87], a[68:71], v[24:27]\n" \
  "buffer_load_dwordx4 a[184:187], v240, %[srdA2], s41 offen offset:3072 sc1\n" \
  "v_mfma_f32_16x16x32_bf16 v[44:47], v[84:87], a[72:75], v[44:47]\n" \
  "ds_read_b128 v[108:111], v247 offset:27648\n" \
  "buffer_load_dwordx4 a[188:191], v240, %[srdA3], s41 offen offset:3072 sc1\n" \
  "v_mfma_f32_16x16x32_bf16 v[64:67], v[84:87], a[76:79], v[64:67]\n" \
  "s_mov_b32 s40, 61440\n" \
  "v_mfma_f32_16x16x32_bf16 v[8:11], v[88:91], a[64:67], v[8:11]\n" \
  "ds_read_b128 v[112:115], v247 offset:28672\n" \
  "v_mfma_f32_16x16x32_bf16 v[28:31], v[88:91], a[68:71], v[28:31]\n" \
  "ds_read_b128 v[116:119], v247 offset:29696\n" \
  "buffer_load_dwordx4 v[200:203], v240, %[srdW2], s40 offen offset:0 sc1\n" \
  "v_mfma_f32_16x16x32_bf16 v[48:51], v[88:91], a[72:75], v[48:51]\n" \
  "buffer_load_dwordx4 v[204:207], v240, %[srdW2], s40 offen offset:1024 sc1\n" \
  "v_mfma_f32_16x16x32_bf16 v[68:71], v[88:91], a[76:79], v[68:71]\n" \
  "buffer_load_dwordx4 v[208:211], v240, %[srdW2], s40 offen offset:2048 sc1\n" \
  "v_mfma_f32_16x16x32_bf16 v[12:15], v[92:95], a[64:67], v[12:15]\n" \
  "v_mfma_f32_16x16x32_bf16 v[32:35], v[92:95], a[68:71], v[32:35]\n" \
  "buffer_load_dwordx4 v[212:215], v240, %[srdW2], s40 offen offset:3072 sc1\n" \
  "v_mfma_f32_16x16x32_bf16 v[52:55], v[92:95], a[72:75], v[52:55]\n" \
  "s_mov_b32 s40, 65536\n" \
  "v_mfma_f32_16x16x32_bf16 v[72:75], v[92:95], a[76:79], v[72:75]\n" \
  "buffer_load_dwordx4 v[216:219], v240, %[srdW2], s40 offen offset:0 sc1\n" \
  "v_mfma_f32_16x16x32_bf16 v[16:19], v[96:99], a[64:67], v[16:19]\n" \
  "v_mfma_f32_16x16x32_bf16 v[36:39], v[96:99], a[68:71], v[36:39]\n" \
  "v_mfma_f32_16x16x32_bf16 v[56:59], v[96:99], a[72:75], v[56:59]\n" \
  "v_mfma_f32_16x16x32_bf16 v[76:79], v[96:99], a[76:79], v[76:79]\n" \
  "s_waitcnt vmcnt(52) lgkmcnt(0)\n" \
  "v_mfma_f32_16x16x32_bf16 v[0:3], v[100:103], a[80:83], v[0:3]\n" \
  "s_waitcnt vmcnt(51)\n" \
  "v_mfma_f32_16x16x32_bf16 v[20:23], v[100:103], a[84:87], v[20:23]\n" \
  "s_mov_b32 s41, 12288\n" \
  "s_waitcnt vmcnt(50)\n" \
  "v_mfma_f32_16x16x32_bf16 v[40:43], v[100:103], a[88:91], v[40:43]\n" \
  "buffer_load_dwordx4 a[192:195], v240, %[srdA0], s41 offen offset:0 sc1\n" \
  "s_waitcnt vmcnt(50)\n" \
  "v_mfma_f32_16x16x32_bf16 v[60:63], v[100:103], a[92:95], v[60:63]\n" \
  "ds_read_b128 v[80:83], v247 offset:30720\n" \
  "buffer_load_dwordx4 a[196:199], v240, %[srdA1], s41 offen offset:0 sc1\n" \
  "v_mfma_f32_16x16x32_bf16 v[4:7], v[104:107], a[80:83], v[4:7]\n" \
  "ds_read_b128 v[84:87], v247 offset:31744\n" \
  "v_mfma_f32_16x16x32_bf16 v[24:27], v[104:107], a[84:87], v[24:27]\n" \
  "buffer_load_dwordx4 a[200:203], v240, %[srdA2], s41 offen offset:0 sc1\n" \
  "v_mfma_f32_16x16x32_bf16 v[44:47], v[104:107], a[88:91], v[44:47]\n" \
  "ds_read_b128 v[88:91], v247 offset:32768\n" \
  "buffer_load_dwordx4 a[204:207], v240, %[srdA3], s41 offen offset:0 sc1\n" \
  "v_mfma_f32_16x16x32_bf16 v[64:67], v[104:107], a[92:95], v[64:67]\n" \
  "s_mov_b32 s40, 65536\n" \
  "v_mfma_f32_16x16x32_bf16 v[8:11], v[108:111], a[80:83], v[8:11]\n" \
  "ds_read_b128 v[92:95], v247 offset:33792\n" \
  "v_mfma_f32_16x16x32_bf16 v[28:31], v[108:111], a[84:87], v[28:31]\n" \
  "ds_read_b128 v[96:99], v247 offset:34816\n" \
  "buffer_load_dwordx4 v[220:223], v240, %[srdW2], s40 offen offset:1024 sc1\n" \
  "v_mfma_f32_16x16x32_bf16 v[48:51], v[108:111], a[88:91], v[48:51]\n" \
  "buffer_load_dwordx4 v[224:227], v240, %[srdW2], s40 offen offset:2048 sc1\n" \
  "v_mfma_f32_16x16x32_bf16 v[68:71], v[108:111], a[92:95], v[68:71]\n" \
  "buffer_load_dwordx4 v[228:231], v240, %[srdW2], s40 offen offset:3072 sc1\n" \
  "v_mfma_f32_16x16x32_bf16 v[12:15], v[112:115], a[80:83], v[12:15]\n" \
  "v_mfma_f32_16x16x32_bf16 v[32:35], v[112:115], a[84:87], v[32:35]\n" \
  "s_mov_b32 s40, 69632\n" \
  "v_mfma_f32_16x16x32_bf16 v[52:55], v[112:115], a[88:91], v[52:55]\n" \
  "buffer_load_dwordx4 v[232:235], v240, %[srdW2], s40 offen offset:0 sc1\n" \
  "v_mfma_f32_16x16x32_bf16 v[72:75], v[112:115], a[92:95], v[72:75]\n" \
  "buffer_load_dwordx4 v[236:239], v240, %[srdW2], s40 offen offset:1024 sc1\n" \
  "v_mfma_f32_16x16x32_bf16 v[16:19], v[116:119], a[80:83], v[16:19]\n" \
  "v_mfma_f32_16x16x32_bf16 v[36:39], v[116:119], a[84:87], v[36:39]\n" \
  "v_mfma_f32_16x16x32_bf16 v[56:59], v[116:119], a[88:91], v[56:59]\n" \
  "v_mfma_f32_16x16x32_bf16 v[76:79], v[116:119], a[92:95], v[76:79]\n" \
  "s_waitcnt vmcnt(52) lgkmcnt(0)\n" \
  "v_mfma_f32_16x16x32_bf16 v[0:3], v[80:83], a[96:99], v[0:3]\n" \
  "s_waitcnt vmcnt(51)\n" \
  "v_mfma_f32_16x16x32_bf16 v[20:23], v[80:83], a[100:103], v[20:23]\n" \
  "s_waitcnt vmcnt(50)\n" \
  "v_mfma_f32_16x16x32_bf16 v[40:43], v[80:83], a[104:107], v[40:43]\n" \
  "s_waitcnt vmcnt(49)\n" \
  "v_mfma_f32_16x16x32_bf16 v[60:63], v[80:83], a[108:111], v[60:63]\n" \
  "ds_read_b128 v[100:103], v247 offset:35840\n" \
  "s_mov_b32 s41, 12288\n" \
  "v_mfma_f32_16x16x32_bf16 v[4:7], v[84:87], a[96:99], v[4:7]\n" \
  "ds_read_b128 v[104:107], v247 offset:36864\n" \
  "v_mfma_f32_16x16x32_bf16 v[24:27], v[84:87], a[100:103], v[24:27]\n" \
  "v_mfma_f32_16x16x32_bf16 v[44:47], v[84:87], a[104:107], v[44:47]\n" \
  "ds_read_b128 v[108:111], v247 offset:37888\n" \
  "buffer_load_dwordx4 a[208:211], v240, %[srdA0], s41 offen offset:1024 sc1\n" \
  "v_mfma_f32_16x16x32_bf16 v[64:67], v[84:87], a[108:111], v[64:67]\n" \
  "v_mfma_f32_16x16x32_bf16 v[8:11], v[88:91], a[96:99], v[8:11]\n" \
  "ds_read_b128 v[112:115], v247 offset:38912\n" \
  "v_mfma_f32_16x16x32_bf16 v[28:31], v[88:91], a[100:103], v[28:31]\n" \
  "ds_read_b128 v[116:119], v247 offset:39936\n" \
  "buffer_load_dwordx4 a[212:215], v240, %[srdA1], s41 offen offset:1024 sc1\n" \
  "v_mfma_f32_16x16x32_bf16 v[48:51], v[88:91], a[104:107], v[48:51]\n" \
  "v_mfma_f32_16x16x32_bf16 v[68:71], v[88:91], a[108:111], v[68:71]\n" \
  "v_mfma_f32_16x16x32_bf16 v[12:15], v[92:95], a[96:99], v[12:15]\n" \
  "buffer_load_dwordx4 a[216:219], v240, %[srdA2], s41 offen offset:1024 sc1\n" \
  "v_mfma_f32_16x16x32_bf16 v[32:35], v[92:95], a[100:103], v[32:35]\n" \
  "v_mfma_f32_16x16x32_bf16 v[52:55], v[92:95], a[104:107], v[52:55]\n" \
  "v_mfma_f32_16x16x32_bf16 v[72:75], v[92:95], a[108:111], v[72:75]\n" \
  "buffer_load_dwordx4 a[220:223], v240, %[srdA3], s41 offen offset:1024 sc1\n" \
  "v_mfma_f32_16x16x32_bf16 v[16:19], v[96:99], a[96:99], v[16:19]\n" \
  "v_mfma_f32_16x16x32_bf16 v[36:39], v[96:99], a[100:103], v[36:39]\n" \
  "v_mfma_f32_16x16x32_bf16 v[56:59], v[96:99], a[104:107], v[56:59]\n" \
  "v_mfma_f32_16x16x32_bf16 v[76:79], v[96:99], a[108:111], v[76:79]\n" \
  "s_waitcnt vmcnt(52) lgkmcnt(0)\n" \
  "v_mfma_f32_16x16x32_bf16 v[0:3], v[100:103], a[112:115], v[0:3]\n" \
  "s_waitcnt vmcnt(51)\n" \
  "v_mfma_f32_16x16x32_bf16 v[20:23], v[100:103], a[116:119], v[20:23]\n" \
  "s_waitcnt vmcnt(50)\n" \
  "v_mfma_f32_16x16x32_bf16 v[40:43], v[100:103], a[120:123], v[40:43]\n" \
  "s_waitcnt vmcnt(49)\n" \
  "v_mfma_f32_16x16x32_bf16 v[60:63], v[100:103], a[124:127], v[60:63]\n" \
  "v_mfma_f32_16x16x32_bf16 v[4:7], v[104:107], a[112:115], v[4:7]\n" \
  "v_mfma_f32_16x16x32_bf16 v[24:27], v[104:107], a[116:119], v[24:27]\n" \
  "v_mfma_f32_16x16x32_bf16 v[44:47], v[104:107], a[120:123], v[44:47]\n" \
  "v_mfma_f32_16x16x32_bf16 v[64:67], v[104:107], a[124:127], v[64:67]\n" \
  "v_mfma_f32_16x16x32_bf16 v[8:11], v[108:111], a[112:115], v[8:11]\n" \
  "v_mfma_f32_16x16x32_bf16 v[28:31], v[108:111], a[116:119], v[28:31]\n" \
  "v_mfma_f32_16x16x32_bf16 v[48:51], v[108:111], a[120:123], v[48:51]\n" \
  "v_mfma_f32_16x16x32_bf16 v[68:71], v[108:111], a[124:127], v[68:71]\n" \
  "v_mfma_f32_16x16x32_bf16 v[12:15], v[112:115], a[112:115], v[12:15]\n" \
  "v_mfma_f32_16x16x32_bf16 v[32:35], v[112:115], a[116:119], v[32:35]\n" \
  "v_mfma_f32_16x16x32_bf16 v[52:55], v[112:115], a[120:123], v[52:55]\n" \
  "v_mfma_f32_16x16x32_bf16 v[72:75], v[112:115], a[124:127], v[72:75]\n" \
  "v_mfma_f32_16x16x32_bf16 v[16:19], v[116:119], a[112:115], v[16:19]\n" \
  "v_mfma_f32_16x16x32_bf16 v[36:39], v[116:119], a[116:119], v[36:39]\n" \
  "v_mfma_f32_16x16x32_bf16 v[56:59], v[116:119], a[120:123], v[56:59]\n" \
  "v_mfma_f32_16x16x32_bf16 v[76:79], v[116:119], a[124:127], v[76:79]\n" \
  "s_waitcnt vmcnt(43)\n" \
  "v_mfma_f32_16x16x32_bf16 v[0:3], v[120:123], a[128:131], v[0:3]\n" \
  "s_waitcnt vmcnt(42)\n" \
  "v_mfma_f32_16x16x32_bf16 v[20:23], v[120:123], a[132:135], v[20:23]\n" \
  "s_waitcnt vmcnt(41)\n" \
  "v_mfma_f32_16x16x32_bf16 v[40:43], v[120:123], a[136:139], v[40:43]\n" \
  "s_waitcnt vmcnt(40)\n" \
  "v_mfma_f32_16x16x32_bf16 v[60:63], v[120:123], a[140:143], v[60:63]\n" \
  "v_mfma_f32_16x16x32_bf16 v[4:7], v[124:127], a[128:131], v[4:7]\n" \
  "v_mfma_f32_16x16x32_bf16 v[24:27], v[124:127], a[132:135], v[24:27]\n" \
  "v_mfma_f32_16x16x32_bf16 v[44:47], v[124:127], a[136:139], v[44:47]\n" \
  "v_mfma_f32_16x16x32_bf16 v[64:67], v[124:127], a[140:143], v[64:67]\n" \
  "v_mfma_f32_16x16x32_bf16 v[8:11], v[128:131], a[128:131], v[8:11]\n" \
  "v_mfma_f32_16x16x32_bf16 v[28:31], v[128:131], a[132:135], v[28:31]\n" \
  "v_mfma_f32_16x16x32_bf16 v[48:51], v[128:131], a[136:139], v[48:51]\n" \
  "v_mfma_f32_16x16x32_bf16 v[68:71], v[128:131], a[140:143], v[68:71]\n" \
  "v_mfma_f32_16x16x32_bf16 v[12:15], v[132:135], a[128:131], v[12:15]\n" \
  "v_mfma_f32_16x16x32_bf16 v[32:35], v[132:135], a[132:135], v[32:35]\n" \
  "v_mfma_f32_16x16x32_bf16 v[52:55], v[132:135], a[136:139], v[52:55]\n" \
  "v_mfma_f32_16x16x32_bf16 v[72:75], v[132:135], a[140:143], v[72:75]\n" \
  "v_mfma_f32_16x16x32_bf16 v[16:19], v[136:139], a[128:131], v[16:19]\n" \
  "v_mfma_f32_16x16x32_bf16 v[36:39], v[136:139], a[132:135], v[36:39]\n" \
  "v_mfma_f32_16x16x32_bf16 v[56:59], v[136:139], a[136:139], v[56:59]\n" \
  "v_mfma_f32_16x16x32_bf16 v[76:79], v[136:139], a[140:143], v[76:79]\n" \
  "s_waitcnt vmcnt(39)\n" \
  "v_mfma_f32_16x16x32_bf16 v[0:3], v[140:143], a[144:147], v[0:3]\n" \
  "s_waitcnt vmcnt(38)\n" \
  "v_mfma_f32_16x16x32_bf16 v[20:23], v[140:143], a[148:151], v[20:23]\n" \
  "s_waitcnt vmcnt(37)\n" \
  "v_mfma_f32_16x16x32_bf16 v[40:43], v[140:143], a[152:155], v[40:43]\n" \
  "s_waitcnt vmcnt(36)\n" \
  "v_mfma_f32_16x16x32_bf16 v[60:63], v[140:143], a[156:159], v[60:63]\n" \
  "v_mfma_f32_16x16x32_bf16 v[4:7], v[144:147], a[144:147], v[4:7]\n" \
  "v_mfma_f32_16x16x32_bf16 v[24:27], v[144:147], a[148:151], v[24:27]\n" \
  "v_mfma_f32_16x16x32_bf16 v[44:47], v[144:147], a[152:155], v[44:47]\n" \
  "v_mfma_f32_16x16x32_bf16 v[64:67], v[144:147], a[156:159], v[64:67]\n" \
  "v_mfma_f32_16x16x32_bf16 v[8:11], v[148:151], a[144:147], v[8:11]\n" \
  "v_mfma_f32_16x16x32_bf16 v[28:31], v[148:151], a[148:151], v[28:31]\n" \
  "v_mfma_f32_16x16x32_bf16 v[48:51], v[148:151], a[152:155], v[48:51]\n" \
  "v_mfma_f32_16x16x32_bf16 v[68:71], v[148:151], a[156:159], v[68:71]\n" \
  "v_mfma_f32_16x16x32_bf16 v[12:15], v[152:155], a[144:147], v[12:15]\n" \
  "v_mfma_f32_16x16x32_bf16 v[32:35], v[152:155], a[148:151], v[32:35]\n" \
  "v_mfma_f32_16x16x32_bf16 v[52:55], v[152:155], a[152:155], v[52:55]\n" \
  "v_mfma_f32_16x16x32_bf16 v[72:75], v[152:155], a[156:159], v[72:75]\n" \
  "v_mfma_f32_16x16x32_bf16 v[16:19], v[156:159], a[144:147], v[16:19]\n" \
  "v_mfma_f32_16x16x32_bf16 v[36:39], v[156:159], a[148:151], v[36:39]\n" \
  "v_mfma_f32_16x16x32_bf16 v[56:59], v[156:159], a[152:155], v[56:59]\n" \
  "v_mfma_f32_16x16x32_bf16 v[76:79], v[156:159], a[156:159], v[76:79]\n" \
  "s_waitcnt vmcnt(30)\n" \
  "v_mfma_f32_16x16x32_bf16 v[0:3], v[160:163], a[160:163], v[0:3]\n" \
  "s_waitcnt vmcnt(29)\n" \
  "v_mfma_f32_16x16x32_bf16 v[20:23], v[160:163], a[164:167], v[20:23]\n" \
  "s_waitcnt vmcnt(28)\n" \
  "v_mfma_f32_16x16x32_bf16 v[40:43], v[160:163], a[168:171], v[40:43]\n" \
  "s_waitcnt vmcnt(27)\n" \
  "v_mfma_f32_16x16x32_bf16 v[60:63], v[160:163], a[172:175], v[60:63]\n" \
  "v_mfma_f32_16x16x32_bf16 v[4:7], v[164:167], a[160:163], v[4:7]\n" \
  "v_mfma_f32_16x16x32_bf16 v[24:27], v[164:167], a[164:167], v[24:27]\n" \
  "v_mfma_f32_16x16x32_bf16 v[44:47], v[164:167], a[168:171], v[44:47]\n" \
  "v_mfma_f32_16x16x32_bf16 v[64:67], v[164:167], a[172:175], v[64:67]\n" \
  "v_mfma_f32_16x16x32_bf16 v[8:11], v[168:171], a[160:163], v[8:11]\n" \
  "v_mfma_f32_16x16x32_bf16 v[28:31], v[168:171], a[164:167], v[28:31]\n" \
  "v_mfma_f32_16x16x32_bf16 v[48:51], v[168:171], a[168:171], v[48:51]\n" \
  "v_mfma_f32_16x16x32_bf16 v[68:71], v[168:171], a[172:175], v[68:71]\n" \
  "v_mfma_f32_16x16x32_bf16 v[12:15], v[172:175], a[160:163], v[12:15]\n" \
  "v_mfma_f32_16x16x32_bf16 v[32:35], v[172:175], a[164:167], v[32:35]\n" \
  "v_mfma_f32_16x16x32_bf16 v[52:55], v[172:175], a[168:171], v[52:55]\n" \
  "v_mfma_f32_16x16x32_bf16 v[72:75], v[172:175], a[172:175], v[72:75]\n" \
  "v_mfma_f32_16x16x32_bf16 v[16:19], v[176:179], a[160:163], v[16:19]\n" \
  "v_mfma_f32_16x16x32_bf16 v[36:39], v[176:179], a[164:167], v[36:39]\n" \
  "v_mfma_f32_16x16x32_bf16 v[56:59], v[176:179], a[168:171], v[56:59]\n" \
  "v_mfma_f32_16x16x32_bf16 v[76:79], v[176:179], a[172:175], v[76:79]\n" \
  "s_waitcnt vmcnt(21)\n" \
  "v_mfma_f32_16x16x32_bf16 v[0:3], v[180:183], a[176:179], v[0:3]\n" \
  "s_waitcnt vmcnt(20)\n" \
  "v_mfma_f32_16x16x32_bf16 v[20:23], v[180:183], a[180:183], v[20:23]\n" \
  "s_waitcnt vmcnt(19)\n" \
  "v_mfma_f32_16x16x32_bf16 v[40:43], v[180:183], a[184:187], v[40:43]\n" \
  "s_waitcnt vmcnt(18)\n" \
  "v_mfma_f32_16x16x32_bf16 v[60:63], v[180:183], a[188:191], v[60:63]\n" \
  "v_mfma_f32_16x16x32_bf16 v[4:7], v[184:187], a[176:179], v[4:7]\n" \
  "v_mfma_f32_16x16x32_bf16 v[24:27], v[184:187], a[180:183], v[24:27]\n" \
  "v_mfma_f32_16x16x32_bf16 v[44:47], v[184:187], a[184:187], v[44:47]\n" \
  "v_mfma_f32_16x16x32_bf16 v[64:67], v[184:187], a[188:191], v[64:67]\n" \
  "v_mfma_f32_16x16x32_bf16 v[8:11], v[188:191], a[176:179], v[8:11]\n" \
  "v_mfma_f32_16x16x32_bf16 v[28:31], v[188:191], a[180:183], v[28:31]\n" \
  "v_mfma_f32_16x16x32_bf16 v[48:51], v[188:191], a[184:187], v[48:51]\n" \
  "v_mfma_f32_16x16x32_bf16 v[68:71], v[188:191], a[188:191], v[68:71]\n" \
  "v_mfma_f32_16x16x32_bf16 v[12:15], v[192:195], a[176:179], v[12:15]\n" \
  "v_mfma_f32_16x16x32_bf16 v[32:35], v[192:195], a[180:183], v[32:35]\n" \
  "v_mfma_f32_16x16x32_bf16 v[52:55], v[192:195], a[184:187], v[52:55]\n" \
  "v_mfma_f32_16x16x32_bf16 v[72:75], v[192:195], a[188:191], v[72:75]\n" \
  "v_mfma_f32_16x16x32_bf16 v[16:19], v[196:199], a[176:179], v[16:19]\n" \
  "v_mfma_f32_16x16x32_bf16 v[36:39], v[196:199], a[180:183], v[36:39]\n" \
  "v_mfma_f32_16x16x32_bf16 v[56:59], v[196:199], a[184:187], v[56:59]\n" \
  "v_mfma_f32_16x16x32_bf16 v[76:79], v[196:199], a[188:191], v[76:79]\n" \
  "s_waitcnt vmcnt(12)\n" \
  "v_mfma_f32_16x16x32_bf16 v[0:3], v[200:203], a[192:195], v[0:3]\n" \
  "s_waitcnt vmcnt(11)\n" \
  "v_mfma_f32_16x16x32_bf16 v[20:23], v[200:203], a[196:199], v[20:23]\n" \
  "s_waitcnt vmcnt(10)\n" \
  "v_mfma_f32_16x16x32_bf16 v[40:43], v[200:203], a[200:203], v[40:43]\n" \
  "s_waitcnt vmcnt(9)\n" \
  "v_mfma_f32_16x16x32_bf16 v[60:63], v[200:203], a[204:207], v[60:63]\n" \
  "v_mfma_f32_16x16x32_bf16 v[4:7], v[204:207], a[192:195], v[4:7]\n" \
  "v_mfma_f32_16x16x32_bf16 v[24:27], v[204:207], a[196:199], v[24:27]\n" \
  "v_mfma_f32_16x16x32_bf16 v[44:47], v[204:207], a[200:203], v[44:47]\n" \
  "v_mfma_f32_16x16x32_bf16 v[64:67], v[204:207], a[204:207], v[64:67]\n" \
  "v_mfma_f32_16x16x32_bf16 v[8:11], v[208:211], a[192:195], v[8:11]\n" \
  "v_mfma_f32_16x16x32_bf16 v[28:31], v[208:211], a[196:199], v[28:31]\n" \
  "v_mfma_f32_16x16x32_bf16 v[48:51], v[208:211], a[200:203], v[48:51]\n" \
  "v_mfma_f32_16x16x32_bf16 v[68:71], v[208:211], a[204:207], v[68:71]\n" \
  "v_mfma_f32_16x16x32_bf16 v[12:15], v[212:215], a[192:195], v[12:15]\n" \
  "v_mfma_f32_16x16x32_bf16 v[32:35], v[212:215], a[196:199], v[32:35]\n" \
  "v_mfma_f32_16x16x32_bf16 v[52:55], v[212:215], a[200:203], v[52:55]\n" \
  "v_mfma_f32_16x16x32_bf16 v[72:75], v[212:215], a[204:207], v[72:75]\n" \
  "v_mfma_f32_16x16x32_bf16 v[16:19], v[216:219], a[192:195], v[16:19]\n" \
  "v_mfma_f32_16x16x32_bf16 v[36:39], v[216:219], a[196:199], v[36:39]\n" \
  "v_mfma_f32_16x16x32_bf16 v[56:59], v[216:219], a[200:203], v[56:59]\n" \
  "v_mfma_f32_16x16x32_bf16 v[76:79], v[216:219], a[204:207], v[76:79]\n" \
  "s_waitcnt vmcnt(3)\n" \
  "v_mfma_f32_16x16x32_bf16 v[0:3], v[220:223], a[208:211], v[0:3]\n" \
  "s_waitcnt vmcnt(2)\n" \
  "v_mfma_f32_16x16x32_bf16 v[20:23], v[220:223], a[212:215], v[20:23]\n" \
  "s_waitcnt vmcnt(1)\n" \
  "v_mfma_f32_16x16x32_bf16 v[40:43], v[220:223], a[216:219], v[40:43]\n" \
  "s_waitcnt vmcnt(0)\n" \
  "v_mfma_f32_16x16x32_bf16 v[60:63], v[220:223], a[220:223], v[60:63]\n" \
  "v_mfma_f32_16x16x32_bf16 v[4:7], v[224:227], a[208:211], v[4:7]\n" \
  "v_mfma_f32_16x16x32_bf16 v[24:27], v[224:227], a[212:215], v[24:27]\n" \
  "v_mfma_f32_16x16x32_bf16 v[44:47], v[224:227], a[216:219], v[44:47]\n" \
  "v_mfma_f32_16x16x32_bf16 v[64:67], v[224:227], a[220:223], v[64:67]\n" \
  "v_mfma_f32_16x16x32_bf16 v[8:11], v[228:231], a[208:211], v[8:11]\n" \
  "v_mfma_f32_16x16x32_bf16 v[28:31], v[228:231], a[212:215], v[28:31]\n" \
  "v_mfma_f32_16x16x32_bf16 v[48:51], v[228:231], a[216:219], v[48:51]\n" \
  "v_mfma_f32_16x16x32_bf16 v[68:71], v[228:231], a[220:223], v[68:71]\n" \
  "v_mfma_f32_16x16x32_bf16 v[12:15], v[232:235], a[208:211], v[12:15]\n" \
  "v_mfma_f32_16x16x32_bf16 v[32:35], v[232:235], a[212:215], v[32:35]\n" \
  "v_mfma_f32_16x16x32_bf16 v[52:55], v[232:235], a[216:219], v[52:55]\n" \
  "v_mfma_f32_16x16x32_bf16 v[72:75], v[232:235], a[220:223], v[72:75]\n" \
  "v_mfma_f32_16x16x32_bf16 v[16:19], v[236:239], a[208:211], v[16:19]\n" \
  "v_mfma_f32_16x16x32_bf16 v[36:39], v[236:239], a[212:215], v[36:39]\n" \
  "v_mfma_f32_16x16x32_bf16 v[56:59], v[236:239], a[216:219], v[56:59]\n" \
  "v_mfma_f32_16x16x32_bf16 v[76:79], v[236:239], a[220:223], v[76:79]\n" \
  "ds_write_b128 v247, v[0:3] offset:0\n" \
  "ds_write_b128 v247, v[4:7] offset:1024\n" \
  "ds_write_b128 v247, v[8:11] offset:2048\n" \
  "ds_write_b128 v247, v[12:15] offset:3072\n" \
  "s_nop 0\n" \
  "ds_write_b128 v247, v[16:19] offset:4096\n" \
  "ds_write_b128 v247, v[20:23] offset:5120\n" \
  "ds_write_b128 v247, v[24:27] offset:6144\n" \
  "ds_write_b128 v247, v[28:31] offset:7168\n" \
  "ds_write_b128 v247, v[32:35] offset:8192\n" \
  "ds_write_b128 v247, v[36:39] offset:9216\n" \
  "ds_write_b128 v247, v[40:43] offset:10240\n" \
  "ds_write_b128 v247, v[44:47] offset:11264\n" \
  "ds_write_b128 v247, v[48:51] offset:12288\n" \
  "ds_write_b128 v247, v[52:55] offset:13312\n" \
  "ds_write_b128 v247, v[56:59] offset:14336\n" \
  "ds_write_b128 v247, v[60:63] offset:15360\n" \
  "ds_write_b128 v247, v[64:67] offset:16384\n" \
  "ds_write_b128 v247, v[68:71] offset:17408\n" \
  "ds_write_b128 v247, v[72:75] offset:18432\n" \
  "ds_write_b128 v247, v[76:79] offset:19456\n" \
  "s_waitcnt lgkmcnt(0)\n"

#define K1_CLOBBERS "v0","v1","v2","v3","v4","v5","v6","v7","v8","v9","v10","v11","v12","v13","v14","v15","v16","v17","v18","v19","v20","v21","v22","v23","v24","v25","v26","v27","v28","v29","v30","v31","v32","v33","v34","v35","v36","v37","v38","v39","v40","v41","v42","v43","v44","v45","v46","v47","v48","v49","v50","v51","v52","v53","v54","v55","v56","v57","v58","v59","v60","v61","v62","v63","v64","v65","v66","v67","v68","v69","v70","v71","v72","v73","v74","v75","v76","v77","v78","v79","v80","v81","v82","v83","v84","v85","v86","v87","v88","v89","v90","v91","v92","v93","v94","v95","v96","v97","v98","v99","v100","v101","v102","v103","v104","v105","v106","v107","v108","v109","v110","v111","v112","v113","v114","v115","v116","v117","v118","v119","v120","v121","v122","v123","v124","v125","v126","v127","v128","v129","v130","v131","v132","v133","v134","v135","v136","v137","v138","v139","v140","v141","v142","v143","v144","v145","v146","v147","v148","v149","v150","v151","v152","v153","v154","v155","v156","v157","v158","v159","v160","v161","v162","v163","v164","v165","v166","v167","v168","v169","v170","v171","v172","v173","v174","v175","v176","v177","v178","v179","v180","v181","v182","v183","v184","v185","v186","v187","v188","v189","v190","v191","v192","v193","v194","v195","v196","v197","v198","v199","v200","v201","v202","v203","v204","v205","v206","v207","v208","v209","v210","v211","v212","v213","v214","v215","v216","v217","v218","v219","v220","v221","v222","v223","v224","v225","v226","v227","v228","v229","v230","v231","v232","v233","v234","v235","v236","v237","v238","v239","v240","v241","v242","v243","v244","v245","v246","v247","v248","v249","v250","v251","v252","v253","a0","a1","a2","a3","a4","a5","a6","a7","a8","a9","a10","a11","a12","a13","a14","a15","a16","a17","a18","a19","a20","a21","a22","a23","a24","a25","a26","a27","a28","a29","a30","a31","a32","a33","a34","a35","a36","a37","a38","a39","a40","a41","a42","a43","a44","a45","a46","a47","a48","a49","a50","a51","a52","a53","a54","a55","a56","a57","a58","a59","a60","a61","a62","a63","a64","a65","a66","a67","a68","a69","a70","a71","a72","a73","a74","a75","a76","a77","a78","a79","a80","a81","a82","a83","a84","a85","a86","a87","a88","a89","a90","a91","a92","a93","a94","a95","a96","a97","a98","a99","a100","a101","a102","a103","a104","a105","a106","a107","a108","a109","a110","a111","a112","a113","a114","a115","a116","a117","a118","a119","a120","a121","a122","a123","a124","a125","a126","a127","a128","a129","a130","a131","a132","a133","a134","a135","a136","a137","a138","a139","a140","a141","a142","a143","a144","a145","a146","a147","a148","a149","a150","a151","a152","a153","a154","a155","a156","a157","a158","a159","a160","a161","a162","a163","a164","a165","a166","a167","a168","a169","a170","a171","a172","a173","a174","a175","a176","a177","a178","a179","a180","a181","a182","a183","a184","a185","a186","a187","a188","a189","a190","a191","a192","a193","a194","a195","a196","a197","a198","a199","a200","a201","a202","a203","a204","a205","a206","a207","a208","a209","a210","a211","a212","a213","a214","a215","a216","a217","a218","a219","a220","a221","a222","a223","a224","a225","a226","a227","a228","a229","a230","a231","a232","a233","a234","a235","a236","a237","a238","a239","a240","a241","a242","a243","a244","a245","a246","a247","a248","a249","a250","a251","a252","a253","a254","a255","s40","s41","s42","s43","s44","s45","s46","s47","s48","s49","s50","s51","s52","s53","s54","s55","s56","s57","s58","s59","s60","s61","s62","s63", "vcc", "scc", "memory"
#define T1_CLOBBERS "v0","v1","v2","v3","v4","v5","v6","v7","v8","v9","v10","v11","v12","v13","v14","v15","v16","v17","v18","v19","v20","v21","v22","v23","v24","v25","v26","v27","v28","v29","v30","v31","v32","v33","v34","v35","v36","v37","v38","v39","v40","v41","v42","v43","v44","v45","v46","v47","v48","v49","v50","v51","v52","v53","v54","v55","v56","v57","v58","v59","v60","v61","v62","v63","v64","v65","v66","v67","v68","v69","v70","v71","v72","v73","v74","v75","v76","v77","v78","v79","v80","v81","v82","v83","v84","v85","v86","v87","v88","v89","v90","v91","v92","v93","v94","v95","v96","v97","v98","v99","v100","v101","v102","v103","v104","v105","v106","v107","v108","v109","v110","v111","v112","v113","v114","v115","v116","v117","v118","v119","v120","v121","v122","v123","v124","v125","v126","v127","v128","v129","v130","v131","v132","v133","v134","v135","v136","v137","v138","v139","v140","v141","v142","v143","v144","v145","v146","v147","v148","v149","v150","v151","v152","v153","v154","v155","v156","v157","v158","v159","v160","v161","v162","v163","v164","v165","v166","v167","v168","v169","v170","v171","v172","v173","v174","v175","v176","v177","v178","v179","v180","v181","v182","v183","v184","v185","v186","v187","v188","v189","v190","v191","v192","v193","v194","v195","v196","v197","v198","v199","v200","v201","v202","v203","v204","v205","v206","v207","v208","v209","v210","v211","v212","v213","v214","v215","v216","a0","a1","a2","a3","a4","a5","a6","a7","a8","a9","a10","a11","a12","a13","a14","a15","a16","a17","a18","a19","a20","a21","a22","a23","a24","a25","a26","a27","a28","a29","a30","a31","a32","a33","a34","a35","a36","a37","a38","a39","a40","a41","a42","a43","a44","a45","a46","a47","a48","a49","a50","a51","a52","a53","a54","a55","a56","a57","a58","a59","a60","a61","a62","a63","a64","a65","a66","a67","a68","a69","a70","a71","a72","a73","a74","a75","a76","a77","a78","a79","a80","a81","a82","a83","a84","a85","a86","a87","a88","a89","a90","a91","a92","a93","a94","a95","a96","a97","a98","a99","a100","a101","a102","a103","a104","a105","a106","a107","a108","a109","a110","a111","a112","a113","a114","a115","a116","a117","a118","a119","a120","a121","a122","a123","a124","a125","a126","a127","a128","a129","a130","a131","a132","a133","a134","a135","a136","a137","a138","a139","a140","a141","a142","a143","a144","a145","a146","a147","a148","a149","a150","a151","a152","a153","a154","a155","a156","a157","a158","a159","a160","a161","a162","a163","a164","a165","a166","a167","a168","a169","a170","a171","a172","a173","a174","a175","a176","a177","a178","a179","a180","a181","a182","a183","a184","a185","a186","a187","a188","a189","a190","a191","a192","a193","a194","a195","a196","a197","a198","a199","a200","a201","a202","a203","a204","a205","a206","a207","a208","a209","a210","a211","a212","a213","a214","a215","a216","a217","a218","a219","a220","a221","a222","a223","a224","a225","a226","a227","a228","a229","a230","a231","a232","a233","a234","a235","a236","a237","a238","a239","a240","a241","a242","a243","a244","a245","a246","a247","a248","a249","a250","a251","a252","a253","a254","a255","s40","s41","s42","s43","s44","s45","s46","s47","s48","s49","s50","s51","s52","s53","s54","s55","s56","s57","s58","s59","s60","s61","s62","s63", "vcc", "scc", "memory"
#define T34_CLOBBERS "v0","v1","v2","v3","v4","v5","v6","v7","v8","v9","v10","v11","v12","v13","v14","v15","v16","v17","v18","v19","v20","v21","v22","v23","v24","v25","v26","v27","v28","v29","v30","v31","v32","v33","v34","v35","v36","v37","v38","v39","v40","v41","v42","v43","v44","v45","v46","v47","v48","v49","v50","v51","v52","v53","v54","v55","v56","v57","v58","v59","v60","v61","v62","v63","v64","v65","v66","v67","v68","v69","v70","v71","v72","v73","v74","v75","v76","v77","v78","v79","v80","v81","v82","v83","v84","v85","v86","v87","v88","v89","v90","v91","v92","v93","v94","v95","v96","v97","v98","v99","v100","v101","v102","v103","v104","v105","v106","v107","v108","v109","v110","v111","v112","v113","v114","v115","v116","v117","v118","v119","v120","v121","v122","v123","v124","v125","v126","v127","v128","v129","v130","v131","v132","v133","v134","v135","v136","v137","v138","v139","v140","v141","v142","v143","v144","v145","v146","v147","v148","v149","v150","v151","v152","v153","v154","v155","v156","v157","v158","v159","v160","v161","v162","v163","v164","v165","v166","v167","v168","v169","v170","v171","v172","v173","v174","v175","v176","v177","v178","v179","v180","v181","v182","v183","v184","v185","v186","v187","v188","v189","v190","v191","v192","v193","v194","v195","v196","v197","v198","v199","v200","v201","v202","v203","v204","v205","v206","v207","v208","v209","v210","v211","v212","v213","v214","v215","v216","v217","v218","v219","v220","v221","v222","v223","v224","v225","v226","v227","v228","v229","v230","v231","v232","v233","v234","v235","v236","v237","v238","v239","v240","v241","v242","v243","v244","v245","v246","v247","a0","a1","a2","a3","a4","a5","a6","a7","a8","a9","a10","a11","a12","a13","a14","a15","a16","a17","a18","a19","a20","a21","a22","a23","a24","a25","a26","a27","a28","a29","a30","a31","a32","a33","a34","a35","a36","a37","a38","a39","a40","a41","a42","a43","a44","a45","a46","a47","a48","a49","a50","a51","a52","a53","a54","a55","a56","a57","a58","a59","a60","a61","a62","a63","a64","a65","a66","a67","a68","a69","a70","a71","a72","a73","a74","a75","a76","a77","a78","a79","a80","a81","a82","a83","a84","a85","a86","a87","a88","a89","a90","a91","a92","a93","a94","a95","a96","a97","a98","a99","a100","a101","a102","a103","a104","a105","a106","a107","a108","a109","a110","a111","a112","a113","a114","a115","a116","a117","a118","a119","a120","a121","a122","a123","a124","a125","a126","a127","a128","a129","a130","a131","a132","a133","a134","a135","a136","a137","a138","a139","a140","a141","a142","a143","a144","a145","a146","a147","a148","a149","a150","a151","a152","a153","a154","a155","a156","a157","a158","a159","a160","a161","a162","a163","a164","a165","a166","a167","a168","a169","a170","a171","a172","a173","a174","a175","a176","a177","a178","a179","a180","a181","a182","a183","a184","a185","a186","a187","a188","a189","a190","a191","a192","a193","a194","a195","a196","a197","a198","a199","a200","a201","a202","a203","a204","a205","a206","a207","a208","a209","a210","a211","a212","a213","a214","a215","a216","a217","a218","a219","a220","a221","a222","a223","s40","s41","s42","s43","s44","s45","s46","s47","s48","s49","s50","s51","s52","s53","s54","s55","s56","s57","s58","s59","s60","s61","s62","s63", "vcc", "scc", "memory"

typedef __attribute__((address_space(1))) u32 gu32;
#define RLX_AGENT __ATOMIC_RELAXED, __HIP_MEMORY_SCOPE_AGENT
constexpr int SY_EPOCH = 0, SY_ERR = 1;
constexpr int SY_MODF = 64;
constexpr int SY_H = 320;
constexpr int SY_REL = 576;
constexpr int SY_P = 1088;
constexpr int SY_OW = 1856;
constexpr int SY_PW = 2880;
constexpr int SY_H2W = 4416;
constexpr int SY_A1W = 5440;

DI unsigned lane_id() { unsigned l; asm volatile("v_mbcnt_lo_u32_b32 %0, -1, 0\n\tv_mbcnt_hi_u32_b32 %0, -1, %0" : "=v"(l)); return l; }
DI void raw_barrier() { asm volatile("s_waitcnt lgkmcnt(0)\n\ts_barrier" ::: "memory"); }
DI void publish_nodrain(gu32* flag, unsigned tag, unsigned w) {
  raw_barrier();
  if (w == 0 && lane_id() == 0) __hip_atomic_store(flag, tag, RLX_AGENT);
}
struct Ptrs {
  const u16* x; const u16* kc; const u16* vc; const u16* wq; const u16* b1; const u16* b2;
  const u16* c; const u16* wmod; const u16* wo; const u16* w1; const u16* w2;
  char* ws; u16* out;
};

typedef __attribute__((address_space(1))) unsigned long long gu64;
constexpr unsigned P0_LDS = 32768;
DI void dma16(unsigned ldsaddr, unsigned voff, rsrc_t srd, unsigned soff) {
  asm volatile("s_mov_b32 m0, %0\n\ts_nop 0\n\tbuffer_load_dwordx4 %1, %2, %3 offen lds" :: "s"(ldsaddr), "v"(voff), "s"(srd), "s"(soff) : "memory");
}
DI void dma16c(unsigned ldsaddr, unsigned voff, rsrc_t srd, unsigned soff) {
  asm volatile("s_mov_b32 m0, %0\n\ts_nop 0\n\tbuffer_load_dwordx4 %1, %2, %3 offen sc1 lds" :: "s"(ldsaddr), "v"(voff), "s"(srd), "s"(soff) : "memory");
}
DI void code_touch() {
  const unsigned lane = lane_id();
  unsigned long long pc; unsigned len;
  asm volatile("s_getpc_b64 %0\n.Lmega_code_begin:\n\ts_mov_b32 %1, .Lmega_code_end-.Lmega_code_begin" : "=s"(pc), "=s"(len));
  for (unsigned off = 0; off < len; off += 8192u) {
    const unsigned o = off + lane * 128u;
    const char* a = (const char*)pc + (o < len ? o : 0u);
    asm volatile("s_mov_b32 m0, %0\n\ts_nop 0\n\tglobal_load_lds_dword %1, off" :: "s"((unsigned)(P0_LDS + 32256)), "v"(a) : "memory");
  }
}
DI void input_touch(const Ptrs& p) {
  const unsigned lane = lane_id();
#pragma unroll
  for (unsigned i = 0; i < 2; ++i) {
    const unsigned o = (blockIdx.x * 128u + i * 64u + lane) * 128u;
    const char* a = (const char*)p.w2 + (o < D * F * 2u ? o : 0u);
    asm volatile("s_mov_b32 m0, %0\n\ts_nop 0\n\tglobal_load_lds_dword %1, off" :: "s"((unsigned)(P0_LDS + 32256)), "v"(a) : "memory");
  }
}
DI void p0_prefetch(const Ptrs& p) {
  const unsigned lane = lane_id(), x = blockIdx.x & 7u, j = blockIdx.x >> 3, hd = 4u * x + (j & 3u);
  rsrc_t srdW = mk_rsrc((const char*)p.wq + hd * 256u, 640u * 8192u - hd * 256u);
  rsrc_t srdK = mk_rsrc((const char*)p.kc + ((size_t)hd * SK + (SK - W)) * 256u, W * 256u);
  const unsigned vW = ((lane >> 2) & 7u) * 8192u + (lane >> 5) * 64u + (lane & 3u) * 16u;
  const unsigned rr = lane >> 4, pos = lane & 15u;
#pragma unroll 1
  for (unsigned jt = 0; jt < 6; ++jt) {
#pragma unroll
    for (unsigned q = 0; q < 16; ++q)
      dma16(65536u + jt * 16384u + q * 1024u, vW, srdW, (64u * jt + 8u * (q >> 1)) * 8192u + (q & 1u) * 128u);
    if (jt == 0 || jt == 3) {
      const unsigned t = jt ? 1u : 0u;
#pragma unroll
      for (unsigned q = 0; q < 16; ++q)
        dma16(t * 16384u + q * 1024u, rr * 256u + 16u * (pos ^ ((4u * (q & 3u) + rr) & 15u)), srdK, (64u * t + 4u * q) * 256u);
    }
  }
}
#define MODOK(b) (((b) >> 3) < 30u)
#define MODSL(b) (((b) & 7u) * 30u + (((b) >> 3) < 30u ? ((b) >> 3) : 0u))
struct ModPre { u32x4 wv[5]; u16 cv[5]; };
DI void p0_mod_load(const Ptrs& p, unsigned w, ModPre& m) {
  const unsigned b = blockIdx.x;
  const unsigned sl = MODSL(b), lane = lane_id(), r0 = 160u * w + 5u * (lane >> 1), col0 = sl * 16u + (lane & 1u) * 8u;
#pragma unroll
  for (int i = 0; i < 5; ++i) m.wv[i] = *(const u32x4*)(p.wmod + (size_t)(r0 + i) * (6 * D) + col0);
#pragma unroll
  for (int i = 0; i < 5; ++i) m.cv[i] = p.c[r0 + i];
}
DI void p0_mod_partial(const ModPre& m, char* lds, unsigned w) {
  const unsigned lane = lane_id();
  float acc[8] = {};
#pragma unroll
  for (int i = 0; i < 5; ++i) {
    const float cf = bf2f(m.cv[i]), sv = cf * __builtin_amdgcn_rcpf(1.f + __expf(-cf));
#pragma unroll
    for (int e = 0; e < 4; ++e) { acc[2 * e] += sv * bflo(m.wv[i][e]); acc[2 * e + 1] += sv * bfhi(m.wv[i][e]); }
  }
#pragma unroll
  for (int e = 0; e < 8; ++e) {
    float x = acc[e];
    x = dpp_add<0x122>(x); x = dpp_add<0x124>(x); x = dpp_add<0x128>(x);
    acc[e] = swap32_add(swap16_add(x));
  }
  if (lane < 2u) {
    f32x4* dst = (f32x4*)(lds + P0_LDS + 64 + w * 64 + lane * 32);
    f32x4 a = {acc[0], acc[1], acc[2], acc[3]}, b = {acc[4], acc[5], acc[6], acc[7]};
    dst[0] = a; dst[1] = b;
  }
}
DI void p0_mod_publish(const Ptrs& p, char* lds, gu32* sy, unsigned tag) {
  const unsigned b = blockIdx.x;
  if (!MODOK(b)) return;
  const unsigned sl = MODSL(b), lane = lane_id();
  if (lane < 16u) {
    const float* part = (const float*)(lds + P0_LDS + 64);
    const float val = (part[lane] + part[16 + lane]) + (part[32 + lane] + part[48 + lane]);
    const unsigned col = sl * 16u + lane;
    if (col < 1280u) __hip_atomic_store((gu64*)(p.ws + WS_G) + col, ((unsigned long long)tag << 32) | __float_as_uint(val), RLX_AGENT);
    __hip_atomic_store((gu32*)(p.ws + WS_MOD) + col, __float_as_uint(val), RLX_AGENT);
  }
  asm volatile("s_waitcnt vmcnt(0)" ::: "memory");
  if (lane == 0) __hip_atomic_store(sy + SY_MODF + sl, tag, RLX_AGENT);
}
DI void p0_ln1_load(const Ptrs& p, unsigned wl, u32x4 (&xv)[5]) {
  const unsigned lane = lane_id(), l16 = lane & 15u, row = blockIdx.x * 8u + wl * 4u + (lane >> 4);
#pragma unroll
  for (int j = 0; j < 5; ++j) xv[j] = *(const u32x4*)(p.x + (size_t)row * D + (l16 + 16 * j) * 8);
}
DI void p0_gran_load(const Ptrs& p, u32x4 (&g)[10]) {
  const unsigned lane = lane_id();
  rsrc_t srdG = mk_rsrc(p.ws + WS_G, 10240u);
#pragma unroll
  for (int i = 0; i < 10; ++i) g[i] = __builtin_amdgcn_raw_buffer_load_b128(srdG, (lane + 64 * i) * 16, 0, 16);
}
DI void p0_ln1(const Ptrs& p, char* lds, unsigned wl, gu32* sy, unsigned tag, const u32x4 (&xv)[5], u32x4 (&g)[10]) {
  const unsigned lane = lane_id(), l16 = lane & 15u, row = blockIdx.x * 8u + wl * 4u + (lane >> 4);
  float v[5][8];
#pragma unroll
  for (int j = 0; j < 5; ++j)
#pragma unroll
    for (int e = 0; e < 4; ++e) { v[j][2 * e] = bflo(xv[j][e]); v[j][2 * e + 1] = bfhi(xv[j][e]); }
  float r; ln_row(v, r);
  rsrc_t srdG = mk_rsrc(p.ws + WS_G, 10240u);
  float* stg = (float*)(lds + P0_LDS + 512 + wl * 5120);
  for (unsigned spins = 0;; ++spins) {
    bool ok = true;
#pragma unroll
    for (int i = 0; i < 10; ++i) ok &= (g[i][1] == tag) & (g[i][3] == tag);
    if (__all(ok) || spins > (1u << 16)) {
#pragma unroll
      for (int i = 0; i < 10; ++i) { f32x2 vv = {__uint_as_float(g[i][0]), __uint_as_float(g[i][2])}; *(f32x2*)(stg + (lane + 64 * i) * 2) = vv; }
      break;
    }
    if (spins) __builtin_amdgcn_s_sleep(1);
#pragma unroll
    for (int i = 0; i < 10; ++i) g[i] = __builtin_amdgcn_raw_buffer_load_b128(srdG, (lane + 64 * i) * 16, 0, 16);
  }
  rsrc_t srdH = mk_rsrc(p.ws + WS_HF, S * D * 2u);
#pragma unroll
  for (int j = 0; j < 5; ++j) {
    const unsigned ch = l16 + 16 * j, k0 = ch * 8;
    const f32x4 g0 = *(const f32x4*)(stg + D + k0), g1 = *(const f32x4*)(stg + D + k0 + 4), s0 = *(const f32x4*)(stg + k0), s1 = *(const f32x4*)(stg + k0 + 4);
    float o[8];
#pragma unroll
    for (int e = 0; e < 4; ++e) { o[e] = (v[j][e] * r * (1.f + g0[e]) + s0[e]) * QSCALE; o[4 + e] = (v[j][4 + e] * r * (1.f + g1[e]) + s1[e]) * QSCALE; }
    u32x4 wv;
#pragma unroll
    for (int e = 0; e < 4; ++e) wv[e] = cvtpk(o[2 * e], o[2 * e + 1]);
    __builtin_amdgcn_raw_buffer_store_b128(wv, srdH, (((row >> 6) * 40 + (ch >> 1)) * 2 + ((row >> 5) & 1)) * 1024 + ((ch & 1) * 32 + (row & 31)) * 16, 0, 16);
  }
  asm volatile("s_waitcnt vmcnt(0)" ::: "memory");
  if (lane == 0) {
    const unsigned old = __hip_atomic_fetch_add((__attribute__((address_space(3))) unsigned*)(lds + P0_LDS), 1u, __ATOMIC_RELAXED, __HIP_MEMORY_SCOPE_WORKGROUP);
    if (old == 1u) __hip_atomic_store(sy + SY_H + blockIdx.x, tag, RLX_AGENT);
  }
}
struct RelPre { u32x4 ld[10]; };
DI void rel_load(const Ptrs& p, unsigned w, RelPre& r) {
  const unsigned lane = lane_id(), t = blockIdx.x * 3u + (w - 1u);
  const bool iswo = t < 512u;
  const unsigned tt = iswo ? t : (t < 752u ? t - 512u : 0u);
  const unsigned k0 = (iswo ? tt >> 2 : tt / 12u) * 32u, n0 = iswo ? (tt & 3u) * 160u : (tt % 12u) * 144u, cpr = iswo ? 20u : 18u, N = iswo ? D : F;
  const u16* src = iswo ? p.wo : p.w1;
#pragma unroll
  for (int i = 0; i < 10; ++i) {
    const unsigned c = lane + 64u * i, cc = c < 32u * cpr ? c : 0u;
    r.ld[i] = *(const u32x4*)(src + (size_t)(k0 + cc / cpr) * N + n0 + (cc % cpr) * 8u);
  }
}
DI void rel_store(const Ptrs& p, char* lds, unsigned w, const RelPre& r) {
  const unsigned lane = lane_id(), g4 = lane >> 4, q = (lane >> 2) & 3u, pp = lane & 3u;
  char* tl = lds + P0_LDS + (w - 1u) * 10752u;
  const unsigned t = blockIdx.x * 3u + (w - 1u);
  if (t < 512u) {
    const unsigned kb = t >> 2, ct = t & 3u, k0 = kb * 32u, h = g4 >> 1;
    rsrc_t srdWO = mk_rsrc(p.ws + WS_WOF, 4096u * D * 2u);
#pragma unroll
    for (int i = 0; i < 10; ++i) { const unsigned c = lane + 64u * i; *(u32x4*)(tl + (c / 20u) * 336u + (c % 20u) * 16u) = r.ld[i]; }
    asm volatile("s_waitcnt lgkmcnt(0)" ::: "memory");
#pragma unroll
    for (int f = 0; f < 10; ++f) {
      const int s = f / 5, i = f % 5;
      const unsigned cb = i * 64 + (g4 & 1u) * 32u + pp * 8u;
      s16x4 lo = __builtin_amdgcn_ds_read_tr16_b64_v4i16((ldsp_t)(tl + (16 * s + 4 * h + q) * 336u + cb));
      s16x4 up = __builtin_amdgcn_ds_read_tr16_b64_v4i16((ldsp_t)(tl + (16 * s + 8 + 4 * h + q) * 336u + cb));
      u32x2 a = __builtin_bit_cast(u32x2, lo), b = __builtin_bit_cast(u32x2, up);
      u32x4 o = {a[0], a[1], b[0], b[1]};
      const unsigned ks = (k0 >> 4) + s;
      __builtin_amdgcn_raw_buffer_store_b128(o, srdWO, ((((ct * 4u + (ks >> 6)) * 64u + (ks & 63u)) * 5u + i) * 64u + lane) * 16u, 0, 16);
    }
  } else if (t < 752u) {
    const unsigned tt = t - 512u, kb = tt / 12u, cb12 = tt % 12u;
    rsrc_t srdW1 = mk_rsrc(p.ws + WS_W1F, 16u * 143360u);
#pragma unroll
    for (int i = 0; i < 9; ++i) { const unsigned c = lane + 64u * i; *(u32x4*)(tl + (c / 18u) * 304u + (c % 18u) * 16u) = r.ld[i]; }
    asm volatile("s_waitcnt lgkmcnt(0)" ::: "memory");
#pragma unroll
    for (int f = 0; f < 9; ++f) {
      s16x4 lo = __builtin_amdgcn_ds_read_tr16_b64_v4i16((ldsp_t)(tl + (8u * g4 + q) * 304u + f * 32u + pp * 8u));
      s16x4 up = __builtin_amdgcn_ds_read_tr16_b64_v4i16((ldsp_t)(tl + (8u * g4 + 4u + q) * 304u + f * 32u + pp * 8u));
      u32x2 a = __builtin_bit_cast(u32x2, lo), b = __builtin_bit_cast(u32x2, up);
      u32x4 o = {a[0], a[1], b[0], b[1]};
      const unsigned nb = cb12 * 9u + f;
      __builtin_amdgcn_raw_buffer_store_b128(o, srdW1, ((((nb / 7u) * 20u + kb) * 7u + nb % 7u) * 64u + lane) * 16u, 0, 16);
    }
  } else {
    rsrc_t srdW1 = mk_rsrc(p.ws + WS_W1F, 16u * 143360u);
    const u32x4 z = {0, 0, 0, 0};
#pragma unroll
    for (int i = 0; i < 5; ++i) { const unsigned f = (t - 752u) * 5u + i; __builtin_amdgcn_raw_buffer_store_b128(z, srdW1, (((15u * 20u + (f >> 2)) * 7u + 3u + (f & 3u)) * 64u + lane) * 16u, 0, 16); }
  }
  asm volatile("s_waitcnt lgkmcnt(0)" ::: "memory");
}
DI void poll_flags(gu32* flags, unsigned n, unsigned tag, gu32* err) {
  const unsigned l = lane_id();
  for (unsigned spins = 0;; ++spins) {
    const unsigned v = l < n ? __hip_atomic_load(flags + l, RLX_AGENT) : tag;
    if (__all(v == tag)) break;
    if (spins > (1u << 16)) { if (l == 0) __hip_atomic_store(err, 1u, RLX_AGENT); break; }
    __builtin_amdgcn_s_sleep(1);
  }
}
DI rsrc_t wo_slice(const Ptrs& p) {
  return mk_rsrc(p.ws + WS_WOF + (size_t)(((blockIdx.x >> 3) & 3u) * 4u + ((blockIdx.x & 7u) >> 1)) * 327680u, 327680u);
}
DI void phase_attn(const Ptrs& p, unsigned w, gu32* sy, unsigned tag) {
  const unsigned x = blockIdx.x & 7u, j = blockIdx.x >> 3;
  const unsigned hd = 4u * x + (j & 3u), qblk = j >> 2;
  const unsigned blk64 = qblk * 4u + w;
  rsrc_t srdW = mk_rsrc((const char*)p.wq + hd * 256u - 1024, 640u * 8192u - hd * 256u + 1024u);
  rsrc_t srdK = mk_rsrc((const char*)p.kc + ((size_t)hd * SK + (SK - W)) * 256u, W * 256u);
  rsrc_t srdV = mk_rsrc((const char*)p.vc + ((size_t)hd * SK + (SK - W)) * 256u - 1024, W * 256u + 1024u);
  rsrc_t srdH = mk_rsrc(p.ws + WS_HF + (size_t)blk64 * 81920u, 81920u);
  rsrc_t srdO = mk_rsrc(p.ws + WS_OF + ((size_t)(2u * blk64) * 256u + hd * 8u) * 1024u, 262144u + 8192u);
  rsrc_t srdA = wo_slice(p);
  const float lim = 1.1529215e18f;
  asm volatile(K1_ASM
      :
      : [srdW] "s"(srdW), [srdK] "s"(srdK), [srdV] "s"(srdV), [srdH] "s"(srdH), [srdO] "s"(srdO), [srdA] "s"(srdA), [w] "s"(w), [lim] "s"(lim), [tag] "s"(tag), [frel] "s"(sy + SY_REL), [frelo] "s"(sy + SY_REL + blockIdx.x),
        [fo] "s"(sy + SY_OW + (qblk * 4u + w) * 32u + hd), [fmod] "s"(sy + SY_MODF + 80)
      : K1_CLOBBERS);
}

DI void phase_t1(const Ptrs& p, unsigned w, gu32* sy, unsigned tag) {
  const unsigned x = blockIdx.x & 7u, j = blockIdx.x >> 3;
  const unsigned kq = x >> 1, rt = (x & 1u) * 8u + (j >> 2), ct = j & 3u;
  const unsigned tb = rt * 4u + w;
  rsrc_t srdA = wo_slice(p);
  rsrc_t srdB = mk_rsrc(p.ws + WS_OF + ((size_t)tb * 256u + kq * 64u) * 1024u, 65536u);
  rsrc_t srdP = mk_rsrc(p.ws + WS_P + ((size_t)kq * S + tb * 32u) * 1280u + ct * 320u, 31u * 1280u + 320u);
  rsrc_t srdW2s = mk_rsrc(p.w2, D * F * 2u), srdW2d = mk_rsrc(p.ws + WS_W2F, 8u * 56u * 5120u);
  const unsigned gw = blockIdx.x * 4u + w, OOB = 0x7ff00000u;
  unsigned gs[2], gd[2];
#pragma unroll
  for (int i = 0; i < 2; ++i) {
    const unsigned u = gw + 1024u * i; const bool ok = i == 0 || gw < 96u;
    const unsigned nb2 = u / 28u, kp2 = u % 28u;
    gs[i] = ok ? kp2 * 64u * 1280u + nb2 * 32u : OOB;
    gd[i] = ok ? (((nb2 / 5u) * 56u + 2u * kp2) * 5u + nb2 % 5u) * 1024u : OOB;
  }
  asm volatile(T1_ASM
      :
      : [srdA] "s"(srdA), [srdB] "s"(srdB), [srdP] "s"(srdP), [w] "s"(w), [srdW2s] "s"(srdW2s), [srdW2d] "s"(srdW2d),
        [gs0] "s"(gs[0]), [gs1] "s"(gs[1]), [gd0] "s"(gd[0]), [gd1] "s"(gd[1]), [tag] "s"(tag), [fpw] "s"(sy + SY_PW + tb * 16u + ct * 4u + kq)
      : T1_CLOBBERS);
}

struct T2Pre { u32x4 xv[3]; f32x4 ga[3][2], g2[3][2], s2[3][2]; };
DI void t2_prefetch(const Ptrs& p, unsigned w, T2Pre& t) {
  const unsigned lane = lane_id(), l32 = lane & 31u, row = blockIdx.x * 8u + w * 2u + (lane >> 5);
  rsrc_t srdM = mk_rsrc(p.ws + WS_MOD, 6u * D * 4u);
#pragma unroll
  for (int j = 0; j < 3; ++j) {
    const unsigned ch = (j < 2 || l32 < 16u) ? l32 + 32u * j : l32;
    t.xv[j] = *(const u32x4*)(p.x + (size_t)row * D + ch * 8);
#pragma unroll
    for (int h = 0; h < 2; ++h) {
      t.ga[j][h] = __builtin_bit_cast(f32x4, __builtin_amdgcn_raw_buffer_load_b128(srdM, (2 * D + ch * 8 + 4 * h) * 4, 0, 16));
      t.g2[j][h] = __builtin_bit_cast(f32x4, __builtin_amdgcn_raw_buffer_load_b128(srdM, (4 * D + ch * 8 + 4 * h) * 4, 0, 16));
      t.s2[j][h] = __builtin_bit_cast(f32x4, __builtin_amdgcn_raw_buffer_load_b128(srdM, (3 * D + ch * 8 + 4 * h) * 4, 0, 16));
    }
  }
}
DI void phase_t2(const Ptrs& p, unsigned w, const T2Pre& t) {
  const unsigned lane = lane_id(), l32 = lane & 31u, row = blockIdx.x * 8u + w * 2u + (lane >> 5);
  rsrc_t srdP = mk_rsrc(p.ws + WS_P, 4u * S * D * 2u);
  rsrc_t srdX1 = mk_rsrc(p.ws + WS_X1, S * D * 2u);
  rsrc_t srdH2 = mk_rsrc(p.ws + WS_H2F, S * D * 2u);
  const bool v2 = l32 < 16u;
  u32x4 pv[4][3];
#pragma unroll
  for (int j = 0; j < 3; ++j)
#pragma unroll
    for (int q = 0; q < 4; ++q) pv[q][j] = __builtin_amdgcn_raw_buffer_load_b128(srdP, (j < 2 || v2) ? ((q * S + row) * D + (l32 + 32u * j) * 8) * 2 : 0x7fff0000u, 0, 16);
  float v[3][8]; float s = 0; u32x4 xo[3];
#pragma unroll
  for (int j = 0; j < 3; ++j) {
#pragma unroll
    for (int e = 0; e < 4; ++e) {
      float a0 = 0, a1 = 0;
#pragma unroll
      for (int q = 0; q < 4; q += 2) { a0 += bflo(pv[q][j][e]) + bflo(pv[q + 1][j][e]); a1 += bfhi(pv[q][j][e]) + bfhi(pv[q + 1][j][e]); }
      xo[j][e] = cvtpk(bflo(t.xv[j][e]) + t.ga[j][e >> 1][(2 * e) & 3] * a0, bfhi(t.xv[j][e]) + t.ga[j][e >> 1][(2 * e + 1) & 3] * a1);
      v[j][2 * e] = bflo(xo[j][e]); v[j][2 * e + 1] = bfhi(xo[j][e]);
      if (j < 2 || v2) s += v[j][2 * e] + v[j][2 * e + 1];
    }
  }
  s = swap16_add(sum16(s));
  const float m = s * (1.f / D); float q = 0;
#pragma unroll
  for (int j = 0; j < 3; ++j)
#pragma unroll
    for (int e = 0; e < 8; ++e) { v[j][e] -= m; if (j < 2 || v2) q += v[j][e] * v[j][e]; }
  q = swap16_add(sum16(q));
  const float r = rsqrtf(q * (1.f / D) + 1e-5f);
#pragma unroll
  for (int j = 0; j < 3; ++j) {
    const unsigned ch = l32 + 32u * j;
    u32x4 wv;
#pragma unroll
    for (int e = 0; e < 4; ++e)
      wv[e] = cvtpk(v[j][2 * e] * r * (1.f + t.g2[j][e >> 1][(2 * e) & 3]) + t.s2[j][e >> 1][(2 * e) & 3],
                    v[j][2 * e + 1] * r * (1.f + t.g2[j][e >> 1][(2 * e + 1) & 3]) + t.s2[j][e >> 1][(2 * e + 1) & 3]);
    __builtin_amdgcn_raw_buffer_store_b128(wv, srdH2, (j < 2 || v2) ? (((row >> 4) * 20 + (ch >> 2)) * 64 + (row & 15) + 16 * (ch & 3)) * 16 : 0x7fff0000u, 0, 16);
  }
#pragma unroll
  for (int j = 0; j < 3; ++j) __builtin_amdgcn_raw_buffer_store_b128(xo[j], srdX1, (j < 2 || v2) ? (row * D + (l32 + 32u * j) * 8) * 2 : 0x7fff0000u, 0, 16);
}
DI void pre_t3_part(const Ptrs& p, unsigned w, int part) {
  const unsigned j = blockIdx.x >> 3, vl16 = lane_id() * 16u;
  rsrc_t srdW1 = mk_rsrc(p.ws + WS_W1F + (size_t)(8u * (blockIdx.x & 1u) + (j & 7u)) * 143360u, 143360u);
  if (part == 0) {
    if (w == 0) return;
#pragma unroll 1
    for (unsigned q = w - 1u; q < 72u; q += 3u) dma16c(q * 1024u, vl16, srdW1, q * 1024u);
  } else {
#pragma unroll 1
    for (unsigned q = 72u + w; q < 140u; q += 4u) dma16c(q * 1024u, vl16, srdW1, q * 1024u);
  }
}
DI void phase_t34(const Ptrs& p, char* lds, unsigned w, gu32* sy, unsigned tag) {
  const unsigned x = blockIdx.x & 7u, j = blockIdx.x >> 3;
  const unsigned rt = 8u * (x >> 1) + (j >> 2), ct = 4u * (x & 1u) + (j & 3u);
  {
    const unsigned rt3 = 4u * (x >> 1) + (j >> 3), ct3 = 8u * (x & 1u) + (j & 7u), mb0 = rt3 * 8u + w * 2u;
    const char* a1f = p.ws + WS_A1F;
    rsrc_t srdH2 = mk_rsrc(p.ws + WS_H2F + (size_t)mb0 * 20480u, 40960u);
    rsrc_t srdB1 = mk_rsrc(p.b1, F * 2u);
    rsrc_t srdA1o = mk_rsrc(a1f + ((size_t)mb0 * 56u + ((7u * ct3) >> 1)) * 1024u, 2u * 56u * 1024u - ((7u * ct3) >> 1) * 1024u);
    rsrc_t srdW2 = mk_rsrc(p.ws + WS_W2F + ((size_t)ct * 56u + 14u * w) * 5120u, 14u * 5120u);
    rsrc_t srdA0 = mk_rsrc(a1f + ((size_t)(rt * 4u + 0u) * 56u + 14u * w) * 1024u, 14u * 1024u);
    rsrc_t srdA1 = mk_rsrc(a1f + ((size_t)(rt * 4u + 1u) * 56u + 14u * w) * 1024u, 14u * 1024u);
    rsrc_t srdA2 = mk_rsrc(a1f + ((size_t)(rt * 4u + 2u) * 56u + 14u * w) * 1024u, 14u * 1024u);
    rsrc_t srdA3 = mk_rsrc(a1f + ((size_t)(rt * 4u + 3u) * 56u + 14u * w) * 1024u, 14u * 1024u);
    gu32* fout = sy + SY_A1W + (rt3 * 4u + w) * 16u + ct3;
    gu32* fin = sy + SY_A1W + ((rt >> 1) * 4u + (rt & 1u) * 2u) * 16u;
    const unsigned par = ct3 & 1u, cbias = 224u * ct3;
    asm volatile(T34_ASM
        :
        : [srdH2] "s"(srdH2), [srdB1] "s"(srdB1), [srdA1o] "s"(srdA1o), [srdW2] "s"(srdW2), [srdA0] "s"(srdA0), [srdA1] "s"(srdA1), [srdA2] "s"(srdA2), [srdA3] "s"(srdA3),
          [w] "s"(w), [tag] "s"(tag), [par] "s"(par), [cbias] "s"(cbias), [fout] "s"(fout), [fin] "s"(fin), [fx1] "s"(sy + SY_P)
        : T34_CLOBBERS);
  }
  const unsigned t = w * 64u + lane_id();
  const f32x4* red = (const f32x4*)lds;
  rsrc_t srdM = mk_rsrc(p.ws + WS_MOD, 6u * D * 4u);
  rsrc_t srdX1 = mk_rsrc(p.ws + WS_X1, S * D * 2u);
  u32x4 xv[3], bv[3]; f32x4 g0[3], g1[3];
#pragma unroll
  for (int i = 0; i < 3; ++i) {
    const unsigned c = (i < 2 || t < 128u) ? t + 256u * i : t;
    const unsigned tok = c / 10, c8 = c % 10, row = rt * 64 + tok, n0 = ct * 80 + c8 * 8;
    xv[i] = __builtin_amdgcn_raw_buffer_load_b128(srdX1, (row * D + n0) * 2, 0, 16);
    bv[i] = *(const u32x4*)(p.b2 + n0);
    g0[i] = __builtin_bit_cast(f32x4, __builtin_amdgcn_raw_buffer_load_b128(srdM, (5 * D + n0) * 4, 0, 16)); g1[i] = __builtin_bit_cast(f32x4, __builtin_amdgcn_raw_buffer_load_b128(srdM, (5 * D + n0 + 4) * 4, 0, 16));
  }
  raw_barrier();
#pragma unroll
  for (int i = 0; i < 3; ++i) {
    if (i == 2 && t >= 128u) break;
    const unsigned c = t + 256u * i;
    const unsigned tok = c / 10, c8 = c % 10, m = tok >> 4, nb = c8 >> 1, g = 2 * (c8 & 1);
    f32x4 s0 = {0, 0, 0, 0}, s1 = {0, 0, 0, 0};
#pragma unroll
    for (int ww = 0; ww < 4; ++ww) {
      s0 += red[(ww * 40 + m * 5 + nb) * 64 + (tok & 15) + 16 * g];
      s1 += red[(ww * 40 + m * 5 + nb) * 64 + (tok & 15) + 16 * (g + 1)];
    }
    const unsigned row = rt * 64 + tok, n0 = ct * 80 + c8 * 8;
    float o[8];
#pragma unroll
    for (int e = 0; e < 4; ++e) {
      const float a0 = e < 2 ? s0[2 * e] : s1[2 * e - 4], a1 = e < 2 ? s0[2 * e + 1] : s1[2 * e - 3];
      const float ga0 = e < 2 ? g0[i][2 * e] : g1[i][2 * e - 4], ga1 = e < 2 ? g0[i][2 * e + 1] : g1[i][2 * e - 3];
      o[2 * e] = bflo(xv[i][e]) + ga0 * (a0 + bflo(bv[i][e])); o[2 * e + 1] = bfhi(xv[i][e]) + ga1 * (a1 + bfhi(bv[i][e]));
    }
    u32x4 wv;
#pragma unroll
    for (int e = 0; e < 4; ++e) wv[e] = cvtpk(o[2 * e], o[2 * e + 1]);
    *(u32x4*)(p.out + (size_t)row * D + n0) = wv;
  }
}

__global__ __launch_bounds__(256, 1) void mega(const u16* __restrict__ x, const u16* __restrict__ kc, const u16* __restrict__ vc, const u16* __restrict__ wq,
                                               const u16* __restrict__ b1, const u16* __restrict__ b2, const u16* __restrict__ c, const u16* __restrict__ wmod,
                                               const u16* __restrict__ wo, const u16* __restrict__ w1, const u16* __restrict__ w2, char* __restrict__ ws, u16* __restrict__ out) {
  extern __shared__ __attribute__((aligned(16))) char lds[];
  Ptrs p = {x, kc, vc, wq, b1, b2, c, wmod, wo, w1, w2, ws, out};
  const unsigned w = __builtin_amdgcn_readfirstlane(threadIdx.x >> 6);
  gu32* sy = (gu32*)(ws + WS_SYNC);
  gu32* err = sy + SY_ERR;
  const unsigned b = blockIdx.x, xg = b & 7u, j = b >> 3;
  const unsigned epoch = __hip_atomic_load(sy + SY_EPOCH, RLX_AGENT);
  u32x4 xv[5];
  ModPre mp;
  p0_mod_load(p, w, mp);
  u32x4 gr[10];
  if (w == 1 || w == 2) { p0_ln1_load(p, w - 1u, xv); }
  if (w == 3) code_touch();
  if (threadIdx.x == 0) { *(unsigned*)(lds + P0_LDS) = 0u; *(unsigned*)(lds + P0_LDS + 4) = 0u; }
  p0_mod_partial(mp, lds, w);
  raw_barrier();
  const unsigned tag = __builtin_amdgcn_readfirstlane(epoch) + 1u;
  if (w == 0) {
    p0_mod_publish(p, lds, sy, tag);
    raw_barrier();
  } else {
    RelPre rp;
    if (w == 3) {
      p0_prefetch(p); rel_load(p, w, rp);
      input_touch(p);
      asm volatile("s_waitcnt vmcnt(63)" ::: "memory");
    } else { p0_gran_load(p, gr); rel_load(p, w, rp); p0_ln1(p, lds, w - 1u, sy, tag, xv, gr); }
    raw_barrier();
    rel_store(p, lds, w, rp);
  }
  poll_flags(sy + SY_H + ((j >> 2) * 4u + w) * 8u, 8, tag, err);
  phase_attn(p, w, sy, tag);
  { const unsigned kq = xg >> 1, rt = (xg & 1u) * 8u + (j >> 2), tb = rt * 4u + w;
    poll_flags(sy + SY_OW + ((rt >> 1) * 4u + ((tb >> 1) & 3u)) * 32u + 8u * kq, 8, tag, err); }
  phase_t1(p, w, sy, tag);
  { const unsigned kq = xg >> 1, rt = (xg & 1u) * 8u + (j >> 2), ct = j & 3u;
    publish_nodrain(sy + SY_P + rt * 16u + ct * 4u + kq, tag, w); }
  {
    T2Pre t2p; t2_prefetch(p, w, t2p);
    pre_t3_part(p, w, 0);
    poll_flags(sy + SY_PW + (b >> 2) * 16u, 16, tag, err);
    phase_t2(p, w, t2p);
  }
  pre_t3_part(p, w, 1);
  asm volatile("s_waitcnt vmcnt(17)" ::: "memory");
  if (lane_id() == 0) __hip_atomic_store(sy + SY_H2W + b * 4u + w, tag, RLX_AGENT);
  asm volatile("s_waitcnt vmcnt(0)" ::: "memory");
  raw_barrier();
  poll_flags(sy + SY_H2W + ((4u * (xg >> 1) + (j >> 3)) * 16u + w * 4u) * 4u, 16, tag, err);
  phase_t34(p, lds, w, sy, tag);
  if (b == 0 && threadIdx.x == 0) __hip_atomic_store(sy + SY_EPOCH, tag, RLX_AGENT);
  asm volatile(".Lmega_code_end:");
}

extern "C" void kernel_launch(void* const* d_in, const int* in_sizes, int n_in, void* d_out, int out_size, void* d_ws, size_t ws_size, hipStream_t stream) {
  (void)hipFuncSetAttribute((const void*)mega, hipFuncAttributeMaxDynamicSharedMemorySize, 160 * 1024);
  mega<<<256, 256, 163840, stream>>>((const u16*)d_in[0], (const u16*)d_in[1], (const u16*)d_in[2], (const u16*)d_in[3], (const u16*)d_in[7], (const u16*)d_in[9],
                                     (const u16*)d_in[10], (const u16*)d_in[11], (const u16*)d_in[4], (const u16*)d_in[6], (const u16*)d_in[8], (char*)d_ws, (u16*)d_out);
}
```
